```python
import torch

def _softmax(x, axis=-1):
    return torch.softmax(x, dim=axis)

def _gelu(x):
    return torch.nn.functional.gelu(x, approximate='tanh')

def _silu(x):
    return torch.nn.functional.silu(x)

def _sigmoid(x):
    return torch.sigmoid(x)

def _layernorm(x, axis=-1, eps=1e-05):
    m = x.mean(dim=axis, keepdim=True)
    v = x.var(dim=axis, keepdim=True, unbiased=False)
    return (x - m) / torch.sqrt(v + eps)

def _scatter_add(tgt, idx, src, axis=0):
    return tgt.clone().index_add_(axis, idx, src)

def _segment_sum(src, seg, n):
    out = torch.zeros((n,) + src.shape[1:], dtype=src.dtype)
    return out.index_add_(0, seg, src)

def _pad(x, pw):
    flat = tuple((v for pair in reversed(pw) for v in pair))
    return torch.nn.functional.pad(x, flat)
kernel_name = 'adaLN_cond__gqa_block_cached__moe_arch_mod__prompt'

def setup_inputs():

    _w = lambda a: (lambda t: t.to(torch.bfloat16) if t.is_floating_point() else t)(torch.as_tensor(a))

    def _A_setup():
        B, Sq, D = (1, 2048, 448)
        Hq, Hkv, Sk, Dh = (16, 8, 2048, 256)
        assert Hq % Hkv == 0, (Hq, Hkv)
        return {'x': _w((lambda *s: torch.randn(*s, dtype=torch.bfloat16) * 0.05)(B, Sq, D)), 'k_cache': _w((lambda *s: torch.randn(*s, dtype=torch.bfloat16) * 3.2)(B, Hkv, Sk, Dh)), 'v_cache': _w((lambda *s: torch.randn(*s, dtype=torch.bfloat16) * 0.4)(B, Hkv, Sk, Dh)), 'wq': _w((lambda *s: torch.randn(*s, dtype=torch.bfloat16) * 3.2)(D, Hq * Dh) * D ** (-0.5)), 'wo': _w((lambda *s: torch.randn(*s, dtype=torch.bfloat16) * 0.4)(Hq * Dh, D) * (Hq * Dh) ** (-0.5))}
    _A_in = _A_setup()

    def _B_setup():
        B, S, D, F, C = (1, 2048, 448, 1792, 1)
        return {'x': _w((lambda *s: torch.randn(*s, dtype=torch.bfloat16) * 0.05)(B, S, D)), 'w_r': _w((lambda *s: torch.randn(*s, dtype=torch.bfloat16) * 3.2)(D)), 'w_gate': _w((lambda *s: torch.randn(*s, dtype=torch.bfloat16) * 0.1)(D, F)), 'w_up': _w((lambda *s: torch.randn(*s, dtype=torch.bfloat16) * 0.1)(D, F)), 'w_down': _w((lambda *s: torch.randn(*s, dtype=torch.bfloat16) * 0.1)(F, D))}
    _B_in = _B_setup()
    return {'A_x': _A_in['x'], 'A_k_cache': _A_in['k_cache'], 'A_v_cache': _A_in['v_cache'], 'A_wq': _A_in['wq'], 'A_wo': _A_in['wo'], 'B_x': _B_in['x'], 'B_w_r': _B_in['w_r'], 'B_w_gate': _B_in['w_gate'], 'B_w_up': _B_in['w_up'], 'B_w_down': _B_in['w_down'], 'c': _w((lambda *s: torch.randn(*s, dtype=torch.bfloat16) * 0.1)(1, 448) * 8.0), 'w_mod': _w((lambda *s: torch.randn(*s, dtype=torch.bfloat16) * 0.1)(448, 6 * 448) * 448 ** (-0.5) * 8.0)}

def reference(A_x, A_k_cache, A_v_cache, A_wq, A_wo, B_x, B_w_r, B_w_gate, B_w_up, B_w_down, c, w_mod):

    def _A_ref(x, k_cache, v_cache, wq, wo):
        B, Sq, D = x.shape
        Hq, Dh = (16, 256)
        Hkv = k_cache.shape[1]
        rep = Hq // Hkv
        q = (lambda x, *d: x.permute(*d))((x @ wq).reshape(B, Sq, Hq, Dh), 0, 2, 1, 3)
        k = (lambda x, n, axis: torch.repeat_interleave(x, n, dim=axis))(k_cache, rep, axis=1)
        v = (lambda x, n, axis: torch.repeat_interleave(x, n, dim=axis))(v_cache, rep, axis=1)
        scores = torch.einsum('bhqd,bhkd->bhqk', q, k) * Dh ** (-0.5)
        p = _softmax(scores, axis=-1)
        o = torch.einsum('bhqk,bhkd->bhqd', p, v)
        return (lambda x, *d: x.permute(*d))(o, 0, 2, 1, 3).reshape(B, Sq, Hq * Dh) @ wo

    def _B_ref(x, w_r, w_gate, w_up, w_down):
        B, S, D = x.shape
        C = 1
        r = x @ w_r
        idx = torch.argsort(-r, axis=-1)[:, :C]
        idx_e = (lambda x, n, axis: torch.repeat_interleave(x, n, dim=axis))(idx.reshape(B, C, 1), D, axis=2)
        x_sel = (lambda x, i, axis: torch.take_along_dim(x, i, dim=axis))(x, idx_e, axis=1)
        g = _silu(x_sel @ w_gate)
        u = x_sel @ w_up
        y = g * u @ w_down
        r_sel = _sigmoid((lambda x, i, axis: torch.take_along_dim(x, i, dim=axis))(r, idx, axis=1))
        y = y * r_sel.reshape(B, C, 1)
        b_off = (torch.arange(B) * S).reshape(B, 1)
        flat_idx = (idx + b_off).reshape(B * C)
        zero = (x * 0.0).reshape(B * S, D)
        upd = _scatter_add(zero, flat_idx, y.reshape(B * C, D), axis=0)
        return x + upd.reshape(B, S, D)
    x = A_x
    mod = (_silu(c) @ w_mod).reshape(c.shape[0], 6, 1, -1)
    sa, ca, ga, sb, cb, gb = (mod[:, 0].clone(), mod[:, 1].clone(), mod[:, 2].clone(), mod[:, 3].clone(), mod[:, 4].clone(), mod[:, 5].clone())
    h = _layernorm(x, axis=-1) * (1 + ca) + sa
    x = x + ga * _A_ref(h, A_k_cache, A_v_cache, A_wq, A_wo)
    h = _layernorm(x, axis=-1) * (1 + cb) + sb
    x = x + gb * _B_ref(h, B_w_r, B_w_gate, B_w_up, B_w_down)
    return x
```

```cpp
#include <hip/hip_runtime.h>
#include <hip/hip_bf16.h>
#include <stdint.h>
typedef unsigned short u16;
#define MIMIC 1
__device__ __forceinline__ float bf2f(u16 v) { return __uint_as_float(((unsigned)v) << 16); }
__device__ __forceinline__ u16 f2bf(float f) { unsigned u = __float_as_uint(f); u += 0x7FFF + ((u >> 16) & 1); return (u16)(u >> 16); }
__device__ __forceinline__ float rbf(float f) { return bf2f(f2bf(f)); }

constexpr int S = 2048, D = 448, HQ = 16, HKV = 8, DH = 256, F = 1792;

__global__ void mod_kernel(const u16* c, const u16* wmod, float* mod) {
  int j = blockIdx.x * 64 + threadIdx.x;
  float acc = 0;
  for (int i = 0; i < D; ++i) { float cv = bf2f(c[i]); float s = rbf(cv / (1.f + __expf(-cv))); acc += s * bf2f(wmod[i * 6 * D + j]); }
  mod[j] = rbf(acc);
}
__device__ float wsum(float v) { for (int o = 32; o; o >>= 1) v += __shfl_xor(v, o); return v; }
__global__ void ln_kernel(const u16* x, const float* sh, const float* sc, u16* h) {
  int row = blockIdx.x, l = threadIdx.x;
  float v[7]; float s = 0;
  for (int i = 0; i < 7; ++i) { v[i] = bf2f(x[row * D + l + 64 * i]); s += v[i]; }
  float mean = wsum(s) / D; float q = 0;
  for (int i = 0; i < 7; ++i) { float d = v[i] - mean; q += d * d; }
  float var = rbf(wsum(q) / D); float m = rbf(mean);
  float den = rbf(sqrtf(rbf(var + 1e-5f)));
  for (int i = 0; i < 7; ++i) { int cidx = l + 64 * i; float t = rbf(rbf(v[i] - m) / den); t = rbf(t * rbf(1.f + sc[cidx])); t = rbf(t + sh[cidx]); h[row * D + cidx] = f2bf(t); }
}
__global__ void gemm_kernel(const u16* A, const u16* B, u16* C, int M, int N, int K) {
  int n = blockIdx.x * 64 + threadIdx.x, m = blockIdx.y;
  float acc = 0;
  for (int k = 0; k < K; ++k) acc += bf2f(A[m * K + k]) * bf2f(B[k * N + n]);
  C[m * N + n] = f2bf(acc);
}
__global__ __launch_bounds__(256) void attn_kernel(const u16* q, const u16* kc, const u16* vc, u16* o) {
  __shared__ float qs[DH]; __shared__ float ps[S]; __shared__ float red[8];
  int row = blockIdx.x, hh = blockIdx.y, kv = hh >> 1, t = threadIdx.x;
  qs[t] = bf2f(q[row * (HQ * DH) + hh * DH + t]); __syncthreads();
  float mx = -1e30f;
  for (int i = 0; i < 8; ++i) { int key = t + 256 * i; const u16* kr = kc + ((size_t)kv * S + key) * DH; float a = 0;
    for (int d = 0; d < DH; d += 8) { uint4 w = *(const uint4*)(kr + d); unsigned ww[4] = {w.x, w.y, w.z, w.w};
      for (int e = 0; e < 4; ++e) { a += qs[d + 2 * e] * __uint_as_float(ww[e] << 16); a += qs[d + 2 * e + 1] * __uint_as_float(ww[e] & 0xFFFF0000u); } }
    a = rbf(a);
    a *= 0.0625f; ps[key] = a; mx = fmaxf(mx, a); }
  for (int of = 32; of; of >>= 1) mx = fmaxf(mx, __shfl_xor(mx, of));
  if ((t & 63) == 0) red[t >> 6] = mx; __syncthreads();
  mx = fmaxf(fmaxf(red[0], red[1]), fmaxf(red[2], red[3]));
  float s = 0;
  for (int i = 0; i < 8; ++i) { int key = t + 256 * i; float e = __expf(ps[key] - mx); ps[key] = e; s += e; }
  s = wsum(s); if ((t & 63) == 0) red[4 + (t >> 6)] = s; __syncthreads();
  s = red[4] + red[5] + red[6] + red[7];
  float inv = 1.f / s; float acc = 0;
  for (int key = 0; key < S; ++key) acc += rbf(ps[key] * inv) * bf2f(vc[((size_t)kv * S + key) * DH + t]);
  o[row * (HQ * DH) + hh * DH + t] = f2bf(acc);
}
__global__ void resid_kernel(const u16* x, const u16* a, const float* g, u16* x1) {
  int i = blockIdx.x * 256 + threadIdx.x; int cidx = i % D;
  x1[i] = f2bf(bf2f(x[i]) + rbf(g[cidx] * bf2f(a[i])));
}
__global__ void router_kernel(const u16* h2, const u16* wr, float* r) {
  int row = blockIdx.x, l = threadIdx.x; float s = 0;
  for (int i = 0; i < 7; ++i) s += bf2f(h2[row * D + l + 64 * i]) * bf2f(wr[l + 64 * i]);
  s = wsum(s); if (l == 0) r[row] = rbf(s);
}
__global__ __launch_bounds__(256) void mlp_kernel(const u16* h2, const float* r, const u16* wg, const u16* wu, const u16* wd, float* y, int* idxo) {
  __shared__ float hid[F]; __shared__ int sidx; __shared__ float xs[D];
  int t = threadIdx.x;
  if (t == 0) { int b = 0; for (int i = 1; i < S; ++i) if (r[i] > r[b]) b = i; sidx = b; *idxo = b; }
  __syncthreads(); int idx = sidx;
  for (int i = t; i < D; i += 256) xs[i] = bf2f(h2[idx * D + i]); __syncthreads();
  for (int f = t; f < F; f += 256) { float g = 0, u = 0; for (int i = 0; i < D; ++i) { g += xs[i] * bf2f(wg[i * F + f]); u += xs[i] * bf2f(wu[i * F + f]); }
    g = rbf(g); u = rbf(u); g = rbf(g / (1.f + __expf(-g))); hid[f] = rbf(g * u); }
  __syncthreads();
  float rs = rbf(1.f / (1.f + __expf(-r[idx])));
  for (int j = t; j < D; j += 256) { float a = 0; for (int f = 0; f < F; ++f) a += hid[f] * bf2f(wd[f * D + j]); y[j] = rbf(rbf(a) * rs); }
}
__global__ void final_kernel(const u16* x1, const u16* h2, const float* gb, const float* y, const int* idx, u16* out) {
  int i = blockIdx.x * 256 + threadIdx.x; int cidx = i % D, row = i / D;
  float b = bf2f(h2[i]); if (row == *idx) b = rbf(b + y[cidx]);
  out[i] = f2bf(bf2f(x1[i]) + rbf(gb[cidx] * b));
}
extern "C" void kernel_launch(void* const* d_in, const int* in_sizes, int n_in, void* d_out, int out_size, void* d_ws, size_t ws_size, hipStream_t stream) {
  const u16* x = (const u16*)d_in[0]; const u16* kc = (const u16*)d_in[1]; const u16* vc = (const u16*)d_in[2];
  const u16* wq = (const u16*)d_in[3]; const u16* wo = (const u16*)d_in[4];
  const u16* wr = (const u16*)d_in[6]; const u16* wg = (const u16*)d_in[7]; const u16* wu = (const u16*)d_in[8]; const u16* wd = (const u16*)d_in[9];
  const u16* c = (const u16*)d_in[10]; const u16* wmod = (const u16*)d_in[11];
  char* ws = (char*)d_ws;
  float* mod = (float*)ws; float* r = (float*)(ws + 16384); float* y = (float*)(ws + 32768); int* idx = (int*)(ws + 40960);
  u16* h = (u16*)(ws + (1 << 20)); u16* q = (u16*)(ws + (4 << 20)); u16* o = (u16*)(ws + (24 << 20)); u16* a = (u16*)(ws + (44 << 20));
  u16* x1 = (u16*)(ws + (48 << 20)); u16* h2 = (u16*)(ws + (52 << 20));
  mod_kernel<<<42, 64, 0, stream>>>(c, wmod, mod);
  ln_kernel<<<S, 64, 0, stream>>>(x, mod, mod + D, h);
  gemm_kernel<<<dim3(64, S), 64, 0, stream>>>(h, wq, q, S, 4096, D);
  attn_kernel<<<dim3(S, HQ), 256, 0, stream>>>(q, kc, vc, o);
  gemm_kernel<<<dim3(7, S), 64, 0, stream>>>(o, wo, a, S, D, 4096);
  resid_kernel<<<S * D / 256, 256, 0, stream>>>(x, a, mod + 2 * D, x1);
  ln_kernel<<<S, 64, 0, stream>>>(x1, mod + 3 * D, mod + 4 * D, h2);
  router_kernel<<<S, 64, 0, stream>>>(h2, wr, r);
  mlp_kernel<<<1, 256, 0, stream>>>(h2, r, wg, wu, wd, y, idx);
  final_kernel<<<S * D / 256, 256, 0, stream>>>(x1, h2, mod + 5 * D, y, idx, (u16*)d_out);
}
```

```cpp
#include <hip/hip_runtime.h>
#include <stdint.h>
typedef unsigned short u16;
typedef unsigned int u32;
typedef __attribute__((ext_vector_type(4))) unsigned int u32x4;
typedef __attribute__((ext_vector_type(2))) unsigned int u32x2;
typedef __attribute__((ext_vector_type(2))) float f32x2;
typedef __attribute__((ext_vector_type(2))) __bf16 bf16x2;
#define DI __device__ __forceinline__
DI float bflo(u32 w) { return __uint_as_float(w << 16); }
DI float bfhi(u32 w) { return __uint_as_float(w & 0xffff0000u); }
DI u32 pk2(float a, float b) { f32x2 v = {a, b}; bf16x2 r = __builtin_convertvector(v, bf16x2); return __builtin_bit_cast(u32, r); }
DI float rbf(float f) { return __uint_as_float(pk2(0.f, f)); }
template <int CTRL> DI float dpp_add(float v) { return v + __int_as_float(__builtin_amdgcn_update_dpp(0, __float_as_int(v), CTRL, 0xf, 0xf, false)); }
template <int CTRL> DI u32 dpp_max(u32 v) { u32 o = (u32)__builtin_amdgcn_update_dpp(0, (int)v, CTRL, 0xf, 0xf, false); return v > o ? v : o; }
DI float row_sum(float v) { v = dpp_add<0x128>(v); v = dpp_add<0x124>(v); v = dpp_add<0x122>(v); return dpp_add<0x121>(v); }
DI float wsum(float v) {
  v = row_sum(v);
  auto a = __builtin_amdgcn_permlane16_swap(__float_as_uint(v), __float_as_uint(v), false, false);
  v = __uint_as_float(a[0]) + __uint_as_float(a[1]);
  auto b = __builtin_amdgcn_permlane32_swap(__float_as_uint(v), __float_as_uint(v), false, false);
  return __uint_as_float(b[0]) + __uint_as_float(b[1]);
}
DI u32 wmax(u32 v) {
  v = dpp_max<0x128>(v); v = dpp_max<0x124>(v); v = dpp_max<0x122>(v); v = dpp_max<0x121>(v);
  auto a = __builtin_amdgcn_permlane16_swap(v, v, false, false); v = a[0] > a[1] ? a[0] : a[1];
  auto b = __builtin_amdgcn_permlane32_swap(v, v, false, false); return b[0] > b[1] ? b[0] : b[1];
}
constexpr int D_ = 448;
constexpr u32 WS_MODG = 4096, WS_FLAG = 16384, WS_KEY = 32768, WS_ROWG = 65536, WS_SMALL = 2 << 20, WS_YP = 1 << 20, WS_PART = 8 << 20;
constexpr u32 PBS = 2176, PGS = 1024 * PBS + 640;
constexpr u32 SPIN = 1u << 18;
#define RSRC(p, n) __builtin_amdgcn_make_buffer_rsrc((void*)(p), (short)0, (int)(n), 0x00020000)
#define LD16(r, off) __builtin_amdgcn_raw_buffer_load_b128(r, off, 0, 16)
#define LD8(r, off) __builtin_amdgcn_raw_buffer_load_b64(r, off, 0, 16)
#define LD4(r, off) __builtin_amdgcn_raw_buffer_load_b32(r, off, 0, 16)
#define ST16(v, r, off) __builtin_amdgcn_raw_buffer_store_b128(v, r, off, 0, 16)
#define ST8(v, r, off) __builtin_amdgcn_raw_buffer_store_b64(v, r, off, 0, 16)
#define ST4(v, r, off) __builtin_amdgcn_raw_buffer_store_b32(v, r, off, 0, 16)
#define LDP(r, off) __builtin_amdgcn_raw_buffer_load_b128(r, off, 0, 0)
#define STP(v, r, off) __builtin_amdgcn_raw_buffer_store_b128(v, r, off, 0, 16)
#define MEMBAR() asm volatile("" ::: "memory")
#define LDSBAR() do { asm volatile("s_waitcnt lgkmcnt(0)\n\ts_barrier" ::: "memory"); } while (0)

DI f32x2 up2(u32 w) { f32x2 r = {bflo(w), bfhi(w)}; return r; }

__global__ __launch_bounds__(256, 1) void fused_block(const u16* __restrict__ x, const u16* __restrict__ kc, const u16* __restrict__ vc, const u16* __restrict__ wq,
    const u16* __restrict__ wo, const u16* __restrict__ wr, const u16* __restrict__ wg, const u16* __restrict__ wu, const u16* __restrict__ wd,
    const u16* __restrict__ c, const u16* __restrict__ wmod, u16* __restrict__ out, char* __restrict__ ws) {
  __shared__ __attribute__((aligned(16))) char lds[163840];
  unsigned w = __builtin_amdgcn_readfirstlane(threadIdx.x >> 6);
  unsigned tag, f;
  u32x4 wv[7];
  const char* pf = ws;
  asm volatile(
    "fb_Lstart:\n"
    "v_mbcnt_lo_u32_b32 v250, -1, 0\n"
    "v_mbcnt_hi_u32_b32 v250, -1, v250\n"
    "s_and_b32 s66, %[blk], 7\n"
    "s_lshr_b32 s67, %[blk], 3\n"
    "s_mov_b64 s[72:73], %[wmod]\n"
    "s_and_b32 s73, s73, 0xffff\n"
    "s_mov_b32 s74, 2408448\n"
    "s_mov_b32 s75, 0x20000\n"
    "s_mov_b64 s[76:77], %[c]\n"
    "s_and_b32 s77, s77, 0xffff\n"
    "s_mov_b32 s78, 896\n"
    "s_mov_b32 s79, 0x20000\n"
    "s_mov_b64 s[52:53], %[ws]\n"
    "s_and_b32 s53, s53, 0xffff\n"
    "s_mov_b32 s54, 1048576\n"
    "s_mov_b32 s55, 0x20000\n"
    "s_and_b32 s68, s67, 0x18\n"
    "s_add_u32 s68, s68, s66\n"
    "s_lshl_b32 s68, s68, 3\n"
    "s_and_b32 s61, s67, 7\n"
    "s_add_u32 s86, s68, s61\n"
    "s_and_b32 s87, s86, 127\n"
    "s_lshl_b32 s61, %[wv], 10\n"
    "s_add_u32 s87, s87, s61\n"
    "v_mul_u32_u24 v10, 5376, v250\n"
    "v_lshlrev_b32 v11, 1, v250\n"
    "buffer_load_dword v229, off, s[52:55], 0 sc1\n"
    "s_lshl_b32 s61, %[blk], 2\n"
    "s_add_u32 s61, s61, %[wv]\n"
    "s_and_b32 s68, s61, 0x3e3\n"
    "s_cmp_eq_u32 s68, 3\n"
    "s_cbranch_scc0 fb_Lnomods\n"
    "v_lshlrev_b32 v1, 7, v250\n"
    "s_lshl_b32 s61, %[blk], 13\n"
    "buffer_load_dword v249, v1, s[52:55], s61 offen sc1\n"
    "fb_Lnomods:\n"
    "s_cmp_lt_u32 s87, 112\n"
    "s_cbranch_scc0 fb_LnomodEa\n"
    "s_lshl_b32 s61, s87, 4\n"
    "s_lshr_b32 s68, s67, 4\n"
    "s_mul_i32 s69, s68, 1204224\n"
    "s_add_u32 s61, s61, s69\n"
    "s_mul_i32 s68, s68, 448\n"
    "v_mov_b32 v79, 0\n"
    "v_mov_b32 v60, 0\n"
    "v_mov_b32 v61, 0\n"
    "v_mov_b32 v62, 0\n"
    "v_mov_b32 v63, 0\n"
    "buffer_load_dwordx4 v[48:51], v10, s[72:75], s61 offen\n"
    "s_add_u32 s61, s61, 344064\n"
    "buffer_load_dwordx4 v[52:55], v10, s[72:75], s61 offen\n"
    "s_add_u32 s61, s61, 344064\n"
    "buffer_load_dwordx4 v[56:59], v10, s[72:75], s61 offen\n"
    "s_add_u32 s61, s61, 344064\n"
    "s_mov_b32 exec_hi, 0\n"
    "buffer_load_dwordx4 v[60:63], v10, s[72:75], s61 offen\n"
    "buffer_load_ushort v79, v11, s[76:79], s68 offen offset:384\n"
    "s_mov_b32 exec_hi, -1\n"
    "buffer_load_ushort v78, v11, s[76:79], s68 offen offset:256\n"
    "buffer_load_ushort v77, v11, s[76:79], s68 offen offset:128\n"
    "buffer_load_ushort v76, v11, s[76:79], s68 offen offset:0\n"
    "fb_LnomodEa:\n"
    "s_and_b32 s61, s87, 0x3ff\n"
    "s_cmp_lt_u32 s61, 112\n"
    "s_cbranch_scc0 fb_Lnomodd\n"
    "s_barrier\n"
    "fb_Lnomodd:\n"
    "s_mov_b64 s[48:49], %[x]\n"
    "s_and_b32 s49, s49, 0xffff\n"
    "s_mov_b32 s50, 1835008\n"
    "s_mov_b32 s51, 0x20000\n"
    "v_lshlrev_b32 v0, 4, v250\n"
    "v_mov_b32 v3, 0x7f000000\n"
    "v_cmp_gt_u32 vcc, 56, v250\n"
    "s_lshl_b32 s85, s67, 2\n"
    "s_add_u32 s85, s85, %[wv]\n"
    "s_mul_i32 s85, s85, 14336\n"
    "v_cndmask_b32 v0, v3, v0, vcc\n"
    "buffer_load_dwordx4 v[144:147], v0, s[48:51], s85 offen offset:0\n"
    "buffer_load_dwordx4 v[148:151], v0, s[48:51], s85 offen offset:896\n"
    "buffer_load_dwordx4 v[152:155], v0, s[48:51], s85 offen offset:1792\n"
    "buffer_load_dwordx4 v[156:159], v0, s[48:51], s85 offen offset:2688\n"
    "s_add_u32 s85, s85, 3584\n"
    "buffer_load_dwordx4 v[160:163], v0, s[48:51], s85 offen offset:0\n"
    "buffer_load_dwordx4 v[164:167], v0, s[48:51], s85 offen offset:896\n"
    "buffer_load_dwordx4 v[168:171], v0, s[48:51], s85 offen offset:1792\n"
    "buffer_load_dwordx4 v[172:175], v0, s[48:51], s85 offen offset:2688\n"
    "s_add_u32 s85, s85, 3584\n"
    "buffer_load_dwordx4 v[176:179], v0, s[48:51], s85 offen offset:0\n"
    "buffer_load_dwordx4 v[180:183], v0, s[48:51], s85 offen offset:896\n"
    "buffer_load_dwordx4 v[184:187], v0, s[48:51], s85 offen offset:1792\n"
    "buffer_load_dwordx4 v[188:191], v0, s[48:51], s85 offen offset:2688\n"
    "s_add_u32 s85, s85, 3584\n"
    "buffer_load_dwordx4 v[192:195], v0, s[48:51], s85 offen offset:0\n"
    "buffer_load_dwordx4 v[196:199], v0, s[48:51], s85 offen offset:896\n"
    "buffer_load_dwordx4 v[200:203], v0, s[48:51], s85 offen offset:1792\n"
    "buffer_load_dwordx4 v[204:207], v0, s[48:51], s85 offen offset:2688\n"
    "s_lshl_b32 s59, %[wv], 12\n"
    "s_mov_b64 s[44:45], %[wq]\n"
    "s_and_b32 s45, s45, 0xffff\n"
    "s_mov_b32 s46, 3670016\n"
    "s_mov_b32 s47, 0x20000\n"
    "v_lshrrev_b32 v0, 3, v250\n"
    "v_bfe_u32 v1, v250, 4, 2\n"
    "v_lshlrev_b32 v1, 1, v1\n"
    "v_and_b32 v2, 7, v250\n"
    "v_xor_b32 v2, v2, v1\n"
    "v_lshlrev_b32 v2, 4, v2\n"
    "v_lshl_add_u32 v244, v0, 13, v2\n"
    "v_add_u32 v245, 64512, v244\n"
    "v_add_u32 v246, 129024, v244\n"
    "v_add_u32 v247, 193536, v244\n"
    "v_lshl_add_u32 v224, v0, 9, v2\n"
    "v_mul_u32_u24 v3, 896, v0\n"
    "v_add_u32 v252, v3, v2\n"
    "s_lshl_b32 s69, s66, 10\n"
    "s_lshl_b32 s63, %[wv], 18\n"
    "s_add_u32 s63, s63, s69\n"
    "s_and_b32 s61, %[wv], 1\n"
    "s_lshl_b32 s70, s61, 18\n"
    "s_add_u32 s70, s70, s69\n"
    "s_lshr_b32 s68, %[wv], 1\n"
    "s_lshl_b32 s68, s68, 9\n"
    "s_add_u32 s70, s70, s68\n"
    "v_lshlrev_b32 v248, 7, v250\n"
    "v_and_b32 v1, 7, v250\n"
    "v_lshlrev_b32 v1, 7, v1\n"
    "v_lshl_add_u32 v3, v0, 13, v1\n"
    "s_mul_i32 s61, s67, 14\n"
    "s_lshl_b32 s68, %[wv], 2\n"
    "s_min_u32 s68, s68, 10\n"
    "s_add_u32 s61, s61, s68\n"
    "s_lshl_b32 s61, s61, 13\n"
    "s_add_u32 s61, s61, s69\n"
    "s_mov_b32 exec_hi, 0\n"
    "buffer_load_dword v249, v3, s[44:47], s61 offen\n"
    "s_mov_b32 exec_hi, -1\n"
    "s_getpc_b64 s[88:89]\n"
    "fb_Lpc:\n"
    "s_sub_u32 s88, s88, fb_Lpc-fb_Lstart\n"
    "s_subb_u32 s89, s89, 0\n"
    "s_and_b32 s89, s89, 0xffff\n"
    "s_mov_b32 s90, fb_Lend-fb_Lstart+11264\n"
    "s_mov_b32 s91, 0x20000\n"
    "s_lshl_b32 s61, s67, 2\n"
    "s_add_u32 s61, s61, %[wv]\n"
    "s_mul_i32 s61, s61, 1024\n"
    "s_mov_b64 exec, 255\n"
    "buffer_load_dword v249, v248, s[88:91], s61 offen\n"
    "s_mov_b64 exec, -1\n"
    "s_mov_b32 s69, 0xffff0000\n"
    "s_lshl_b32 s68, s66, 20\n"
    "s_mov_b64 s[48:49], %[k]\n"
    "s_add_u32 s48, s48, s68\n"
    "s_addc_u32 s49, s49, 0\n"
    "s_and_b32 s49, s49, 0xffff\n"
    "s_mov_b32 s50, 1048576\n"
    "s_mov_b32 s51, 0x20000\n"
    "s_mov_b64 s[88:89], %[v]\n"
    "s_add_u32 s88, s88, s68\n"
    "s_addc_u32 s89, s89, 0\n"
    "s_and_b32 s89, s89, 0xffff\n"
    "s_mov_b32 s90, 1048576\n"
    "s_mov_b32 s91, 0x20000\n"
    "s_mov_b32 s71, 0x3db8aa3b\n"
    "v_lshrrev_b32 v0, 2, v250\n"
    "v_bfe_u32 v1, v250, 3, 2\n"
    "v_and_b32 v2, 3, v250\n"
    "v_lshlrev_b32 v3, 7, v0\n"
    "v_lshl_add_u32 v3, v2, 3, v3\n"
    "v_lshl_add_u32 v3, v1, 5, v3\n"
    "v_add_u32 v216, 65536, v3\n"
    "s_lshl_b32 s61, %[wv], 5\n"
    "v_xor_b32 v233, s61, v216\n"
    "v_xor_b32 v232, s61, v3\n"
    "v_lshlrev_b32 v0, 6, v0\n"
    "v_and_b32 v1, 1, v250\n"
    "v_lshl_add_u32 v0, v1, 5, v0\n"
    "v_bfe_u32 v1, v250, 1, 1\n"
    "v_lshl_add_u32 v0, v1, 3, v0\n"
    "s_mul_i32 s61, %[wv], 14848\n"
    "s_add_u32 s61, s61, 98304\n"
    "v_add_u32 v240, s61, v0\n"
    "v_and_b32 v1, 15, v250\n"
    "v_lshrrev_b32 v2, 4, v250\n"
    "v_mul_u32_u24 v0, 928, v1\n"
    "v_lshl_add_u32 v0, v2, 4, v0\n"
    "v_add_u32 v241, 98304, v0\n"
    "s_waitcnt vmcnt(18)\n"
    "v_add_u32 v229, 1, v229\n"
    "s_nop 0\n"
    "v_readfirstlane_b32 %[tag], v229\n"
    "s_cmp_lt_u32 s87, 112\n"
    "s_cbranch_scc0 fb_LnomodEb\n"
    "s_lshr_b32 s68, s67, 4\n"
    "s_mul_i32 s68, s68, 7168\n"
    "s_lshl_b32 s61, s87, 6\n"
    "s_add_u32 s61, s61, s68\n"
    "s_add_u32 s61, s61, 49152\n"
    "s_waitcnt vmcnt(18)\n"
    "v_lshlrev_b32 v76, 16, v76\n"
    "v_lshlrev_b32 v77, 16, v77\n"
    "v_lshlrev_b32 v78, 16, v78\n"
    "v_lshlrev_b32 v79, 16, v79\n"
    "v_mul_f32 v84, 0xbfb8aa3b, v76\n"
    "v_mul_f32 v85, 0xbfb8aa3b, v77\n"
    "v_mul_f32 v86, 0xbfb8aa3b, v78\n"
    "v_mul_f32 v87, 0xbfb8aa3b, v79\n"
    "v_exp_f32 v84, v84\n"
    "v_exp_f32 v85, v85\n"
    "v_exp_f32 v86, v86\n"
    "v_exp_f32 v87, v87\n"
    "v_add_f32 v84, 1.0, v84\n"
    "v_add_f32 v85, 1.0, v85\n"
    "v_add_f32 v86, 1.0, v86\n"
    "v_add_f32 v87, 1.0, v87\n"
    "v_rcp_f32 v84, v84\n"
    "v_rcp_f32 v85, v85\n"
    "v_rcp_f32 v86, v86\n"
    "v_rcp_f32 v87, v87\n"
    "v_mul_f32 v76, v76, v84\n"
    "v_mul_f32 v77, v77, v85\n"
    "v_mul_f32 v78, v78, v86\n"
    "v_mul_f32 v79, v79, v87\n"
    "v_cvt_pk_bf16_f32 v84, v76, 0\n"
    "v_cvt_pk_bf16_f32 v92, 0, v76\n"
    "v_cvt_pk_bf16_f32 v85, v77, 0\n"
    "v_cvt_pk_bf16_f32 v93, 0, v77\n"
    "v_cvt_pk_bf16_f32 v86, v78, 0\n"
    "v_cvt_pk_bf16_f32 v94, 0, v78\n"
    "v_cvt_pk_bf16_f32 v87, v79, 0\n"
    "v_cvt_pk_bf16_f32 v95, 0, v79\n"
    "v_dot2_f32_bf16 v100, v48, v84, 0\n"
    "v_dot2_f32_bf16 v101, v48, v92, 0\n"
    "v_dot2_f32_bf16 v102, v49, v84, 0\n"
    "v_dot2_f32_bf16 v103, v49, v92, 0\n"
    "v_dot2_f32_bf16 v104, v50, v84, 0\n"
    "v_dot2_f32_bf16 v105, v50, v92, 0\n"
    "v_dot2_f32_bf16 v106, v51, v84, 0\n"
    "v_dot2_f32_bf16 v107, v51, v92, 0\n"
    "v_dot2_f32_bf16 v100, v52, v85, v100\n"
    "v_dot2_f32_bf16 v101, v52, v93, v101\n"
    "v_dot2_f32_bf16 v102, v53, v85, v102\n"
    "v_dot2_f32_bf16 v103, v53, v93, v103\n"
    "v_dot2_f32_bf16 v104, v54, v85, v104\n"
    "v_dot2_f32_bf16 v105, v54, v93, v105\n"
    "v_dot2_f32_bf16 v106, v55, v85, v106\n"
    "v_dot2_f32_bf16 v107, v55, v93, v107\n"
    "v_dot2_f32_bf16 v100, v56, v86, v100\n"
    "v_dot2_f32_bf16 v101, v56, v94, v101\n"
    "v_dot2_f32_bf16 v102, v57, v86, v102\n"
    "v_dot2_f32_bf16 v103, v57, v94, v103\n"
    "v_dot2_f32_bf16 v104, v58, v86, v104\n"
    "v_dot2_f32_bf16 v105, v58, v94, v105\n"
    "v_dot2_f32_bf16 v106, v59, v86, v106\n"
    "v_dot2_f32_bf16 v107, v59, v94, v107\n"
    "v_dot2_f32_bf16 v100, v60, v87, v100\n"
    "v_dot2_f32_bf16 v101, v60, v95, v101\n"
    "v_dot2_f32_bf16 v102, v61, v87, v102\n"
    "v_dot2_f32_bf16 v103, v61, v95, v103\n"
    "v_dot2_f32_bf16 v104, v62, v87, v104\n"
    "v_dot2_f32_bf16 v105, v62, v95, v105\n"
    "v_dot2_f32_bf16 v106, v63, v87, v106\n"
    "v_dot2_f32_bf16 v107, v63, v95, v107\n"
    "s_nop 2\n"
    "v_permlane32_swap_b32 v100, v104\n"
    "v_permlane32_swap_b32 v101, v105\n"
    "v_permlane32_swap_b32 v102, v106\n"
    "v_permlane32_swap_b32 v103, v107\n"
    "v_add_f32 v100, v100, v104\n"
    "v_add_f32 v101, v101, v105\n"
    "v_add_f32 v102, v102, v106\n"
    "v_add_f32 v103, v103, v107\n"
    "s_nop 0\n"
    "v_permlane16_swap_b32 v100, v102\n"
    "v_permlane16_swap_b32 v101, v103\n"
    "v_add_f32 v100, v100, v102\n"
    "v_add_f32 v101, v101, v103\n"
    "s_nop 0\n"
    "v_add_f32_dpp v100, v100, v100 row_ror:8 row_mask:0xf bank_mask:0xf\n"
    "v_add_f32_dpp v101, v101, v101 row_ror:8 row_mask:0xf bank_mask:0xf\n"
    "s_nop 0\n"
    "v_add_f32_dpp v100, v100, v100 row_ror:4 row_mask:0xf bank_mask:0xf\n"
    "v_add_f32_dpp v101, v101, v101 row_ror:4 row_mask:0xf bank_mask:0xf\n"
    "s_nop 0\n"
    "v_add_f32_dpp v100, v100, v100 row_ror:2 row_mask:0xf bank_mask:0xf\n"
    "v_add_f32_dpp v101, v101, v101 row_ror:2 row_mask:0xf bank_mask:0xf\n"
    "s_nop 0\n"
    "v_add_f32_dpp v100, v100, v100 row_ror:1 row_mask:0xf bank_mask:0xf\n"
    "v_add_f32_dpp v101, v101, v101 row_ror:1 row_mask:0xf bank_mask:0xf\n"
    "v_mov_b32 v102, v101\n"
    "v_mov_b32 v101, v229\n"
    "v_mov_b32 v103, v229\n"
    "s_mov_b32 exec_lo, 0x00010001\n"
    "s_mov_b32 exec_hi, 0x00010001\n"
    "buffer_store_dwordx4 v[100:103], v250, s[52:55], s61 offen sc1\n"
    "s_mov_b64 exec, -1\n"
    "s_nop 0\n"
    "fb_LnomodEb:\n"
    "s_and_b32 s68, %[wv], 1\n"
    "s_mul_i32 s61, s68, 3584\n"
    "s_add_u32 s61, s61, 49152\n"
    "v_lshlrev_b32 v8, 4, v250\n"
    "v_and_b32 v13, 31, v250\n"
    "v_lshlrev_b32 v13, 4, v13\n"
    "v_add_u32 v8, s61, v8\n"
    "v_add_u32 v13, s61, v13\n"
    "v_lshlrev_b32 v9, 5, v250\n"
    "v_add_u32 v9, 157696, v9\n"
    "s_xor_b32 s68, s68, 1\n"
    "s_lshl_b32 s68, s68, 11\n"
    "s_add_u32 s68, s68, 157696\n"
    "v_lshlrev_b32 v12, 3, v250\n"
    "v_add_u32 v12, s68, v12\n"
    "s_mov_b32 s58, 7168\n"
    "s_waitcnt vmcnt(16)\n"
    "v_lshlrev_b32 v16, 16, v144\n"
    "v_and_b32 v17, s69, v144\n"
    "v_lshlrev_b32 v18, 16, v145\n"
    "v_and_b32 v19, s69, v145\n"
    "v_lshlrev_b32 v20, 16, v146\n"
    "v_and_b32 v21, s69, v146\n"
    "v_lshlrev_b32 v22, 16, v147\n"
    "v_and_b32 v23, s69, v147\n"
    "v_pk_add_f32 v[144:145], v[16:17], v[18:19]\n"
    "v_pk_mul_f32 v[146:147], v[16:17], v[16:17]\n"
    "v_pk_fma_f32 v[146:147], v[18:19], v[18:19], v[146:147]\n"
    "v_pk_add_f32 v[144:145], v[144:145], v[20:21]\n"
    "v_pk_fma_f32 v[146:147], v[20:21], v[20:21], v[146:147]\n"
    "v_pk_add_f32 v[144:145], v[144:145], v[22:23]\n"
    "v_pk_fma_f32 v[146:147], v[22:23], v[22:23], v[146:147]\n"
    "v_add_f32 v144, v144, v145\n"
    "v_add_f32 v146, v146, v147\n"
    "v_lshlrev_b32 v24, 16, v148\n"
    "v_and_b32 v25, s69, v148\n"
    "v_lshlrev_b32 v26, 16, v149\n"
    "v_and_b32 v27, s69, v149\n"
    "v_lshlrev_b32 v28, 16, v150\n"
    "v_and_b32 v29, s69, v150\n"
    "v_lshlrev_b32 v30, 16, v151\n"
    "v_and_b32 v31, s69, v151\n"
    "v_pk_add_f32 v[148:149], v[24:25], v[26:27]\n"
    "v_pk_mul_f32 v[150:151], v[24:25], v[24:25]\n"
    "v_pk_fma_f32 v[150:151], v[26:27], v[26:27], v[150:151]\n"
    "v_pk_add_f32 v[148:149], v[148:149], v[28:29]\n"
    "v_pk_fma_f32 v[150:151], v[28:29], v[28:29], v[150:151]\n"
    "v_pk_add_f32 v[148:149], v[148:149], v[30:31]\n"
    "v_pk_fma_f32 v[150:151], v[30:31], v[30:31], v[150:151]\n"
    "v_add_f32 v148, v148, v149\n"
    "v_add_f32 v150, v150, v151\n"
    "s_waitcnt vmcnt(14)\n"
    "v_lshlrev_b32 v32, 16, v152\n"
    "v_and_b32 v33, s69, v152\n"
    "v_lshlrev_b32 v34, 16, v153\n"
    "v_and_b32 v35, s69, v153\n"
    "v_lshlrev_b32 v36, 16, v154\n"
    "v_and_b32 v37, s69, v154\n"
    "v_lshlrev_b32 v38, 16, v155\n"
    "v_and_b32 v39, s69, v155\n"
    "v_pk_add_f32 v[152:153], v[32:33], v[34:35]\n"
    "v_pk_mul_f32 v[154:155], v[32:33], v[32:33]\n"
    "v_pk_fma_f32 v[154:155], v[34:35], v[34:35], v[154:155]\n"
    "v_pk_add_f32 v[152:153], v[152:153], v[36:37]\n"
    "v_pk_fma_f32 v[154:155], v[36:37], v[36:37], v[154:155]\n"
    "v_pk_add_f32 v[152:153], v[152:153], v[38:39]\n"
    "v_pk_fma_f32 v[154:155], v[38:39], v[38:39], v[154:155]\n"
    "v_add_f32 v152, v152, v153\n"
    "v_add_f32 v154, v154, v155\n"
    "v_lshlrev_b32 v40, 16, v156\n"
    "v_and_b32 v41, s69, v156\n"
    "v_lshlrev_b32 v42, 16, v157\n"
    "v_and_b32 v43, s69, v157\n"
    "v_lshlrev_b32 v44, 16, v158\n"
    "v_and_b32 v45, s69, v158\n"
    "v_lshlrev_b32 v46, 16, v159\n"
    "v_and_b32 v47, s69, v159\n"
    "v_pk_add_f32 v[156:157], v[40:41], v[42:43]\n"
    "v_pk_mul_f32 v[158:159], v[40:41], v[40:41]\n"
    "v_pk_fma_f32 v[158:159], v[42:43], v[42:43], v[158:159]\n"
    "v_pk_add_f32 v[156:157], v[156:157], v[44:45]\n"
    "v_pk_fma_f32 v[158:159], v[44:45], v[44:45], v[158:159]\n"
    "v_pk_add_f32 v[156:157], v[156:157], v[46:47]\n"
    "v_pk_fma_f32 v[158:159], v[46:47], v[46:47], v[158:159]\n"
    "v_add_f32 v156, v156, v157\n"
    "v_add_f32 v158, v158, v159\n"
    "s_waitcnt vmcnt(12)\n"
    "v_lshlrev_b32 v48, 16, v160\n"
    "v_and_b32 v49, s69, v160\n"
    "v_lshlrev_b32 v50, 16, v161\n"
    "v_and_b32 v51, s69, v161\n"
    "v_lshlrev_b32 v52, 16, v162\n"
    "v_and_b32 v53, s69, v162\n"
    "v_lshlrev_b32 v54, 16, v163\n"
    "v_and_b32 v55, s69, v163\n"
    "v_pk_add_f32 v[160:161], v[48:49], v[50:51]\n"
    "v_pk_mul_f32 v[162:163], v[48:49], v[48:49]\n"
    "v_pk_fma_f32 v[162:163], v[50:51], v[50:51], v[162:163]\n"
    "v_pk_add_f32 v[160:161], v[160:161], v[52:53]\n"
    "v_pk_fma_f32 v[162:163], v[52:53], v[52:53], v[162:163]\n"
    "v_pk_add_f32 v[160:161], v[160:161], v[54:55]\n"
    "v_pk_fma_f32 v[162:163], v[54:55], v[54:55], v[162:163]\n"
    "v_add_f32 v160, v160, v161\n"
    "v_add_f32 v162, v162, v163\n"
    "v_lshlrev_b32 v56, 16, v164\n"
    "v_and_b32 v57, s69, v164\n"
    "v_lshlrev_b32 v58, 16, v165\n"
    "v_and_b32 v59, s69, v165\n"
    "v_lshlrev_b32 v60, 16, v166\n"
    "v_and_b32 v61, s69, v166\n"
    "v_lshlrev_b32 v62, 16, v167\n"
    "v_and_b32 v63, s69, v167\n"
    "v_pk_add_f32 v[164:165], v[56:57], v[58:59]\n"
    "v_pk_mul_f32 v[166:167], v[56:57], v[56:57]\n"
    "v_pk_fma_f32 v[166:167], v[58:59], v[58:59], v[166:167]\n"
    "v_pk_add_f32 v[164:165], v[164:165], v[60:61]\n"
    "v_pk_fma_f32 v[166:167], v[60:61], v[60:61], v[166:167]\n"
    "v_pk_add_f32 v[164:165], v[164:165], v[62:63]\n"
    "v_pk_fma_f32 v[166:167], v[62:63], v[62:63], v[166:167]\n"
    "v_add_f32 v164, v164, v165\n"
    "v_add_f32 v166, v166, v167\n"
    "s_waitcnt vmcnt(10)\n"
    "v_lshlrev_b32 v64, 16, v168\n"
    "v_and_b32 v65, s69, v168\n"
    "v_lshlrev_b32 v66, 16, v169\n"
    "v_and_b32 v67, s69, v169\n"
    "v_lshlrev_b32 v68, 16, v170\n"
    "v_and_b32 v69, s69, v170\n"
    "v_lshlrev_b32 v70, 16, v171\n"
    "v_and_b32 v71, s69, v171\n"
    "v_pk_add_f32 v[168:169], v[64:65], v[66:67]\n"
    "v_pk_mul_f32 v[170:171], v[64:65], v[64:65]\n"
    "v_pk_fma_f32 v[170:171], v[66:67], v[66:67], v[170:171]\n"
    "v_pk_add_f32 v[168:169], v[168:169], v[68:69]\n"
    "v_pk_fma_f32 v[170:171], v[68:69], v[68:69], v[170:171]\n"
    "v_pk_add_f32 v[168:169], v[168:169], v[70:71]\n"
    "v_pk_fma_f32 v[170:171], v[70:71], v[70:71], v[170:171]\n"
    "v_add_f32 v168, v168, v169\n"
    "v_add_f32 v170, v170, v171\n"
    "s_add_u32 m0, s59, 32768\n"
    "s_add_u32 s62, s63, 0\n"
    "buffer_load_dwordx4 v244, s[44:47], s62 offen offset:0 lds\n"
    "v_lshlrev_b32 v72, 16, v172\n"
    "v_and_b32 v73, s69, v172\n"
    "v_lshlrev_b32 v74, 16, v173\n"
    "v_and_b32 v75, s69, v173\n"
    "v_lshlrev_b32 v76, 16, v174\n"
    "v_and_b32 v77, s69, v174\n"
    "v_lshlrev_b32 v78, 16, v175\n"
    "v_and_b32 v79, s69, v175\n"
    "v_pk_add_f32 v[172:173], v[72:73], v[74:75]\n"
    "v_pk_mul_f32 v[174:175], v[72:73], v[72:73]\n"
    "v_pk_fma_f32 v[174:175], v[74:75], v[74:75], v[174:175]\n"
    "v_pk_add_f32 v[172:173], v[172:173], v[76:77]\n"
    "v_pk_fma_f32 v[174:175], v[76:77], v[76:77], v[174:175]\n"
    "v_pk_add_f32 v[172:173], v[172:173], v[78:79]\n"
    "v_pk_fma_f32 v[174:175], v[78:79], v[78:79], v[174:175]\n"
    "v_add_f32 v172, v172, v173\n"
    "v_add_f32 v174, v174, v175\n"
    "buffer_load_dwordx4 v245, s[44:47], s62 offen offset:1024 lds\n"
    "s_waitcnt vmcnt(10)\n"
    "v_lshlrev_b32 v80, 16, v176\n"
    "v_and_b32 v81, s69, v176\n"
    "v_lshlrev_b32 v82, 16, v177\n"
    "v_and_b32 v83, s69, v177\n"
    "v_lshlrev_b32 v84, 16, v178\n"
    "v_and_b32 v85, s69, v178\n"
    "v_lshlrev_b32 v86, 16, v179\n"
    "v_and_b32 v87, s69, v179\n"
    "v_pk_add_f32 v[176:177], v[80:81], v[82:83]\n"
    "v_pk_mul_f32 v[178:179], v[80:81], v[80:81]\n"
    "v_pk_fma_f32 v[178:179], v[82:83], v[82:83], v[178:179]\n"
    "v_pk_add_f32 v[176:177], v[176:177], v[84:85]\n"
    "v_pk_fma_f32 v[178:179], v[84:85], v[84:85], v[178:179]\n"
    "v_pk_add_f32 v[176:177], v[176:177], v[86:87]\n"
    "v_pk_fma_f32 v[178:179], v[86:87], v[86:87], v[178:179]\n"
    "v_add_f32 v176, v176, v177\n"
    "v_add_f32 v178, v178, v179\n"
    "buffer_load_dwordx4 v246, s[44:47], s62 offen offset:2048 lds\n"
    "v_lshlrev_b32 v88, 16, v180\n"
    "v_and_b32 v89, s69, v180\n"
    "v_lshlrev_b32 v90, 16, v181\n"
    "v_and_b32 v91, s69, v181\n"
    "v_lshlrev_b32 v92, 16, v182\n"
    "v_and_b32 v93, s69, v182\n"
    "v_lshlrev_b32 v94, 16, v183\n"
    "v_and_b32 v95, s69, v183\n"
    "v_pk_add_f32 v[180:181], v[88:89], v[90:91]\n"
    "v_pk_mul_f32 v[182:183], v[88:89], v[88:89]\n"
    "v_pk_fma_f32 v[182:183], v[90:91], v[90:91], v[182:183]\n"
    "v_pk_add_f32 v[180:181], v[180:181], v[92:93]\n"
    "v_pk_fma_f32 v[182:183], v[92:93], v[92:93], v[182:183]\n"
    "v_pk_add_f32 v[180:181], v[180:181], v[94:95]\n"
    "v_pk_fma_f32 v[182:183], v[94:95], v[94:95], v[182:183]\n"
    "v_add_f32 v180, v180, v181\n"
    "v_add_f32 v182, v182, v183\n"
    "buffer_load_dwordx4 v247, s[44:47], s62 offen offset:3072 lds\n"
    "s_waitcnt vmcnt(10)\n"
    "v_lshlrev_b32 v96, 16, v184\n"
    "v_and_b32 v97, s69, v184\n"
    "v_lshlrev_b32 v98, 16, v185\n"
    "v_and_b32 v99, s69, v185\n"
    "v_lshlrev_b32 v100, 16, v186\n"
    "v_and_b32 v101, s69, v186\n"
    "v_lshlrev_b32 v102, 16, v187\n"
    "v_and_b32 v103, s69, v187\n"
    "v_pk_add_f32 v[184:185], v[96:97], v[98:99]\n"
    "v_pk_mul_f32 v[186:187], v[96:97], v[96:97]\n"
    "v_pk_fma_f32 v[186:187], v[98:99], v[98:99], v[186:187]\n"
    "v_pk_add_f32 v[184:185], v[184:185], v[100:101]\n"
    "v_pk_fma_f32 v[186:187], v[100:101], v[100:101], v[186:187]\n"
    "v_pk_add_f32 v[184:185], v[184:185], v[102:103]\n"
    "v_pk_fma_f32 v[186:187], v[102:103], v[102:103], v[186:187]\n"
    "v_add_f32 v184, v184, v185\n"
    "v_add_f32 v186, v186, v187\n"
    "s_add_u32 m0, s59, 49152\n"
    "s_add_u32 s62, s63, 512\n"
    "buffer_load_dwordx4 v244, s[44:47], s62 offen offset:0 lds\n"
    "v_lshlrev_b32 v104, 16, v188\n"
    "v_and_b32 v105, s69, v188\n"
    "v_lshlrev_b32 v106, 16, v189\n"
    "v_and_b32 v107, s69, v189\n"
    "v_lshlrev_b32 v108, 16, v190\n"
    "v_and_b32 v109, s69, v190\n"
    "v_lshlrev_b32 v110, 16, v191\n"
    "v_and_b32 v111, s69, v191\n"
    "v_pk_add_f32 v[188:189], v[104:105], v[106:107]\n"
    "v_pk_mul_f32 v[190:191], v[104:105], v[104:105]\n"
    "v_pk_fma_f32 v[190:191], v[106:107], v[106:107], v[190:191]\n"
    "v_pk_add_f32 v[188:189], v[188:189], v[108:109]\n"
    "v_pk_fma_f32 v[190:191], v[108:109], v[108:109], v[190:191]\n"
    "v_pk_add_f32 v[188:189], v[188:189], v[110:111]\n"
    "v_pk_fma_f32 v[190:191], v[110:111], v[110:111], v[190:191]\n"
    "v_add_f32 v188, v188, v189\n"
    "v_add_f32 v190, v190, v191\n"
    "buffer_load_dwordx4 v245, s[44:47], s62 offen offset:1024 lds\n"
    "s_waitcnt vmcnt(10)\n"
    "v_lshlrev_b32 v112, 16, v192\n"
    "v_and_b32 v113, s69, v192\n"
    "v_lshlrev_b32 v114, 16, v193\n"
    "v_and_b32 v115, s69, v193\n"
    "v_lshlrev_b32 v116, 16, v194\n"
    "v_and_b32 v117, s69, v194\n"
    "v_lshlrev_b32 v118, 16, v195\n"
    "v_and_b32 v119, s69, v195\n"
    "v_pk_add_f32 v[192:193], v[112:113], v[114:115]\n"
    "v_pk_mul_f32 v[194:195], v[112:113], v[112:113]\n"
    "v_pk_fma_f32 v[194:195], v[114:115], v[114:115], v[194:195]\n"
    "v_pk_add_f32 v[192:193], v[192:193], v[116:117]\n"
    "v_pk_fma_f32 v[194:195], v[116:117], v[116:117], v[194:195]\n"
    "v_pk_add_f32 v[192:193], v[192:193], v[118:119]\n"
    "v_pk_fma_f32 v[194:195], v[118:119], v[118:119], v[194:195]\n"
    "v_add_f32 v192, v192, v193\n"
    "v_add_f32 v194, v194, v195\n"
    "buffer_load_dwordx4 v246, s[44:47], s62 offen offset:2048 lds\n"
    "v_lshlrev_b32 v120, 16, v196\n"
    "v_and_b32 v121, s69, v196\n"
    "v_lshlrev_b32 v122, 16, v197\n"
    "v_and_b32 v123, s69, v197\n"
    "v_lshlrev_b32 v124, 16, v198\n"
    "v_and_b32 v125, s69, v198\n"
    "v_lshlrev_b32 v126, 16, v199\n"
    "v_and_b32 v127, s69, v199\n"
    "v_pk_add_f32 v[196:197], v[120:121], v[122:123]\n"
    "v_pk_mul_f32 v[198:199], v[120:121], v[120:121]\n"
    "v_pk_fma_f32 v[198:199], v[122:123], v[122:123], v[198:199]\n"
    "v_pk_add_f32 v[196:197], v[196:197], v[124:125]\n"
    "v_pk_fma_f32 v[198:199], v[124:125], v[124:125], v[198:199]\n"
    "v_pk_add_f32 v[196:197], v[196:197], v[126:127]\n"
    "v_pk_fma_f32 v[198:199], v[126:127], v[126:127], v[198:199]\n"
    "v_add_f32 v196, v196, v197\n"
    "v_add_f32 v198, v198, v199\n"
    "buffer_load_dwordx4 v247, s[44:47], s62 offen offset:3072 lds\n"
    "s_waitcnt vmcnt(10)\n"
    "v_lshlrev_b32 v128, 16, v200\n"
    "v_and_b32 v129, s69, v200\n"
    "v_lshlrev_b32 v130, 16, v201\n"
    "v_and_b32 v131, s69, v201\n"
    "v_lshlrev_b32 v132, 16, v202\n"
    "v_and_b32 v133, s69, v202\n"
    "v_lshlrev_b32 v134, 16, v203\n"
    "v_and_b32 v135, s69, v203\n"
    "v_pk_add_f32 v[200:201], v[128:129], v[130:131]\n"
    "v_pk_mul_f32 v[202:203], v[128:129], v[128:129]\n"
    "v_pk_fma_f32 v[202:203], v[130:131], v[130:131], v[202:203]\n"
    "v_pk_add_f32 v[200:201], v[200:201], v[132:133]\n"
    "v_pk_fma_f32 v[202:203], v[132:133], v[132:133], v[202:203]\n"
    "v_pk_add_f32 v[200:201], v[200:201], v[134:135]\n"
    "v_pk_fma_f32 v[202:203], v[134:135], v[134:135], v[202:203]\n"
    "v_add_f32 v200, v200, v201\n"
    "v_add_f32 v202, v202, v203\n"
    "s_add_u32 m0, s59, 65536\n"
    "s_add_u32 s62, s63, 1048576\n"
    "buffer_load_dwordx4 v244, s[44:47], s62 offen offset:0 lds\n"
    "v_lshlrev_b32 v136, 16, v204\n"
    "v_and_b32 v137, s69, v204\n"
    "v_lshlrev_b32 v138, 16, v205\n"
    "v_and_b32 v139, s69, v205\n"
    "v_lshlrev_b32 v140, 16, v206\n"
    "v_and_b32 v141, s69, v206\n"
    "v_lshlrev_b32 v142, 16, v207\n"
    "v_and_b32 v143, s69, v207\n"
    "v_pk_add_f32 v[204:205], v[136:137], v[138:139]\n"
    "v_pk_mul_f32 v[206:207], v[136:137], v[136:137]\n"
    "v_pk_fma_f32 v[206:207], v[138:139], v[138:139], v[206:207]\n"
    "v_pk_add_f32 v[204:205], v[204:205], v[140:141]\n"
    "v_pk_fma_f32 v[206:207], v[140:141], v[140:141], v[206:207]\n"
    "v_pk_add_f32 v[204:205], v[204:205], v[142:143]\n"
    "v_pk_fma_f32 v[206:207], v[142:143], v[142:143], v[206:207]\n"
    "v_add_f32 v204, v204, v205\n"
    "v_add_f32 v206, v206, v207\n"
    "buffer_load_dwordx4 v245, s[44:47], s62 offen offset:1024 lds\n"
    "v_permlane32_swap_b32 v144, v176\n"
    "v_permlane32_swap_b32 v148, v180\n"
    "v_permlane32_swap_b32 v152, v184\n"
    "v_permlane32_swap_b32 v156, v188\n"
    "v_permlane32_swap_b32 v160, v192\n"
    "v_permlane32_swap_b32 v164, v196\n"
    "v_permlane32_swap_b32 v168, v200\n"
    "v_permlane32_swap_b32 v172, v204\n"
    "v_permlane32_swap_b32 v146, v178\n"
    "v_permlane32_swap_b32 v150, v182\n"
    "v_permlane32_swap_b32 v154, v186\n"
    "v_permlane32_swap_b32 v158, v190\n"
    "v_permlane32_swap_b32 v162, v194\n"
    "v_permlane32_swap_b32 v166, v198\n"
    "v_permlane32_swap_b32 v170, v202\n"
    "v_permlane32_swap_b32 v174, v206\n"
    "v_add_f32 v144, v144, v176\n"
    "v_add_f32 v148, v148, v180\n"
    "v_add_f32 v152, v152, v184\n"
    "v_add_f32 v156, v156, v188\n"
    "v_add_f32 v160, v160, v192\n"
    "v_add_f32 v164, v164, v196\n"
    "v_add_f32 v168, v168, v200\n"
    "v_add_f32 v172, v172, v204\n"
    "v_add_f32 v146, v146, v178\n"
    "v_add_f32 v150, v150, v182\n"
    "v_add_f32 v154, v154, v186\n"
    "v_add_f32 v158, v158, v190\n"
    "v_add_f32 v162, v162, v194\n"
    "v_add_f32 v166, v166, v198\n"
    "v_add_f32 v170, v170, v202\n"
    "v_add_f32 v174, v174, v206\n"
    "v_permlane16_swap_b32 v144, v160\n"
    "v_permlane16_swap_b32 v148, v164\n"
    "v_permlane16_swap_b32 v152, v168\n"
    "v_permlane16_swap_b32 v156, v172\n"
    "v_permlane16_swap_b32 v146, v162\n"
    "v_permlane16_swap_b32 v150, v166\n"
    "v_permlane16_swap_b32 v154, v170\n"
    "v_permlane16_swap_b32 v158, v174\n"
    "v_add_f32 v0, v144, v160\n"
    "v_add_f32 v1, v148, v164\n"
    "v_add_f32 v2, v152, v168\n"
    "v_add_f32 v3, v156, v172\n"
    "v_add_f32 v4, v146, v162\n"
    "v_add_f32 v5, v150, v166\n"
    "v_add_f32 v6, v154, v170\n"
    "v_add_f32 v7, v158, v174\n"
    "v_add_f32_dpp v0, v0, v0 row_ror:8 row_mask:0xf bank_mask:0xf\n"
    "v_add_f32_dpp v1, v1, v1 row_ror:8 row_mask:0xf bank_mask:0xf\n"
    "v_add_f32_dpp v2, v2, v2 row_ror:8 row_mask:0xf bank_mask:0xf\n"
    "v_add_f32_dpp v3, v3, v3 row_ror:8 row_mask:0xf bank_mask:0xf\n"
    "v_add_f32_dpp v4, v4, v4 row_ror:8 row_mask:0xf bank_mask:0xf\n"
    "v_add_f32_dpp v5, v5, v5 row_ror:8 row_mask:0xf bank_mask:0xf\n"
    "v_add_f32_dpp v6, v6, v6 row_ror:8 row_mask:0xf bank_mask:0xf\n"
    "v_add_f32_dpp v7, v7, v7 row_ror:8 row_mask:0xf bank_mask:0xf\n"
    "v_add_f32_dpp v0, v0, v0 row_ror:4 row_mask:0xf bank_mask:0xf\n"
    "v_add_f32_dpp v1, v1, v1 row_ror:4 row_mask:0xf bank_mask:0xf\n"
    "v_add_f32_dpp v2, v2, v2 row_ror:4 row_mask:0xf bank_mask:0xf\n"
    "v_add_f32_dpp v3, v3, v3 row_ror:4 row_mask:0xf bank_mask:0xf\n"
    "v_add_f32_dpp v4, v4, v4 row_ror:4 row_mask:0xf bank_mask:0xf\n"
    "v_add_f32_dpp v5, v5, v5 row_ror:4 row_mask:0xf bank_mask:0xf\n"
    "v_add_f32_dpp v6, v6, v6 row_ror:4 row_mask:0xf bank_mask:0xf\n"
    "v_add_f32_dpp v7, v7, v7 row_ror:4 row_mask:0xf bank_mask:0xf\n"
    "v_add_f32_dpp v0, v0, v0 row_ror:2 row_mask:0xf bank_mask:0xf\n"
    "v_add_f32_dpp v1, v1, v1 row_ror:2 row_mask:0xf bank_mask:0xf\n"
    "v_add_f32_dpp v2, v2, v2 row_ror:2 row_mask:0xf bank_mask:0xf\n"
    "v_add_f32_dpp v3, v3, v3 row_ror:2 row_mask:0xf bank_mask:0xf\n"
    "v_add_f32_dpp v4, v4, v4 row_ror:2 row_mask:0xf bank_mask:0xf\n"
    "v_add_f32_dpp v5, v5, v5 row_ror:2 row_mask:0xf bank_mask:0xf\n"
    "v_add_f32_dpp v6, v6, v6 row_ror:2 row_mask:0xf bank_mask:0xf\n"
    "v_add_f32_dpp v7, v7, v7 row_ror:2 row_mask:0xf bank_mask:0xf\n"
    "v_add_f32_dpp v0, v0, v0 row_ror:1 row_mask:0xf bank_mask:0xf\n"
    "v_add_f32_dpp v1, v1, v1 row_ror:1 row_mask:0xf bank_mask:0xf\n"
    "v_add_f32_dpp v2, v2, v2 row_ror:1 row_mask:0xf bank_mask:0xf\n"
    "v_add_f32_dpp v3, v3, v3 row_ror:1 row_mask:0xf bank_mask:0xf\n"
    "v_add_f32_dpp v4, v4, v4 row_ror:1 row_mask:0xf bank_mask:0xf\n"
    "v_add_f32_dpp v5, v5, v5 row_ror:1 row_mask:0xf bank_mask:0xf\n"
    "v_add_f32_dpp v6, v6, v6 row_ror:1 row_mask:0xf bank_mask:0xf\n"
    "v_add_f32_dpp v7, v7, v7 row_ror:1 row_mask:0xf bank_mask:0xf\n"
    "v_mul_f32 v0, 0x3b124925, v0\n"
    "v_mul_f32 v1, 0x3b124925, v1\n"
    "v_mul_f32 v2, 0x3b124925, v2\n"
    "v_mul_f32 v3, 0x3b124925, v3\n"
    "v_mul_f32 v4, 0x3b124925, v4\n"
    "v_mul_f32 v5, 0x3b124925, v5\n"
    "v_mul_f32 v6, 0x3b124925, v6\n"
    "v_mul_f32 v7, 0x3b124925, v7\n"
    "v_fma_f32 v4, -v0, v0, v4\n"
    "v_fma_f32 v5, -v1, v1, v5\n"
    "v_fma_f32 v6, -v2, v2, v6\n"
    "v_fma_f32 v7, -v3, v3, v7\n"
    "v_max_f32 v4, 0, v4\n"
    "v_max_f32 v5, 0, v5\n"
    "v_max_f32 v6, 0, v6\n"
    "v_max_f32 v7, 0, v7\n"
    "v_cvt_pk_bf16_f32 v4, 0, v4\n"
    "v_cvt_pk_bf16_f32 v5, 0, v5\n"
    "v_cvt_pk_bf16_f32 v6, 0, v6\n"
    "v_cvt_pk_bf16_f32 v7, 0, v7\n"
    "v_add_f32 v4, 0x3727c5ac, v4\n"
    "v_add_f32 v5, 0x3727c5ac, v5\n"
    "v_add_f32 v6, 0x3727c5ac, v6\n"
    "v_add_f32 v7, 0x3727c5ac, v7\n"
    "v_cvt_pk_bf16_f32 v4, 0, v4\n"
    "v_cvt_pk_bf16_f32 v5, 0, v5\n"
    "v_cvt_pk_bf16_f32 v6, 0, v6\n"
    "v_cvt_pk_bf16_f32 v7, 0, v7\n"
    "v_sqrt_f32 v4, v4\n"
    "v_sqrt_f32 v5, v5\n"
    "v_sqrt_f32 v6, v6\n"
    "v_sqrt_f32 v7, v7\n"
    "v_cvt_pk_bf16_f32 v0, 0, -v0\n"
    "v_cvt_pk_bf16_f32 v1, 0, -v1\n"
    "v_cvt_pk_bf16_f32 v2, 0, -v2\n"
    "v_cvt_pk_bf16_f32 v3, 0, -v3\n"
    "v_cvt_pk_bf16_f32 v4, 0, v4\n"
    "v_cvt_pk_bf16_f32 v5, 0, v5\n"
    "v_cvt_pk_bf16_f32 v6, 0, v6\n"
    "v_cvt_pk_bf16_f32 v7, 0, v7\n"
    "v_rcp_f32 v4, v4\n"
    "v_rcp_f32 v5, v5\n"
    "v_rcp_f32 v6, v6\n"
    "v_rcp_f32 v7, v7\n"
    "s_nop 0\n"
    "v_mul_f32 v0, v0, v4\n"
    "v_mul_f32 v1, v1, v5\n"
    "v_mul_f32 v2, v2, v6\n"
    "v_mul_f32 v3, v3, v7\n"
    "v_readlane_b32 s96, v4, 0\n"
    "v_readlane_b32 s97, v0, 0\n"
    "v_readlane_b32 s98, v5, 0\n"
    "v_readlane_b32 s99, v1, 0\n"
    "v_pk_fma_f32 v[16:17], v[16:17], s[96:97], s[96:97] op_sel:[0,0,1] op_sel_hi:[1,0,1]\n"
    "v_pk_fma_f32 v[18:19], v[18:19], s[96:97], s[96:97] op_sel:[0,0,1] op_sel_hi:[1,0,1]\n"
    "v_pk_fma_f32 v[20:21], v[20:21], s[96:97], s[96:97] op_sel:[0,0,1] op_sel_hi:[1,0,1]\n"
    "v_pk_fma_f32 v[22:23], v[22:23], s[96:97], s[96:97] op_sel:[0,0,1] op_sel_hi:[1,0,1]\n"
    "buffer_load_dwordx4 v246, s[44:47], s62 offen offset:2048 lds\n"
    "v_readlane_b32 s96, v6, 0\n"
    "v_readlane_b32 s97, v2, 0\n"
    "v_pk_fma_f32 v[24:25], v[24:25], s[98:99], s[98:99] op_sel:[0,0,1] op_sel_hi:[1,0,1]\n"
    "v_pk_fma_f32 v[26:27], v[26:27], s[98:99], s[98:99] op_sel:[0,0,1] op_sel_hi:[1,0,1]\n"
    "v_pk_fma_f32 v[28:29], v[28:29], s[98:99], s[98:99] op_sel:[0,0,1] op_sel_hi:[1,0,1]\n"
    "v_pk_fma_f32 v[30:31], v[30:31], s[98:99], s[98:99] op_sel:[0,0,1] op_sel_hi:[1,0,1]\n"
    "buffer_load_dwordx4 v247, s[44:47], s62 offen offset:3072 lds\n"
    "v_readlane_b32 s98, v7, 0\n"
    "v_readlane_b32 s99, v3, 0\n"
    "v_pk_fma_f32 v[32:33], v[32:33], s[96:97], s[96:97] op_sel:[0,0,1] op_sel_hi:[1,0,1]\n"
    "v_pk_fma_f32 v[34:35], v[34:35], s[96:97], s[96:97] op_sel:[0,0,1] op_sel_hi:[1,0,1]\n"
    "v_pk_fma_f32 v[36:37], v[36:37], s[96:97], s[96:97] op_sel:[0,0,1] op_sel_hi:[1,0,1]\n"
    "v_pk_fma_f32 v[38:39], v[38:39], s[96:97], s[96:97] op_sel:[0,0,1] op_sel_hi:[1,0,1]\n"
    "s_add_u32 m0, s59, 81920\n"
    "s_add_u32 s62, s63, 1049088\n"
    "buffer_load_dwordx4 v244, s[44:47], s62 offen offset:0 lds\n"
    "v_readlane_b32 s96, v4, 16\n"
    "v_readlane_b32 s97, v0, 16\n"
    "v_pk_fma_f32 v[40:41], v[40:41], s[98:99], s[98:99] op_sel:[0,0,1] op_sel_hi:[1,0,1]\n"
    "v_pk_fma_f32 v[42:43], v[42:43], s[98:99], s[98:99] op_sel:[0,0,1] op_sel_hi:[1,0,1]\n"
    "v_pk_fma_f32 v[44:45], v[44:45], s[98:99], s[98:99] op_sel:[0,0,1] op_sel_hi:[1,0,1]\n"
    "v_pk_fma_f32 v[46:47], v[46:47], s[98:99], s[98:99] op_sel:[0,0,1] op_sel_hi:[1,0,1]\n"
    "buffer_load_dwordx4 v245, s[44:47], s62 offen offset:1024 lds\n"
    "v_readlane_b32 s98, v5, 16\n"
    "v_readlane_b32 s99, v1, 16\n"
    "v_pk_fma_f32 v[48:49], v[48:49], s[96:97], s[96:97] op_sel:[0,0,1] op_sel_hi:[1,0,1]\n"
    "v_pk_fma_f32 v[50:51], v[50:51], s[96:97], s[96:97] op_sel:[0,0,1] op_sel_hi:[1,0,1]\n"
    "v_pk_fma_f32 v[52:53], v[52:53], s[96:97], s[96:97] op_sel:[0,0,1] op_sel_hi:[1,0,1]\n"
    "v_pk_fma_f32 v[54:55], v[54:55], s[96:97], s[96:97] op_sel:[0,0,1] op_sel_hi:[1,0,1]\n"
    "buffer_load_dwordx4 v246, s[44:47], s62 offen offset:2048 lds\n"
    "v_readlane_b32 s96, v6, 16\n"
    "v_readlane_b32 s97, v2, 16\n"
    "v_pk_fma_f32 v[56:57], v[56:57], s[98:99], s[98:99] op_sel:[0,0,1] op_sel_hi:[1,0,1]\n"
    "v_pk_fma_f32 v[58:59], v[58:59], s[98:99], s[98:99] op_sel:[0,0,1] op_sel_hi:[1,0,1]\n"
    "v_pk_fma_f32 v[60:61], v[60:61], s[98:99], s[98:99] op_sel:[0,0,1] op_sel_hi:[1,0,1]\n"
    "v_pk_fma_f32 v[62:63], v[62:63], s[98:99], s[98:99] op_sel:[0,0,1] op_sel_hi:[1,0,1]\n"
    "buffer_load_dwordx4 v247, s[44:47], s62 offen offset:3072 lds\n"
    "v_readlane_b32 s98, v7, 16\n"
    "v_readlane_b32 s99, v3, 16\n"
    "v_pk_fma_f32 v[64:65], v[64:65], s[96:97], s[96:97] op_sel:[0,0,1] op_sel_hi:[1,0,1]\n"
    "v_pk_fma_f32 v[66:67], v[66:67], s[96:97], s[96:97] op_sel:[0,0,1] op_sel_hi:[1,0,1]\n"
    "v_pk_fma_f32 v[68:69], v[68:69], s[96:97], s[96:97] op_sel:[0,0,1] op_sel_hi:[1,0,1]\n"
    "v_pk_fma_f32 v[70:71], v[70:71], s[96:97], s[96:97] op_sel:[0,0,1] op_sel_hi:[1,0,1]\n"
    "s_add_u32 m0, s59, 0\n"
    "s_add_u32 s62, s63, 2097152\n"
    "buffer_load_dwordx4 v244, s[44:47], s62 offen offset:0 lds\n"
    "v_readlane_b32 s96, v4, 32\n"
    "v_readlane_b32 s97, v0, 32\n"
    "v_pk_fma_f32 v[72:73], v[72:73], s[98:99], s[98:99] op_sel:[0,0,1] op_sel_hi:[1,0,1]\n"
    "v_pk_fma_f32 v[74:75], v[74:75], s[98:99], s[98:99] op_sel:[0,0,1] op_sel_hi:[1,0,1]\n"
    "v_pk_fma_f32 v[76:77], v[76:77], s[98:99], s[98:99] op_sel:[0,0,1] op_sel_hi:[1,0,1]\n"
    "v_pk_fma_f32 v[78:79], v[78:79], s[98:99], s[98:99] op_sel:[0,0,1] op_sel_hi:[1,0,1]\n"
    "buffer_load_dwordx4 v245, s[44:47], s62 offen offset:1024 lds\n"
    "v_readlane_b32 s98, v5, 32\n"
    "v_readlane_b32 s99, v1, 32\n"
    "v_pk_fma_f32 v[80:81], v[80:81], s[96:97], s[96:97] op_sel:[0,0,1] op_sel_hi:[1,0,1]\n"
    "v_pk_fma_f32 v[82:83], v[82:83], s[96:97], s[96:97] op_sel:[0,0,1] op_sel_hi:[1,0,1]\n"
    "v_pk_fma_f32 v[84:85], v[84:85], s[96:97], s[96:97] op_sel:[0,0,1] op_sel_hi:[1,0,1]\n"
    "v_pk_fma_f32 v[86:87], v[86:87], s[96:97], s[96:97] op_sel:[0,0,1] op_sel_hi:[1,0,1]\n"
    "s_cmp_ge_u32 %[wv], 2\n"
    "s_cbranch_scc0 fb_Lnomodp1\n"
    "buffer_load_dwordx4 v[144:147], v8, s[52:55], 0 offen offset:0 sc1\n"
    "buffer_load_dwordx4 v[148:151], v8, s[52:55], 0 offen offset:1024 sc1\n"
    "buffer_load_dwordx4 v[152:155], v8, s[52:55], 0 offen offset:2048 sc1\n"
    "buffer_load_dwordx4 v[156:159], v13, s[52:55], 0 offen offset:3072 sc1\n"
    "buffer_load_dwordx4 v[160:163], v8, s[52:55], s58 offen offset:0 sc1\n"
    "buffer_load_dwordx4 v[164:167], v8, s[52:55], s58 offen offset:1024 sc1\n"
    "buffer_load_dwordx4 v[168:171], v8, s[52:55], s58 offen offset:2048 sc1\n"
    "buffer_load_dwordx4 v[172:175], v13, s[52:55], s58 offen offset:3072 sc1\n"
    "fb_Lnomodp1:\n"
    "buffer_load_dwordx4 v246, s[44:47], s62 offen offset:2048 lds\n"
    "v_readlane_b32 s96, v6, 32\n"
    "v_readlane_b32 s97, v2, 32\n"
    "v_pk_fma_f32 v[88:89], v[88:89], s[98:99], s[98:99] op_sel:[0,0,1] op_sel_hi:[1,0,1]\n"
    "v_pk_fma_f32 v[90:91], v[90:91], s[98:99], s[98:99] op_sel:[0,0,1] op_sel_hi:[1,0,1]\n"
    "v_pk_fma_f32 v[92:93], v[92:93], s[98:99], s[98:99] op_sel:[0,0,1] op_sel_hi:[1,0,1]\n"
    "v_pk_fma_f32 v[94:95], v[94:95], s[98:99], s[98:99] op_sel:[0,0,1] op_sel_hi:[1,0,1]\n"
    "buffer_load_dwordx4 v247, s[44:47], s62 offen offset:3072 lds\n"
    "v_readlane_b32 s98, v7, 32\n"
    "v_readlane_b32 s99, v3, 32\n"
    "v_pk_fma_f32 v[96:97], v[96:97], s[96:97], s[96:97] op_sel:[0,0,1] op_sel_hi:[1,0,1]\n"
    "v_pk_fma_f32 v[98:99], v[98:99], s[96:97], s[96:97] op_sel:[0,0,1] op_sel_hi:[1,0,1]\n"
    "v_pk_fma_f32 v[100:101], v[100:101], s[96:97], s[96:97] op_sel:[0,0,1] op_sel_hi:[1,0,1]\n"
    "v_pk_fma_f32 v[102:103], v[102:103], s[96:97], s[96:97] op_sel:[0,0,1] op_sel_hi:[1,0,1]\n"
    "v_readlane_b32 s96, v4, 48\n"
    "v_readlane_b32 s97, v0, 48\n"
    "v_pk_fma_f32 v[104:105], v[104:105], s[98:99], s[98:99] op_sel:[0,0,1] op_sel_hi:[1,0,1]\n"
    "v_pk_fma_f32 v[106:107], v[106:107], s[98:99], s[98:99] op_sel:[0,0,1] op_sel_hi:[1,0,1]\n"
    "v_pk_fma_f32 v[108:109], v[108:109], s[98:99], s[98:99] op_sel:[0,0,1] op_sel_hi:[1,0,1]\n"
    "v_pk_fma_f32 v[110:111], v[110:111], s[98:99], s[98:99] op_sel:[0,0,1] op_sel_hi:[1,0,1]\n"
    "v_readlane_b32 s98, v5, 48\n"
    "v_readlane_b32 s99, v1, 48\n"
    "v_pk_fma_f32 v[112:113], v[112:113], s[96:97], s[96:97] op_sel:[0,0,1] op_sel_hi:[1,0,1]\n"
    "v_pk_fma_f32 v[114:115], v[114:115], s[96:97], s[96:97] op_sel:[0,0,1] op_sel_hi:[1,0,1]\n"
    "v_pk_fma_f32 v[116:117], v[116:117], s[96:97], s[96:97] op_sel:[0,0,1] op_sel_hi:[1,0,1]\n"
    "v_pk_fma_f32 v[118:119], v[118:119], s[96:97], s[96:97] op_sel:[0,0,1] op_sel_hi:[1,0,1]\n"
    "v_readlane_b32 s96, v6, 48\n"
    "v_readlane_b32 s97, v2, 48\n"
    "v_pk_fma_f32 v[120:121], v[120:121], s[98:99], s[98:99] op_sel:[0,0,1] op_sel_hi:[1,0,1]\n"
    "v_pk_fma_f32 v[122:123], v[122:123], s[98:99], s[98:99] op_sel:[0,0,1] op_sel_hi:[1,0,1]\n"
    "v_pk_fma_f32 v[124:125], v[124:125], s[98:99], s[98:99] op_sel:[0,0,1] op_sel_hi:[1,0,1]\n"
    "v_pk_fma_f32 v[126:127], v[126:127], s[98:99], s[98:99] op_sel:[0,0,1] op_sel_hi:[1,0,1]\n"
    "v_readlane_b32 s98, v7, 48\n"
    "v_readlane_b32 s99, v3, 48\n"
    "v_pk_fma_f32 v[128:129], v[128:129], s[96:97], s[96:97] op_sel:[0,0,1] op_sel_hi:[1,0,1]\n"
    "v_pk_fma_f32 v[130:131], v[130:131], s[96:97], s[96:97] op_sel:[0,0,1] op_sel_hi:[1,0,1]\n"
    "v_pk_fma_f32 v[132:133], v[132:133], s[96:97], s[96:97] op_sel:[0,0,1] op_sel_hi:[1,0,1]\n"
    "v_pk_fma_f32 v[134:135], v[134:135], s[96:97], s[96:97] op_sel:[0,0,1] op_sel_hi:[1,0,1]\n"
    "v_pk_fma_f32 v[136:137], v[136:137], s[98:99], s[98:99] op_sel:[0,0,1] op_sel_hi:[1,0,1]\n"
    "v_pk_fma_f32 v[138:139], v[138:139], s[98:99], s[98:99] op_sel:[0,0,1] op_sel_hi:[1,0,1]\n"
    "v_pk_fma_f32 v[140:141], v[140:141], s[98:99], s[98:99] op_sel:[0,0,1] op_sel_hi:[1,0,1]\n"
    "v_pk_fma_f32 v[142:143], v[142:143], s[98:99], s[98:99] op_sel:[0,0,1] op_sel_hi:[1,0,1]\n"
    "s_cmp_ge_u32 %[wv], 2\n"
    "s_cbranch_scc1 fb_Lnomodkv\n"
    "s_mul_i32 s61, s66, 458752\n"
    "s_mul_i32 s68, s67, 14336\n"
    "s_add_u32 s61, s61, s68\n"
    "s_lshr_b32 s68, s67, 2\n"
    "s_lshl_b32 s68, s68, 3\n"
    "s_add_u32 s68, s68, s66\n"
    "s_lshl_b32 s68, s68, 2\n"
    "s_and_b32 s95, s67, 3\n"
    "s_add_u32 s68, s68, s95\n"
    "s_mul_i32 s68, s68, 14336\n"
    "s_cmp_eq_u32 %[wv], 0\n"
    "s_cselect_b64 s[92:93], %[wo], %[wd]\n"
    "s_cselect_b32 s61, s61, s68\n"
    "s_cselect_b32 s68, -1, 1605632\n"
    "s_add_u32 s94, s61, 14336\n"
    "s_min_u32 s94, s94, s68\n"
    "s_and_b32 s93, s93, 0xffff\n"
    "s_mov_b32 s95, 0x20000\n"
    "s_add_u32 s68, s61, 8192\n"
    "buffer_load_dword v249, v248, s[92:95], s61 offen\n"
    "buffer_load_dword v249, v248, s[92:95], s68 offen\n"
    "s_cmp_eq_u32 %[wv], 0\n"
    "s_cselect_b64 s[92:93], %[wg], %[wu]\n"
    "s_and_b32 s93, s93, 0xffff\n"
    "s_mov_b32 s94, 1605632\n"
    "v_and_b32 v1, 15, v250\n"
    "v_min_u32 v1, 13, v1\n"
    "v_mul_u32_u24 v2, 3584, v1\n"
    "v_lshrrev_b32 v3, 4, v250\n"
    "v_lshl_add_u32 v2, v3, 10, v2\n"
    "s_mul_i32 s61, s67, 50176\n"
    "s_lshl_b32 s68, s66, 7\n"
    "s_add_u32 s61, s61, s68\n"
    "buffer_load_dword v249, v2, s[92:95], s61 offen\n"
    "s_branch fb_Lshared\n"
    "fb_Lnomodkv:\n"
    "s_mov_b32 s83, 0x40000\n"
    "s_cmp_eq_u32 %[wv], 3\n"
    "s_cselect_b32 s82, 1.0, 0\n"
    "s_waitcnt vmcnt(2)\n"
    "fb_Lpoll:\n"
    "v_and_b32 v1, v145, v147\n"
    "v_or_b32 v2, v145, v147\n"
    "v_bitop3_b32 v1, v1, v149, v151 bitop3:0x80\n"
    "v_or3_b32 v2, v2, v149, v151\n"
    "v_bitop3_b32 v1, v1, v153, v155 bitop3:0x80\n"
    "v_or3_b32 v2, v2, v153, v155\n"
    "v_bitop3_b32 v1, v1, v157, v159 bitop3:0x80\n"
    "v_or3_b32 v2, v2, v157, v159\n"
    "v_bitop3_b32 v1, v1, v161, v163 bitop3:0x80\n"
    "v_or3_b32 v2, v2, v161, v163\n"
    "v_bitop3_b32 v1, v1, v165, v167 bitop3:0x80\n"
    "v_or3_b32 v2, v2, v165, v167\n"
    "v_bitop3_b32 v1, v1, v169, v171 bitop3:0x80\n"
    "v_or3_b32 v2, v2, v169, v171\n"
    "v_bitop3_b32 v1, v1, v173, v175 bitop3:0x80\n"
    "v_or3_b32 v2, v2, v173, v175\n"
    "v_xor_b32 v1, v1, v229\n"
    "v_xor_b32 v2, v2, v229\n"
    "v_or_b32 v1, v1, v2\n"
    "v_cmp_ne_u32 vcc, 0, v1\n"
    "s_sub_u32 s83, s83, 1\n"
    "s_cbranch_scc1 fb_Lgot\n"
    "s_cbranch_vccz fb_Lgot\n"
    "buffer_load_dwordx4 v[144:147], v8, s[52:55], 0 offen offset:0 sc1\n"
    "buffer_load_dwordx4 v[148:151], v8, s[52:55], 0 offen offset:1024 sc1\n"
    "buffer_load_dwordx4 v[152:155], v8, s[52:55], 0 offen offset:2048 sc1\n"
    "buffer_load_dwordx4 v[156:159], v13, s[52:55], 0 offen offset:3072 sc1\n"
    "buffer_load_dwordx4 v[160:163], v8, s[52:55], s58 offen offset:0 sc1\n"
    "buffer_load_dwordx4 v[164:167], v8, s[52:55], s58 offen offset:1024 sc1\n"
    "buffer_load_dwordx4 v[168:171], v8, s[52:55], s58 offen offset:2048 sc1\n"
    "buffer_load_dwordx4 v[172:175], v13, s[52:55], s58 offen offset:3072 sc1\n"
    "s_waitcnt vmcnt(0)\n"
    "s_branch fb_Lpoll\n"
    "fb_Lgot:\n"
    "v_add_f32 v176, v144, v160\n"
    "v_add_f32 v177, v146, v162\n"
    "v_add_f32 v178, v148, v164\n"
    "v_add_f32 v179, v150, v166\n"
    "v_add_f32 v180, v152, v168\n"
    "v_add_f32 v181, v154, v170\n"
    "v_add_f32 v182, v156, v172\n"
    "v_add_f32 v183, v158, v174\n"
    "v_cvt_pk_bf16_f32 v176, 0, v176\n"
    "v_cvt_pk_bf16_f32 v177, 0, v177\n"
    "v_cvt_pk_bf16_f32 v178, 0, v178\n"
    "v_cvt_pk_bf16_f32 v179, 0, v179\n"
    "v_cvt_pk_bf16_f32 v180, 0, v180\n"
    "v_cvt_pk_bf16_f32 v181, 0, v181\n"
    "v_cvt_pk_bf16_f32 v182, 0, v182\n"
    "v_cvt_pk_bf16_f32 v183, 0, v183\n"
    "v_add_f32 v176, s82, v176\n"
    "v_add_f32 v177, s82, v177\n"
    "v_add_f32 v178, s82, v178\n"
    "v_add_f32 v179, s82, v179\n"
    "v_add_f32 v180, s82, v180\n"
    "v_add_f32 v181, s82, v181\n"
    "v_add_f32 v182, s82, v182\n"
    "v_add_f32 v183, s82, v183\n"
    "v_cvt_pk_bf16_f32 v176, 0, v176\n"
    "v_cvt_pk_bf16_f32 v177, 0, v177\n"
    "v_cvt_pk_bf16_f32 v178, 0, v178\n"
    "v_cvt_pk_bf16_f32 v179, 0, v179\n"
    "v_cvt_pk_bf16_f32 v180, 0, v180\n"
    "v_cvt_pk_bf16_f32 v181, 0, v181\n"
    "v_cvt_pk_bf16_f32 v182, 0, v182\n"
    "v_cvt_pk_bf16_f32 v183, 0, v183\n"
    "v_mul_f32 v176, s71, v176\n"
    "v_mul_f32 v177, s71, v177\n"
    "v_mul_f32 v178, s71, v178\n"
    "v_mul_f32 v179, s71, v179\n"
    "v_mul_f32 v180, s71, v180\n"
    "v_mul_f32 v181, s71, v181\n"
    "v_mul_f32 v182, s71, v182\n"
    "v_mul_f32 v183, s71, v183\n"
    "ds_write_b64 v12, v[176:177] offset:0\n"
    "ds_write_b64 v12, v[178:179] offset:512\n"
    "ds_write_b64 v12, v[180:181] offset:1024\n"
    "ds_write_b64 v12, v[182:183] offset:1536\n"
    "s_waitcnt lgkmcnt(0)\n"
    "fb_Lshared:\n"
    "s_barrier\n"
    "ds_read_b128 v[176:179], v9 offset:0\n"
    "ds_read_b128 v[180:183], v9 offset:16\n"
    "ds_read_b128 v[184:187], v9 offset:2048\n"
    "ds_read_b128 v[188:191], v9 offset:2064\n"
    "s_mov_b32 exec_hi, 0x00ffffff\n"
    "s_waitcnt lgkmcnt(0)\n"
    "v_pk_fma_f32 v[16:17], v[16:17], v[176:177], v[184:185]\n"
    "v_pk_fma_f32 v[18:19], v[18:19], v[178:179], v[186:187]\n"
    "v_pk_fma_f32 v[20:21], v[20:21], v[180:181], v[188:189]\n"
    "v_pk_fma_f32 v[22:23], v[22:23], v[182:183], v[190:191]\n"
    "v_cvt_pk_bf16_f32 v16, v16, v17\n"
    "v_cvt_pk_bf16_f32 v17, v18, v19\n"
    "v_cvt_pk_bf16_f32 v18, v20, v21\n"
    "v_cvt_pk_bf16_f32 v19, v22, v23\n"
    "ds_write_b64 v240, v[16:17] offset:0\n"
    "ds_write_b64 v240, v[18:19] offset:16\n"
    "v_pk_fma_f32 v[24:25], v[24:25], v[176:177], v[184:185]\n"
    "v_pk_fma_f32 v[26:27], v[26:27], v[178:179], v[186:187]\n"
    "v_pk_fma_f32 v[28:29], v[28:29], v[180:181], v[188:189]\n"
    "v_pk_fma_f32 v[30:31], v[30:31], v[182:183], v[190:191]\n"
    "v_cvt_pk_bf16_f32 v24, v24, v25\n"
    "v_cvt_pk_bf16_f32 v25, v26, v27\n"
    "v_cvt_pk_bf16_f32 v26, v28, v29\n"
    "v_cvt_pk_bf16_f32 v27, v30, v31\n"
    "ds_write_b64 v240, v[24:25] offset:928\n"
    "ds_write_b64 v240, v[26:27] offset:944\n"
    "v_pk_fma_f32 v[32:33], v[32:33], v[176:177], v[184:185]\n"
    "v_pk_fma_f32 v[34:35], v[34:35], v[178:179], v[186:187]\n"
    "v_pk_fma_f32 v[36:37], v[36:37], v[180:181], v[188:189]\n"
    "v_pk_fma_f32 v[38:39], v[38:39], v[182:183], v[190:191]\n"
    "v_cvt_pk_bf16_f32 v32, v32, v33\n"
    "v_cvt_pk_bf16_f32 v33, v34, v35\n"
    "v_cvt_pk_bf16_f32 v34, v36, v37\n"
    "v_cvt_pk_bf16_f32 v35, v38, v39\n"
    "ds_write_b64 v240, v[32:33] offset:1856\n"
    "ds_write_b64 v240, v[34:35] offset:1872\n"
    "v_pk_fma_f32 v[40:41], v[40:41], v[176:177], v[184:185]\n"
    "v_pk_fma_f32 v[42:43], v[42:43], v[178:179], v[186:187]\n"
    "v_pk_fma_f32 v[44:45], v[44:45], v[180:181], v[188:189]\n"
    "v_pk_fma_f32 v[46:47], v[46:47], v[182:183], v[190:191]\n"
    "v_cvt_pk_bf16_f32 v40, v40, v41\n"
    "v_cvt_pk_bf16_f32 v41, v42, v43\n"
    "v_cvt_pk_bf16_f32 v42, v44, v45\n"
    "v_cvt_pk_bf16_f32 v43, v46, v47\n"
    "ds_write_b64 v240, v[40:41] offset:2784\n"
    "ds_write_b64 v240, v[42:43] offset:2800\n"
    "v_pk_fma_f32 v[48:49], v[48:49], v[176:177], v[184:185]\n"
    "v_pk_fma_f32 v[50:51], v[50:51], v[178:179], v[186:187]\n"
    "v_pk_fma_f32 v[52:53], v[52:53], v[180:181], v[188:189]\n"
    "v_pk_fma_f32 v[54:55], v[54:55], v[182:183], v[190:191]\n"
    "v_cvt_pk_bf16_f32 v48, v48, v49\n"
    "v_cvt_pk_bf16_f32 v49, v50, v51\n"
    "v_cvt_pk_bf16_f32 v50, v52, v53\n"
    "v_cvt_pk_bf16_f32 v51, v54, v55\n"
    "ds_write_b64 v240, v[48:49] offset:3712\n"
    "ds_write_b64 v240, v[50:51] offset:3728\n"
    "v_pk_fma_f32 v[56:57], v[56:57], v[176:177], v[184:185]\n"
    "v_pk_fma_f32 v[58:59], v[58:59], v[178:179], v[186:187]\n"
    "v_pk_fma_f32 v[60:61], v[60:61], v[180:181], v[188:189]\n"
    "v_pk_fma_f32 v[62:63], v[62:63], v[182:183], v[190:191]\n"
    "v_cvt_pk_bf16_f32 v56, v56, v57\n"
    "v_cvt_pk_bf16_f32 v57, v58, v59\n"
    "v_cvt_pk_bf16_f32 v58, v60, v61\n"
    "v_cvt_pk_bf16_f32 v59, v62, v63\n"
    "ds_write_b64 v240, v[56:57] offset:4640\n"
    "ds_write_b64 v240, v[58:59] offset:4656\n"
    "v_pk_fma_f32 v[64:65], v[64:65], v[176:177], v[184:185]\n"
    "v_pk_fma_f32 v[66:67], v[66:67], v[178:179], v[186:187]\n"
    "v_pk_fma_f32 v[68:69], v[68:69], v[180:181], v[188:189]\n"
    "v_pk_fma_f32 v[70:71], v[70:71], v[182:183], v[190:191]\n"
    "v_cvt_pk_bf16_f32 v64, v64, v65\n"
    "v_cvt_pk_bf16_f32 v65, v66, v67\n"
    "v_cvt_pk_bf16_f32 v66, v68, v69\n"
    "v_cvt_pk_bf16_f32 v67, v70, v71\n"
    "ds_write_b64 v240, v[64:65] offset:5568\n"
    "ds_write_b64 v240, v[66:67] offset:5584\n"
    "v_pk_fma_f32 v[72:73], v[72:73], v[176:177], v[184:185]\n"
    "v_pk_fma_f32 v[74:75], v[74:75], v[178:179], v[186:187]\n"
    "v_pk_fma_f32 v[76:77], v[76:77], v[180:181], v[188:189]\n"
    "v_pk_fma_f32 v[78:79], v[78:79], v[182:183], v[190:191]\n"
    "v_cvt_pk_bf16_f32 v72, v72, v73\n"
    "v_cvt_pk_bf16_f32 v73, v74, v75\n"
    "v_cvt_pk_bf16_f32 v74, v76, v77\n"
    "v_cvt_pk_bf16_f32 v75, v78, v79\n"
    "ds_write_b64 v240, v[72:73] offset:6496\n"
    "ds_write_b64 v240, v[74:75] offset:6512\n"
    "v_pk_fma_f32 v[80:81], v[80:81], v[176:177], v[184:185]\n"
    "v_pk_fma_f32 v[82:83], v[82:83], v[178:179], v[186:187]\n"
    "v_pk_fma_f32 v[84:85], v[84:85], v[180:181], v[188:189]\n"
    "v_pk_fma_f32 v[86:87], v[86:87], v[182:183], v[190:191]\n"
    "v_cvt_pk_bf16_f32 v80, v80, v81\n"
    "v_cvt_pk_bf16_f32 v81, v82, v83\n"
    "v_cvt_pk_bf16_f32 v82, v84, v85\n"
    "v_cvt_pk_bf16_f32 v83, v86, v87\n"
    "ds_write_b64 v240, v[80:81] offset:7424\n"
    "ds_write_b64 v240, v[82:83] offset:7440\n"
    "v_pk_fma_f32 v[88:89], v[88:89], v[176:177], v[184:185]\n"
    "v_pk_fma_f32 v[90:91], v[90:91], v[178:179], v[186:187]\n"
    "v_pk_fma_f32 v[92:93], v[92:93], v[180:181], v[188:189]\n"
    "v_pk_fma_f32 v[94:95], v[94:95], v[182:183], v[190:191]\n"
    "v_cvt_pk_bf16_f32 v88, v88, v89\n"
    "v_cvt_pk_bf16_f32 v89, v90, v91\n"
    "v_cvt_pk_bf16_f32 v90, v92, v93\n"
    "v_cvt_pk_bf16_f32 v91, v94, v95\n"
    "ds_write_b64 v240, v[88:89] offset:8352\n"
    "ds_write_b64 v240, v[90:91] offset:8368\n"
    "v_pk_fma_f32 v[96:97], v[96:97], v[176:177], v[184:185]\n"
    "v_pk_fma_f32 v[98:99], v[98:99], v[178:179], v[186:187]\n"
    "v_pk_fma_f32 v[100:101], v[100:101], v[180:181], v[188:189]\n"
    "v_pk_fma_f32 v[102:103], v[102:103], v[182:183], v[190:191]\n"
    "v_cvt_pk_bf16_f32 v96, v96, v97\n"
    "v_cvt_pk_bf16_f32 v97, v98, v99\n"
    "v_cvt_pk_bf16_f32 v98, v100, v101\n"
    "v_cvt_pk_bf16_f32 v99, v102, v103\n"
    "ds_write_b64 v240, v[96:97] offset:9280\n"
    "ds_write_b64 v240, v[98:99] offset:9296\n"
    "v_pk_fma_f32 v[104:105], v[104:105], v[176:177], v[184:185]\n"
    "v_pk_fma_f32 v[106:107], v[106:107], v[178:179], v[186:187]\n"
    "v_pk_fma_f32 v[108:109], v[108:109], v[180:181], v[188:189]\n"
    "v_pk_fma_f32 v[110:111], v[110:111], v[182:183], v[190:191]\n"
    "v_cvt_pk_bf16_f32 v104, v104, v105\n"
    "v_cvt_pk_bf16_f32 v105, v106, v107\n"
    "v_cvt_pk_bf16_f32 v106, v108, v109\n"
    "v_cvt_pk_bf16_f32 v107, v110, v111\n"
    "ds_write_b64 v240, v[104:105] offset:10208\n"
    "ds_write_b64 v240, v[106:107] offset:10224\n"
    "v_pk_fma_f32 v[112:113], v[112:113], v[176:177], v[184:185]\n"
    "v_pk_fma_f32 v[114:115], v[114:115], v[178:179], v[186:187]\n"
    "v_pk_fma_f32 v[116:117], v[116:117], v[180:181], v[188:189]\n"
    "v_pk_fma_f32 v[118:119], v[118:119], v[182:183], v[190:191]\n"
    "v_cvt_pk_bf16_f32 v112, v112, v113\n"
    "v_cvt_pk_bf16_f32 v113, v114, v115\n"
    "v_cvt_pk_bf16_f32 v114, v116, v117\n"
    "v_cvt_pk_bf16_f32 v115, v118, v119\n"
    "ds_write_b64 v240, v[112:113] offset:11136\n"
    "ds_write_b64 v240, v[114:115] offset:11152\n"
    "v_pk_fma_f32 v[120:121], v[120:121], v[176:177], v[184:185]\n"
    "v_pk_fma_f32 v[122:123], v[122:123], v[178:179], v[186:187]\n"
    "v_pk_fma_f32 v[124:125], v[124:125], v[180:181], v[188:189]\n"
    "v_pk_fma_f32 v[126:127], v[126:127], v[182:183], v[190:191]\n"
    "v_cvt_pk_bf16_f32 v120, v120, v121\n"
    "v_cvt_pk_bf16_f32 v121, v122, v123\n"
    "v_cvt_pk_bf16_f32 v122, v124, v125\n"
    "v_cvt_pk_bf16_f32 v123, v126, v127\n"
    "ds_write_b64 v240, v[120:121] offset:12064\n"
    "ds_write_b64 v240, v[122:123] offset:12080\n"
    "v_pk_fma_f32 v[128:129], v[128:129], v[176:177], v[184:185]\n"
    "v_pk_fma_f32 v[130:131], v[130:131], v[178:179], v[186:187]\n"
    "v_pk_fma_f32 v[132:133], v[132:133], v[180:181], v[188:189]\n"
    "v_pk_fma_f32 v[134:135], v[134:135], v[182:183], v[190:191]\n"
    "v_cvt_pk_bf16_f32 v128, v128, v129\n"
    "v_cvt_pk_bf16_f32 v129, v130, v131\n"
    "v_cvt_pk_bf16_f32 v130, v132, v133\n"
    "v_cvt_pk_bf16_f32 v131, v134, v135\n"
    "ds_write_b64 v240, v[128:129] offset:12992\n"
    "ds_write_b64 v240, v[130:131] offset:13008\n"
    "v_pk_fma_f32 v[136:137], v[136:137], v[176:177], v[184:185]\n"
    "v_pk_fma_f32 v[138:139], v[138:139], v[178:179], v[186:187]\n"
    "v_pk_fma_f32 v[140:141], v[140:141], v[180:181], v[188:189]\n"
    "v_pk_fma_f32 v[142:143], v[142:143], v[182:183], v[190:191]\n"
    "v_cvt_pk_bf16_f32 v136, v136, v137\n"
    "v_cvt_pk_bf16_f32 v137, v138, v139\n"
    "v_cvt_pk_bf16_f32 v138, v140, v141\n"
    "v_cvt_pk_bf16_f32 v139, v142, v143\n"
    "ds_write_b64 v240, v[136:137] offset:13920\n"
    "ds_write_b64 v240, v[138:139] offset:13936\n"
    "s_mov_b32 exec_hi, -1\n"
    "s_waitcnt lgkmcnt(0)\n"
    "s_waitcnt vmcnt(8)\n"
    "s_barrier\n"
    "ds_read_b64_tr_b16 v[48:49], v232 offset:32768\n"
    "ds_read_b64_tr_b16 v[50:51], v232 offset:34816\n"
    "ds_read_b64_tr_b16 v[52:53], v232 offset:36864\n"
    "ds_read_b64_tr_b16 v[54:55], v232 offset:38912\n"
    "ds_read_b64_tr_b16 v[56:57], v232 offset:40960\n"
    "ds_read_b64_tr_b16 v[58:59], v232 offset:43008\n"
    "ds_read_b64_tr_b16 v[60:61], v232 offset:45056\n"
    "ds_read_b64_tr_b16 v[62:63], v232 offset:47104\n"
    "ds_read_b128 a[0:3], v241 offset:0\n"
    "ds_read_b128 a[16:19], v241 offset:64\n"
    "ds_read_b128 a[32:35], v241 offset:128\n"
    "ds_read_b128 a[48:51], v241 offset:192\n"
    "ds_read_b128 a[4:7], v241 offset:14848\n"
    "ds_read_b128 a[20:23], v241 offset:14912\n"
    "ds_read_b128 a[36:39], v241 offset:14976\n"
    "ds_read_b128 a[52:55], v241 offset:15040\n"
    "ds_read_b128 a[8:11], v241 offset:29696\n"
    "ds_read_b128 a[24:27], v241 offset:29760\n"
    "ds_read_b128 a[40:43], v241 offset:29824\n"
    "ds_read_b128 a[56:59], v241 offset:29888\n"
    "ds_read_b128 a[12:15], v241 offset:44544\n"
    "ds_read_b128 a[28:31], v241 offset:44608\n"
    "ds_read_b128 a[44:47], v241 offset:44672\n"
    "ds_read_b128 a[60:63], v241 offset:44736\n"
    "ds_read_b64_tr_b16 v[64:65], v232 offset:49152\n"
    "ds_read_b64_tr_b16 v[66:67], v232 offset:51200\n"
    "ds_read_b64_tr_b16 v[68:69], v232 offset:53248\n"
    "ds_read_b64_tr_b16 v[70:71], v232 offset:55296\n"
    "ds_read_b64_tr_b16 v[72:73], v232 offset:57344\n"
    "ds_read_b64_tr_b16 v[74:75], v232 offset:59392\n"
    "ds_read_b64_tr_b16 v[76:77], v232 offset:61440\n"
    "ds_read_b64_tr_b16 v[78:79], v232 offset:63488\n"
    "s_mul_i32 s68, s66, 2228864\n"
    "s_add_u32 s68, s68, 8388608\n"
    "s_mov_b64 s[76:77], %[ws]\n"
    "s_add_u32 s76, s76, s68\n"
    "s_addc_u32 s77, s77, 0\n"
    "s_and_b32 s77, s77, 0xffff\n"
    "s_mov_b32 s78, 2228224\n"
    "s_mov_b32 s79, 0x20000\n"
    "s_waitcnt lgkmcnt(15)\n"
    "v_mfma_f32_16x16x32_bf16 v[0:3], v[48:51], a[0:3], 0\n"
    "s_add_u32 m0, s59, 16384\n"
    "ds_read_b64_tr_b16 v[80:81], v233 offset:0\n"
    "s_add_u32 s62, s63, 2097664\n"
    "ds_read_b64_tr_b16 v[82:83], v233 offset:2048\n"
    "v_mfma_f32_16x16x32_bf16 v[0:3], v[52:55], a[16:19], v[0:3]\n"
    "ds_read_b64_tr_b16 v[84:85], v233 offset:4096\n"
    "buffer_load_dwordx4 v244, s[44:47], s62 offen offset:0 lds\n"
    "v_mfma_f32_16x16x32_bf16 v[0:3], v[56:59], a[32:35], v[0:3]\n"
    "ds_read_b64_tr_b16 v[86:87], v233 offset:6144\n"
    "ds_read_b64_tr_b16 v[88:89], v233 offset:8192\n"
    "v_mfma_f32_16x16x32_bf16 v[0:3], v[60:63], a[48:51], v[0:3]\n"
    "ds_read_b64_tr_b16 v[90:91], v233 offset:10240\n"
    "ds_read_b64_tr_b16 v[92:93], v233 offset:12288\n"
    "v_mfma_f32_16x16x32_bf16 v[4:7], v[48:51], a[4:7], 0\n"
    "ds_read_b64_tr_b16 v[94:95], v233 offset:14336\n"
    "ds_read_b128 a[64:67], v241 offset:256\n"
    "v_mfma_f32_16x16x32_bf16 v[4:7], v[52:55], a[20:23], v[4:7]\n"
    "ds_read_b128 a[80:83], v241 offset:320\n"
    "buffer_load_dwordx4 v245, s[44:47], s62 offen offset:1024 lds\n"
    "v_mfma_f32_16x16x32_bf16 v[4:7], v[56:59], a[36:39], v[4:7]\n"
    "ds_read_b128 a[96:99], v241 offset:384\n"
    "ds_read_b128 a[112:115], v241 offset:448\n"
    "v_mfma_f32_16x16x32_bf16 v[4:7], v[60:63], a[52:55], v[4:7]\n"
    "ds_read_b128 a[68:71], v241 offset:15104\n"
    "s_waitcnt lgkmcnt(15)\n"
    "v_mfma_f32_16x16x32_bf16 v[8:11], v[48:51], a[8:11], 0\n"
    "ds_read_b128 a[84:87], v241 offset:15168\n"
    "v_mfma_f32_16x16x32_bf16 v[8:11], v[52:55], a[24:27], v[8:11]\n"
    "buffer_load_dwordx4 v246, s[44:47], s62 offen offset:2048 lds\n"
    "v_mfma_f32_16x16x32_bf16 v[8:11], v[56:59], a[40:43], v[8:11]\n"
    "ds_read_b128 a[100:103], v241 offset:15232\n"
    "v_mfma_f32_16x16x32_bf16 v[8:11], v[60:63], a[56:59], v[8:11]\n"
    "ds_read_b128 a[116:119], v241 offset:15296\n"
    "v_mfma_f32_16x16x32_bf16 v[12:15], v[48:51], a[12:15], 0\n"
    "v_mfma_f32_16x16x32_bf16 v[12:15], v[52:55], a[28:31], v[12:15]\n"
    "buffer_load_dwordx4 v247, s[44:47], s62 offen offset:3072 lds\n"
    "v_mfma_f32_16x16x32_bf16 v[12:15], v[56:59], a[44:47], v[12:15]\n"
    "s_waitcnt vmcnt(8)\n"
    "v_mfma_f32_16x16x32_bf16 v[12:15], v[60:63], a[60:63], v[12:15]\n"
    "s_waitcnt lgkmcnt(0)\n"
    "s_barrier\n"
    "v_mfma_f32_16x16x32_bf16 v[16:19], v[64:67], a[0:3], 0\n"
    "s_add_u32 m0, s59, 32768\n"
    "ds_read_b64_tr_b16 v[96:97], v233 offset:16384\n"
    "s_add_u32 s62, s70, 3145728\n"
    "ds_read_b64_tr_b16 v[98:99], v233 offset:18432\n"
    "v_mfma_f32_16x16x32_bf16 v[16:19], v[68:71], a[16:19], v[16:19]\n"
    "ds_read_b64_tr_b16 v[100:101], v233 offset:20480\n"
    "buffer_load_dwordx4 v244, s[44:47], s62 offen offset:0 lds\n"
    "v_mfma_f32_16x16x32_bf16 v[16:19], v[72:75], a[32:35], v[16:19]\n"
    "ds_read_b64_tr_b16 v[102:103], v233 offset:22528\n"
    "ds_read_b64_tr_b16 v[104:105], v233 offset:24576\n"
    "v_mfma_f32_16x16x32_bf16 v[16:19], v[76:79], a[48:51], v[16:19]\n"
    "ds_read_b64_tr_b16 v[106:107], v233 offset:26624\n"
    "buffer_load_dwordx4 v245, s[44:47], s62 offen offset:1024 lds\n"
    "v_mfma_f32_16x16x32_bf16 v[20:23], v[64:67], a[4:7], 0\n"
    "ds_read_b64_tr_b16 v[108:109], v233 offset:28672\n"
    "ds_read_b64_tr_b16 v[110:111], v233 offset:30720\n"
    "v_mfma_f32_16x16x32_bf16 v[20:23], v[68:71], a[20:23], v[20:23]\n"
    "ds_read_b128 a[72:75], v241 offset:29952\n"
    "buffer_load_dwordx4 v246, s[44:47], s62 offen offset:2048 lds\n"
    "v_mfma_f32_16x16x32_bf16 v[20:23], v[72:75], a[36:39], v[20:23]\n"
    "ds_read_b128 a[88:91], v241 offset:30016\n"
    "ds_read_b128 a[104:107], v241 offset:30080\n"
    "v_mfma_f32_16x16x32_bf16 v[20:23], v[76:79], a[52:55], v[20:23]\n"
    "ds_read_b128 a[120:123], v241 offset:30144\n"
    "buffer_load_dwordx4 v247, s[44:47], s62 offen offset:3072 lds\n"
    "v_mfma_f32_16x16x32_bf16 v[24:27], v[64:67], a[8:11], 0\n"
    "s_add_u32 m0, s59, 49152\n"
    "ds_read_b128 a[76:79], v241 offset:44800\n"
    "s_add_u32 s62, s63, 128\n"
    "ds_read_b128 a[92:95], v241 offset:44864\n"
    "v_mfma_f32_16x16x32_bf16 v[24:27], v[68:71], a[24:27], v[24:27]\n"
    "ds_read_b128 a[108:111], v241 offset:44928\n"
    "buffer_load_dwordx4 v244, s[44:47], s62 offen offset:0 lds\n"
    "v_mfma_f32_16x16x32_bf16 v[24:27], v[72:75], a[40:43], v[24:27]\n"
    "ds_read_b128 a[124:127], v241 offset:44992\n"
    "v_mfma_f32_16x16x32_bf16 v[24:27], v[76:79], a[56:59], v[24:27]\n"
    "buffer_load_dwordx4 v245, s[44:47], s62 offen offset:1024 lds\n"
    "v_mfma_f32_16x16x32_bf16 v[28:31], v[64:67], a[12:15], 0\n"
    "v_mfma_f32_16x16x32_bf16 v[28:31], v[68:71], a[28:31], v[28:31]\n"
    "buffer_load_dwordx4 v246, s[44:47], s62 offen offset:2048 lds\n"
    "v_mfma_f32_16x16x32_bf16 v[28:31], v[72:75], a[44:47], v[28:31]\n"
    "s_waitcnt vmcnt(11)\n"
    "buffer_load_dwordx4 v247, s[44:47], s62 offen offset:3072 lds\n"
    "v_mfma_f32_16x16x32_bf16 v[28:31], v[76:79], a[60:63], v[28:31]\n"
    "s_waitcnt lgkmcnt(0)\n"
    "s_barrier\n"
    "v_mfma_f32_16x16x32_bf16 v[0:3], v[80:83], a[64:67], v[0:3]\n"
    "s_add_u32 m0, s59, 65536\n"
    "ds_read_b64_tr_b16 v[48:49], v232 offset:0\n"
    "s_add_u32 s62, s63, 1048704\n"
    "ds_read_b64_tr_b16 v[50:51], v232 offset:2048\n"
    "v_mfma_f32_16x16x32_bf16 v[0:3], v[84:87], a[80:83], v[0:3]\n"
    "ds_read_b64_tr_b16 v[52:53], v232 offset:4096\n"
    "buffer_load_dwordx4 v244, s[44:47], s62 offen offset:0 lds\n"
    "v_mfma_f32_16x16x32_bf16 v[0:3], v[88:91], a[96:99], v[0:3]\n"
    "ds_read_b64_tr_b16 v[54:55], v232 offset:6144\n"
    "ds_read_b64_tr_b16 v[56:57], v232 offset:8192\n"
    "v_mfma_f32_16x16x32_bf16 v[0:3], v[92:95], a[112:115], v[0:3]\n"
    "ds_read_b64_tr_b16 v[58:59], v232 offset:10240\n"
    "ds_read_b64_tr_b16 v[60:61], v232 offset:12288\n"
    "v_mfma_f32_16x16x32_bf16 v[4:7], v[80:83], a[68:71], v[4:7]\n"
    "ds_read_b64_tr_b16 v[62:63], v232 offset:14336\n"
    "ds_read_b128 a[192:195], v241 offset:512\n"
    "v_mfma_f32_16x16x32_bf16 v[4:7], v[84:87], a[84:87], v[4:7]\n"
    "ds_read_b128 a[208:211], v241 offset:576\n"
    "buffer_load_dwordx4 v245, s[44:47], s62 offen offset:1024 lds\n"
    "v_mfma_f32_16x16x32_bf16 v[4:7], v[88:91], a[100:103], v[4:7]\n"
    "ds_read_b128 a[224:227], v241 offset:640\n"
    "ds_read_b128 a[240:243], v241 offset:704\n"
    "v_mfma_f32_16x16x32_bf16 v[4:7], v[92:95], a[116:119], v[4:7]\n"
    "ds_read_b128 a[196:199], v241 offset:15360\n"
    "v_mfma_f32_16x16x32_bf16 v[8:11], v[80:83], a[72:75], v[8:11]\n"
    "ds_read_b128 a[212:215], v241 offset:15424\n"
    "v_mfma_f32_16x16x32_bf16 v[8:11], v[84:87], a[88:91], v[8:11]\n"
    "buffer_load_dwordx4 v246, s[44:47], s62 offen offset:2048 lds\n"
    "v_mfma_f32_16x16x32_bf16 v[8:11], v[88:91], a[104:107], v[8:11]\n"
    "ds_read_b128 a[228:231], v241 offset:15488\n"
    "v_mfma_f32_16x16x32_bf16 v[8:11], v[92:95], a[120:123], v[8:11]\n"
    "ds_read_b128 a[244:247], v241 offset:15552\n"
    "v_mfma_f32_16x16x32_bf16 v[12:15], v[80:83], a[76:79], v[12:15]\n"
    "v_mfma_f32_16x16x32_bf16 v[12:15], v[84:87], a[92:95], v[12:15]\n"
    "buffer_load_dwordx4 v247, s[44:47], s62 offen offset:3072 lds\n"
    "v_mfma_f32_16x16x32_bf16 v[12:15], v[88:91], a[108:111], v[12:15]\n"
    "s_waitcnt vmcnt(12)\n"
    "v_mfma_f32_16x16x32_bf16 v[12:15], v[92:95], a[124:127], v[12:15]\n"
    "s_waitcnt lgkmcnt(0)\n"
    "s_barrier\n"
    "v_mfma_f32_16x16x32_bf16 v[16:19], v[96:99], a[64:67], v[16:19]\n"
    "s_add_u32 m0, s59, 81920\n"
    "ds_read_b64_tr_b16 v[64:65], v232 offset:16384\n"
    "s_add_u32 s62, s63, 2097280\n"
    "ds_read_b64_tr_b16 v[66:67], v232 offset:18432\n"
    "v_mfma_f32_16x16x32_bf16 v[16:19], v[100:103], a[80:83], v[16:19]\n"
    "ds_read_b64_tr_b16 v[68:69], v232 offset:20480\n"
    "buffer_load_dwordx4 v244, s[44:47], s62 offen offset:0 lds\n"
    "v_mfma_f32_16x16x32_bf16 v[16:19], v[104:107], a[96:99], v[16:19]\n"
    "ds_read_b64_tr_b16 v[70:71], v232 offset:22528\n"
    "ds_read_b64_tr_b16 v[72:73], v232 offset:24576\n"
    "v_mfma_f32_16x16x32_bf16 v[16:19], v[108:111], a[112:115], v[16:19]\n"
    "ds_read_b64_tr_b16 v[74:75], v232 offset:26624\n"
    "ds_read_b64_tr_b16 v[76:77], v232 offset:28672\n"
    "v_mfma_f32_16x16x32_bf16 v[20:23], v[96:99], a[68:71], v[20:23]\n"
    "ds_read_b64_tr_b16 v[78:79], v232 offset:30720\n"
    "ds_read_b128 a[200:203], v241 offset:30208\n"
    "v_mfma_f32_16x16x32_bf16 v[20:23], v[100:103], a[84:87], v[20:23]\n"
    "ds_read_b128 a[216:219], v241 offset:30272\n"
    "buffer_load_dwordx4 v245, s[44:47], s62 offen offset:1024 lds\n"
    "v_mfma_f32_16x16x32_bf16 v[20:23], v[104:107], a[100:103], v[20:23]\n"
    "ds_read_b128 a[232:235], v241 offset:30336\n"
    "ds_read_b128 a[248:251], v241 offset:30400\n"
    "v_mfma_f32_16x16x32_bf16 v[20:23], v[108:111], a[116:119], v[20:23]\n"
    "ds_read_b128 a[204:207], v241 offset:45056\n"
    "v_mfma_f32_16x16x32_bf16 v[24:27], v[96:99], a[72:75], v[24:27]\n"
    "ds_read_b128 a[220:223], v241 offset:45120\n"
    "v_mfma_f32_16x16x32_bf16 v[24:27], v[100:103], a[88:91], v[24:27]\n"
    "buffer_load_dwordx4 v246, s[44:47], s62 offen offset:2048 lds\n"
    "v_mfma_f32_16x16x32_bf16 v[24:27], v[104:107], a[104:107], v[24:27]\n"
    "ds_read_b128 a[236:239], v241 offset:45184\n"
    "v_mfma_f32_16x16x32_bf16 v[24:27], v[108:111], a[120:123], v[24:27]\n"
    "ds_read_b128 a[252:255], v241 offset:45248\n"
    "v_mfma_f32_16x16x32_bf16 v[28:31], v[96:99], a[76:79], v[28:31]\n"
    "v_mfma_f32_16x16x32_bf16 v[28:31], v[100:103], a[92:95], v[28:31]\n"
    "buffer_load_dwordx4 v247, s[44:47], s62 offen offset:3072 lds\n"
    "v_mfma_f32_16x16x32_bf16 v[28:31], v[104:107], a[108:111], v[28:31]\n"
    "s_waitcnt vmcnt(12)\n"
    "v_mfma_f32_16x16x32_bf16 v[28:31], v[108:111], a[124:127], v[28:31]\n"
    "s_waitcnt lgkmcnt(0)\n"
    "s_barrier\n"
    "v_mfma_f32_16x16x32_bf16 v[0:3], v[48:51], a[192:195], v[0:3]\n"
    "s_add_u32 m0, s59, 0\n"
    "ds_read_b64_tr_b16 v[80:81], v232 offset:32768\n"
    "s_add_u32 s62, s70, 3145856\n"
    "ds_read_b64_tr_b16 v[82:83], v232 offset:34816\n"
    "v_mfma_f32_16x16x32_bf16 v[0:3], v[52:55], a[208:211], v[0:3]\n"
    "ds_read_b64_tr_b16 v[84:85], v232 offset:36864\n"
    "buffer_load_dwordx4 v244, s[44:47], s62 offen offset:0 lds\n"
    "v_mfma_f32_16x16x32_bf16 v[0:3], v[56:59], a[224:227], v[0:3]\n"
    "ds_read_b64_tr_b16 v[86:87], v232 offset:38912\n"
    "ds_read_b64_tr_b16 v[88:89], v232 offset:40960\n"
    "v_mfma_f32_16x16x32_bf16 v[0:3], v[60:63], a[240:243], v[0:3]\n"
    "ds_read_b64_tr_b16 v[90:91], v232 offset:43008\n"
    "ds_read_b64_tr_b16 v[92:93], v232 offset:45056\n"
    "v_mfma_f32_16x16x32_bf16 v[4:7], v[48:51], a[196:199], v[4:7]\n"
    "ds_read_b64_tr_b16 v[94:95], v232 offset:47104\n"
    "ds_read_b128 v[112:115], v241 offset:768\n"
    "v_mfma_f32_16x16x32_bf16 v[4:7], v[52:55], a[212:215], v[4:7]\n"
    "ds_read_b128 v[128:131], v241 offset:832\n"
    "buffer_load_dwordx4 v245, s[44:47], s62 offen offset:1024 lds\n"
    "v_mfma_f32_16x16x32_bf16 v[4:7], v[56:59], a[228:231], v[4:7]\n"
    "ds_read_b128 v[116:119], v241 offset:15616\n"
    "ds_read_b128 v[132:135], v241 offset:15680\n"
    "v_mfma_f32_16x16x32_bf16 v[4:7], v[60:63], a[244:247], v[4:7]\n"
    "ds_read_b128 v[120:123], v241 offset:30464\n"
    "v_mfma_f32_16x16x32_bf16 v[8:11], v[48:51], a[200:203], v[8:11]\n"
    "ds_read_b128 v[136:139], v241 offset:30528\n"
    "v_mfma_f32_16x16x32_bf16 v[8:11], v[52:55], a[216:219], v[8:11]\n"
    "buffer_load_dwordx4 v246, s[44:47], s62 offen offset:2048 lds\n"
    "v_mfma_f32_16x16x32_bf16 v[8:11], v[56:59], a[232:235], v[8:11]\n"
    "ds_read_b128 v[124:127], v241 offset:45312\n"
    "v_mfma_f32_16x16x32_bf16 v[8:11], v[60:63], a[248:251], v[8:11]\n"
    "ds_read_b128 v[140:143], v241 offset:45376\n"
    "v_mfma_f32_16x16x32_bf16 v[12:15], v[48:51], a[204:207], v[12:15]\n"
    "v_mfma_f32_16x16x32_bf16 v[12:15], v[52:55], a[220:223], v[12:15]\n"
    "buffer_load_dwordx4 v247, s[44:47], s62 offen offset:3072 lds\n"
    "v_mfma_f32_16x16x32_bf16 v[12:15], v[56:59], a[236:239], v[12:15]\n"
    "s_waitcnt vmcnt(12)\n"
    "v_mfma_f32_16x16x32_bf16 v[12:15], v[60:63], a[252:255], v[12:15]\n"
    "s_waitcnt lgkmcnt(0)\n"
    "s_barrier\n"
    "v_mfma_f32_16x16x32_bf16 v[16:19], v[64:67], a[192:195], v[16:19]\n"
    "s_add_u32 m0, s59, 16384\n"
    "s_add_u32 s62, s63, 640\n"
    "v_mfma_f32_16x16x32_bf16 v[16:19], v[68:71], a[208:211], v[16:19]\n"
    "buffer_load_dwordx4 v244, s[44:47], s62 offen offset:0 lds\n"
    "v_mfma_f32_16x16x32_bf16 v[16:19], v[72:75], a[224:227], v[16:19]\n"
    "ds_read_b64_tr_b16 v[96:97], v232 offset:49152\n"
    "v_and_b32 v34, 15, v250\n"
    "v_mfma_f32_16x16x32_bf16 v[16:19], v[76:79], a[240:243], v[16:19]\n"
    "ds_read_b64_tr_b16 v[98:99], v232 offset:51200\n"
    "v_lshrrev_b32 v35, 4, v250\n"
    "v_mfma_f32_16x16x32_bf16 v[20:23], v[64:67], a[196:199], v[20:23]\n"
    "ds_read_b64_tr_b16 v[100:101], v232 offset:53248\n"
    "v_and_b32 v36, 31, v250\n"
    "v_mfma_f32_16x16x32_bf16 v[20:23], v[68:71], a[212:215], v[20:23]\n"
    "buffer_load_dwordx4 v245, s[44:47], s62 offen offset:1024 lds\n"
    "v_mfma_f32_16x16x32_bf16 v[20:23], v[72:75], a[228:231], v[20:23]\n"
    "ds_read_b64_tr_b16 v[102:103], v232 offset:55296\n"
    "v_lshrrev_b32 v37, 5, v250\n"
    "v_mfma_f32_16x16x32_bf16 v[20:23], v[76:79], a[244:247], v[20:23]\n"
    "s_and_b32 s68, %[wv], 1\n"
    "ds_read_b64_tr_b16 v[104:105], v232 offset:57344\n"
    "v_mfma_f32_16x16x32_bf16 v[24:27], v[64:67], a[200:203], v[24:27]\n"
    "s_lshl_b32 s68, s68, 3\n"
    "ds_read_b64_tr_b16 v[106:107], v232 offset:59392\n"
    "v_mfma_f32_16x16x32_bf16 v[24:27], v[68:71], a[216:219], v[24:27]\n"
    "buffer_load_dwordx4 v246, s[44:47], s62 offen offset:2048 lds\n"
    "v_mfma_f32_16x16x32_bf16 v[24:27], v[72:75], a[232:235], v[24:27]\n"
    "ds_read_b64_tr_b16 v[108:109], v232 offset:61440\n"
    "v_or_b32 v32, 0, v37\n"
    "v_mfma_f32_16x16x32_bf16 v[24:27], v[76:79], a[248:251], v[24:27]\n"
    "ds_read_b64_tr_b16 v[110:111], v232 offset:63488\n"
    "v_or_b32 v32, s68, v32\n"
    "v_mfma_f32_16x16x32_bf16 v[28:31], v[64:67], a[204:207], v[28:31]\n"
    "v_xor_b32 v32, v32, v36\n"
    "v_lshlrev_b32 v32, 4, v32\n"
    "v_mfma_f32_16x16x32_bf16 v[28:31], v[68:71], a[220:223], v[28:31]\n"
    "buffer_load_dwordx4 v247, s[44:47], s62 offen offset:3072 lds\n"
    "v_mfma_f32_16x16x32_bf16 v[28:31], v[72:75], a[236:239], v[28:31]\n"
    "s_waitcnt vmcnt(12)\n"
    "v_mfma_f32_16x16x32_bf16 v[28:31], v[76:79], a[252:255], v[28:31]\n"
    "s_waitcnt lgkmcnt(0)\n"
    "s_barrier\n"
    "v_mfma_f32_16x16x32_bf16 v[0:3], v[80:83], v[112:115], v[0:3]\n"
    "s_add_u32 m0, s59, 32768\n"
    "ds_read_b64_tr_b16 v[48:49], v233 offset:0\n"
    "s_add_u32 s62, s63, 1049216\n"
    "ds_read_b64_tr_b16 v[50:51], v233 offset:2048\n"
    "v_lshl_add_u32 v220, v37, 9, v32\n"
    "v_mfma_f32_16x16x32_bf16 v[0:3], v[84:87], v[128:131], v[0:3]\n"
    "buffer_load_dwordx4 v244, s[44:47], s62 offen offset:0 lds\n"
    "v_or_b32 v32, 2, v37\n"
    "v_mfma_f32_16x16x32_bf16 v[4:7], v[80:83], v[116:119], v[4:7]\n"
    "ds_read_b64_tr_b16 v[52:53], v233 offset:4096\n"
    "v_or_b32 v32, s68, v32\n"
    "v_xor_b32 v32, v32, v36\n"
    "v_mfma_f32_16x16x32_bf16 v[4:7], v[84:87], v[132:135], v[4:7]\n"
    "buffer_load_dwordx4 v245, s[44:47], s62 offen offset:1024 lds\n"
    "v_lshlrev_b32 v32, 4, v32\n"
    "v_mfma_f32_16x16x32_bf16 v[8:11], v[80:83], v[120:123], v[8:11]\n"
    "ds_read_b64_tr_b16 v[54:55], v233 offset:6144\n"
    "v_lshl_add_u32 v221, v37, 9, v32\n"
    "v_mfma_f32_16x16x32_bf16 v[8:11], v[84:87], v[136:139], v[8:11]\n"
    "buffer_load_dwordx4 v246, s[44:47], s62 offen offset:2048 lds\n"
    "v_mfma_f32_16x16x32_bf16 v[12:15], v[80:83], v[124:127], v[12:15]\n"
    "ds_read_b64_tr_b16 v[56:57], v233 offset:8192\n"
    "v_or_b32 v32, 4, v37\n"
    "v_mfma_f32_16x16x32_bf16 v[12:15], v[84:87], v[140:143], v[12:15]\n"
    "buffer_load_dwordx4 v247, s[44:47], s62 offen offset:3072 lds\n"
    "v_mfma_f32_16x16x32_bf16 v[16:19], v[88:91], v[112:115], v[16:19]\n"
    "s_add_u32 m0, s59, 49152\n"
    "ds_read_b64_tr_b16 v[58:59], v233 offset:10240\n"
    "s_add_u32 s62, s63, 2097792\n"
    "ds_read_b64_tr_b16 v[60:61], v233 offset:12288\n"
    "v_mfma_f32_16x16x32_bf16 v[16:19], v[92:95], v[128:131], v[16:19]\n"
    "buffer_load_dwordx4 v244, s[44:47], s62 offen offset:0 lds\n"
    "v_mfma_f32_16x16x32_bf16 v[20:23], v[88:91], v[116:119], v[20:23]\n"
    "ds_read_b64_tr_b16 v[62:63], v233 offset:14336\n"
    "v_or_b32 v32, s68, v32\n"
    "v_mfma_f32_16x16x32_bf16 v[20:23], v[92:95], v[132:135], v[20:23]\n"
    "buffer_load_dwordx4 v245, s[44:47], s62 offen offset:1024 lds\n"
    "v_mfma_f32_16x16x32_bf16 v[24:27], v[88:91], v[120:123], v[24:27]\n"
    "v_xor_b32 v32, v32, v36\n"
    "v_lshlrev_b32 v32, 4, v32\n"
    "v_mfma_f32_16x16x32_bf16 v[24:27], v[92:95], v[136:139], v[24:27]\n"
    "buffer_load_dwordx4 v246, s[44:47], s62 offen offset:2048 lds\n"
    "v_mfma_f32_16x16x32_bf16 v[28:31], v[88:91], v[124:127], v[28:31]\n"
    "s_waitcnt vmcnt(15)\n"
    "buffer_load_dwordx4 v247, s[44:47], s62 offen offset:3072 lds\n"
    "v_mfma_f32_16x16x32_bf16 v[28:31], v[92:95], v[140:143], v[28:31]\n"
    "s_waitcnt lgkmcnt(0)\n"
    "s_barrier\n"
    "v_mfma_f32_16x16x32_bf16 v[176:179], v[96:99], a[0:3], 0\n"
    "s_add_u32 m0, s59, 65536\n"
    "s_add_u32 s62, s63, 256\n"
    "v_mfma_f32_16x16x32_bf16 v[176:179], v[100:103], a[16:19], v[176:179]\n"
    "buffer_load_dwordx4 v244, s[44:47], s62 offen offset:0 lds\n"
    "v_mfma_f32_16x16x32_bf16 v[176:179], v[104:107], a[32:35], v[176:179]\n"
    "ds_read_b64_tr_b16 v[64:65], v233 offset:16384\n"
    "v_lshl_add_u32 v222, v37, 9, v32\n"
    "v_mfma_f32_16x16x32_bf16 v[176:179], v[108:111], a[48:51], v[176:179]\n"
    "ds_read_b64_tr_b16 v[66:67], v233 offset:18432\n"
    "v_or_b32 v32, 6, v37\n"
    "v_mfma_f32_16x16x32_bf16 v[180:183], v[96:99], a[4:7], 0\n"
    "ds_read_b64_tr_b16 v[68:69], v233 offset:20480\n"
    "v_or_b32 v32, s68, v32\n"
    "v_mfma_f32_16x16x32_bf16 v[180:183], v[100:103], a[20:23], v[180:183]\n"
    "buffer_load_dwordx4 v245, s[44:47], s62 offen offset:1024 lds\n"
    "v_mfma_f32_16x16x32_bf16 v[180:183], v[104:107], a[36:39], v[180:183]\n"
    "ds_read_b64_tr_b16 v[70:71], v233 offset:22528\n"
    "v_xor_b32 v32, v32, v36\n"
    "v_mfma_f32_16x16x32_bf16 v[180:183], v[108:111], a[52:55], v[180:183]\n"
    "ds_read_b64_tr_b16 v[72:73], v233 offset:24576\n"
    "v_lshlrev_b32 v32, 4, v32\n"
    "v_mfma_f32_16x16x32_bf16 v[184:187], v[96:99], a[8:11], 0\n"
    "ds_read_b64_tr_b16 v[74:75], v233 offset:26624\n"
    "v_lshl_add_u32 v223, v37, 9, v32\n"
    "v_mfma_f32_16x16x32_bf16 v[184:187], v[100:103], a[24:27], v[184:187]\n"
    "buffer_load_dwordx4 v246, s[44:47], s62 offen offset:2048 lds\n"
    "v_mfma_f32_16x16x32_bf16 v[184:187], v[104:107], a[40:43], v[184:187]\n"
    "s_and_b32 s68, s67, 0\n"
    "ds_read_b64_tr_b16 v[76:77], v233 offset:28672\n"
    "v_mfma_f32_16x16x32_bf16 v[184:187], v[108:111], a[56:59], v[184:187]\n"
    "s_lshl_b32 s68, s68, 16\n"
    "ds_read_b64_tr_b16 v[78:79], v233 offset:30720\n"
    "v_mfma_f32_16x16x32_bf16 v[188:191], v[96:99], a[12:15], 0\n"
    "s_add_u32 s56, s68, s59\n"
    "s_lshl_b32 s57, %[wv], 14\n"
    "v_mfma_f32_16x16x32_bf16 v[188:191], v[100:103], a[28:31], v[188:191]\n"
    "buffer_load_dwordx4 v247, s[44:47], s62 offen offset:3072 lds\n"
    "v_mfma_f32_16x16x32_bf16 v[188:191], v[104:107], a[44:47], v[188:191]\n"
    "s_waitcnt vmcnt(16)\n"
    "v_mfma_f32_16x16x32_bf16 v[188:191], v[108:111], a[60:63], v[188:191]\n"
    "s_waitcnt lgkmcnt(0)\n"
    "s_barrier\n"
    "v_mfma_f32_16x16x32_bf16 v[176:179], v[48:51], a[64:67], v[176:179]\n"
    "s_add_u32 s57, s57, s68\n"
    "s_lshl_b32 s68, %[wv], 14\n"
    "s_add_u32 m0, s59, 81920\n"
    "s_add_u32 s62, s63, 1048832\n"
    "v_mfma_f32_16x16x32_bf16 v[176:179], v[52:55], a[80:83], v[176:179]\n"
    "buffer_load_dwordx4 v244, s[44:47], s62 offen offset:0 lds\n"
    "v_mfma_f32_16x16x32_bf16 v[176:179], v[56:59], a[96:99], v[176:179]\n"
    "s_add_u32 s68, s68, 98304\n"
    "ds_read_b64_tr_b16 v[80:81], v232 offset:0\n"
    "v_or_b32 v33, 0, v35\n"
    "v_cvt_pk_bf16_f32 v144, v0, v1\n"
    "v_mfma_f32_16x16x32_bf16 v[176:179], v[60:63], a[112:115], v[176:179]\n"
    "ds_read_b64_tr_b16 v[82:83], v232 offset:2048\n"
    "v_xor_b32 v33, v33, v34\n"
    "v_cvt_pk_bf16_f32 v145, v2, v3\n"
    "v_mfma_f32_16x16x32_bf16 v[176:179], v[64:67], a[192:195], v[176:179]\n"
    "ds_read_b64_tr_b16 v[84:85], v232 offset:4096\n"
    "v_lshlrev_b32 v33, 4, v33\n"
    "v_cvt_pk_bf16_f32 v146, v4, v5\n"
    "v_mfma_f32_16x16x32_bf16 v[176:179], v[68:71], a[208:211], v[176:179]\n"
    "buffer_load_dwordx4 v245, s[44:47], s62 offen offset:1024 lds\n"
    "v_lshl_add_u32 v212, v34, 9, v33\n"
    "v_mfma_f32_16x16x32_bf16 v[176:179], v[72:75], a[224:227], v[176:179]\n"
    "ds_read_b64_tr_b16 v[86:87], v232 offset:6144\n"
    "v_cvt_pk_bf16_f32 v147, v6, v7\n"
    "v_mfma_f32_16x16x32_bf16 v[176:179], v[76:79], a[240:243], v[176:179]\n"
    "ds_read_b64_tr_b16 v[88:89], v232 offset:8192\n"
    "v_cvt_pk_bf16_f32 v148, v8, v9\n"
    "v_mfma_f32_16x16x32_bf16 v[180:183], v[48:51], a[68:71], v[180:183]\n"
    "ds_read_b64_tr_b16 v[90:91], v232 offset:10240\n"
    "v_cvt_pk_bf16_f32 v149, v10, v11\n"
    "v_mfma_f32_16x16x32_bf16 v[180:183], v[52:55], a[84:87], v[180:183]\n"
    "buffer_load_dwordx4 v246, s[44:47], s62 offen offset:2048 lds\n"
    "v_mfma_f32_16x16x32_bf16 v[180:183], v[56:59], a[100:103], v[180:183]\n"
    "ds_read_b64_tr_b16 v[92:93], v232 offset:12288\n"
    "v_cvt_pk_bf16_f32 v150, v12, v13\n"
    "v_mfma_f32_16x16x32_bf16 v[180:183], v[60:63], a[116:119], v[180:183]\n"
    "ds_read_b64_tr_b16 v[94:95], v232 offset:14336\n"
    "v_cvt_pk_bf16_f32 v151, v14, v15\n"
    "v_mfma_f32_16x16x32_bf16 v[180:183], v[64:67], a[196:199], v[180:183]\n"
    "v_add_u32 v236, s68, v212\n"
    "v_or_b32 v33, 4, v35\n"
    "v_mfma_f32_16x16x32_bf16 v[180:183], v[68:71], a[212:215], v[180:183]\n"
    "buffer_load_dwordx4 v247, s[44:47], s62 offen offset:3072 lds\n"
    "v_mfma_f32_16x16x32_bf16 v[180:183], v[72:75], a[228:231], v[180:183]\n"
    "v_xor_b32 v33, v33, v34\n"
    "s_waitcnt vmcnt(16)\n"
    "v_mfma_f32_16x16x32_bf16 v[180:183], v[76:79], a[244:247], v[180:183]\n"
    "s_waitcnt lgkmcnt(0)\n"
    "s_barrier\n"
    "v_mfma_f32_16x16x32_bf16 v[184:187], v[48:51], a[72:75], v[184:187]\n"
    "s_add_u32 m0, s59, 0\n"
    "v_lshlrev_b32 v33, 4, v33\n"
    "v_lshl_add_u32 v213, v34, 9, v33\n"
    "s_add_u32 s62, s63, 2097408\n"
    "v_mfma_f32_16x16x32_bf16 v[184:187], v[52:55], a[88:91], v[184:187]\n"
    "buffer_load_dwordx4 v244, s[44:47], s62 offen offset:0 lds\n"
    "v_add_u32 v237, s68, v213\n"
    "v_mfma_f32_16x16x32_bf16 v[184:187], v[56:59], a[104:107], v[184:187]\n"
    "ds_read_b64_tr_b16 v[96:97], v232 offset:16384\n"
    "v_or_b32 v33, 8, v35\n"
    "v_cvt_pk_bf16_f32 v152, v16, v17\n"
    "v_mfma_f32_16x16x32_bf16 v[184:187], v[60:63], a[120:123], v[184:187]\n"
    "ds_read_b64_tr_b16 v[98:99], v232 offset:18432\n"
    "v_xor_b32 v33, v33, v34\n"
    "v_cvt_pk_bf16_f32 v153, v18, v19\n"
    "v_mfma_f32_16x16x32_bf16 v[184:187], v[64:67], a[200:203], v[184:187]\n"
    "ds_read_b64_tr_b16 v[100:101], v232 offset:20480\n"
    "v_lshlrev_b32 v33, 4, v33\n"
    "v_cvt_pk_bf16_f32 v154, v20, v21\n"
    "v_mfma_f32_16x16x32_bf16 v[184:187], v[68:71], a[216:219], v[184:187]\n"
    "buffer_load_dwordx4 v245, s[44:47], s62 offen offset:1024 lds\n"
    "v_lshl_add_u32 v214, v34, 9, v33\n"
    "v_mfma_f32_16x16x32_bf16 v[184:187], v[72:75], a[232:235], v[184:187]\n"
    "ds_read_b64_tr_b16 v[102:103], v232 offset:22528\n"
    "v_cvt_pk_bf16_f32 v155, v22, v23\n"
    "v_mfma_f32_16x16x32_bf16 v[184:187], v[76:79], a[248:251], v[184:187]\n"
    "ds_read_b64_tr_b16 v[104:105], v232 offset:24576\n"
    "v_cvt_pk_bf16_f32 v156, v24, v25\n"
    "v_mfma_f32_16x16x32_bf16 v[188:191], v[48:51], a[76:79], v[188:191]\n"
    "ds_read_b64_tr_b16 v[106:107], v232 offset:26624\n"
    "v_cvt_pk_bf16_f32 v157, v26, v27\n"
    "v_mfma_f32_16x16x32_bf16 v[188:191], v[52:55], a[92:95], v[188:191]\n"
    "buffer_load_dwordx4 v246, s[44:47], s62 offen offset:2048 lds\n"
    "v_mfma_f32_16x16x32_bf16 v[188:191], v[56:59], a[108:111], v[188:191]\n"
    "ds_read_b64_tr_b16 v[108:109], v232 offset:28672\n"
    "v_cvt_pk_bf16_f32 v158, v28, v29\n"
    "v_mfma_f32_16x16x32_bf16 v[188:191], v[60:63], a[124:127], v[188:191]\n"
    "ds_read_b64_tr_b16 v[110:111], v232 offset:30720\n"
    "v_cvt_pk_bf16_f32 v159, v30, v31\n"
    "v_mfma_f32_16x16x32_bf16 v[188:191], v[64:67], a[204:207], v[188:191]\n"
    "v_add_u32 v238, s68, v214\n"
    "v_or_b32 v33, 12, v35\n"
    "v_mfma_f32_16x16x32_bf16 v[188:191], v[68:71], a[220:223], v[188:191]\n"
    "buffer_load_dwordx4 v247, s[44:47], s62 offen offset:3072 lds\n"
    "v_mfma_f32_16x16x32_bf16 v[188:191], v[72:75], a[236:239], v[188:191]\n"
    "v_xor_b32 v33, v33, v34\n"
    "s_waitcnt vmcnt(16)\n"
    "v_mfma_f32_16x16x32_bf16 v[188:191], v[76:79], a[252:255], v[188:191]\n"
    "s_waitcnt lgkmcnt(0)\n"
    "s_barrier\n"
    "v_mfma_f32_16x16x32_bf16 v[176:179], v[80:83], v[112:115], v[176:179]\n"
    "s_add_u32 m0, s59, 16384\n"
    "s_add_u32 s62, s70, 3145984\n"
    "v_mfma_f32_16x16x32_bf16 v[176:179], v[84:87], v[128:131], v[176:179]\n"
    "buffer_load_dwordx4 v244, s[44:47], s62 offen offset:0 lds\n"
    "v_mfma_f32_16x16x32_bf16 v[180:183], v[80:83], v[116:119], v[180:183]\n"
    "ds_read_b64_tr_b16 v[48:49], v232 offset:32768\n"
    "v_lshlrev_b32 v33, 4, v33\n"
    "v_mfma_f32_16x16x32_bf16 v[180:183], v[84:87], v[132:135], v[180:183]\n"
    "ds_read_b64_tr_b16 v[50:51], v232 offset:34816\n"
    "v_lshl_add_u32 v215, v34, 9, v33\n"
    "v_mfma_f32_16x16x32_bf16 v[184:187], v[80:83], v[120:123], v[184:187]\n"
    "ds_read_b64_tr_b16 v[52:53], v232 offset:36864\n"
    "v_add_u32 v239, s68, v215\n"
    "v_mfma_f32_16x16x32_bf16 v[184:187], v[84:87], v[136:139], v[184:187]\n"
    "buffer_load_dwordx4 v245, s[44:47], s62 offen offset:1024 lds\n"
    "v_mfma_f32_16x16x32_bf16 v[188:191], v[80:83], v[124:127], v[188:191]\n"
    "s_lshl_b32 s68, %[wv], 1\n"
    "ds_read_b64_tr_b16 v[54:55], v232 offset:38912\n"
    "v_mfma_f32_16x16x32_bf16 v[188:191], v[84:87], v[140:143], v[188:191]\n"
    "ds_read_b64_tr_b16 v[56:57], v232 offset:40960\n"
    "v_or_b32 v32, s68, v37\n"
    "v_mfma_f32_16x16x32_bf16 v[192:195], v[88:91], v[112:115], 0\n"
    "ds_read_b64_tr_b16 v[58:59], v232 offset:43008\n"
    "v_xor_b32 v32, v32, v34\n"
    "v_mfma_f32_16x16x32_bf16 v[192:195], v[92:95], v[128:131], v[192:195]\n"
    "buffer_load_dwordx4 v246, s[44:47], s62 offen offset:2048 lds\n"
    "v_mfma_f32_16x16x32_bf16 v[192:195], v[96:99], a[0:3], v[192:195]\n"
    "ds_read_b64_tr_b16 v[60:61], v232 offset:45056\n"
    "v_lshlrev_b32 v32, 4, v32\n"
    "v_mfma_f32_16x16x32_bf16 v[192:195], v[100:103], a[16:19], v[192:195]\n"
    "ds_read_b64_tr_b16 v[62:63], v232 offset:47104\n"
    "v_lshl_add_u32 v32, v34, 9, v32\n"
    "v_mfma_f32_16x16x32_bf16 v[192:195], v[104:107], a[32:35], v[192:195]\n"
    "v_and_b32 v33, 1, v35\n"
    "v_lshl_add_u32 v32, v33, 3, v32\n"
    "v_mfma_f32_16x16x32_bf16 v[192:195], v[108:111], a[48:51], v[192:195]\n"
    "buffer_load_dwordx4 v247, s[44:47], s62 offen offset:3072 lds\n"
    "v_mfma_f32_16x16x32_bf16 v[196:199], v[88:91], v[116:119], 0\n"
    "s_waitcnt vmcnt(16)\n"
    "v_mfma_f32_16x16x32_bf16 v[196:199], v[92:95], v[132:135], v[196:199]\n"
    "s_waitcnt lgkmcnt(0)\n"
    "s_barrier\n"
    "v_mfma_f32_16x16x32_bf16 v[196:199], v[96:99], a[4:7], v[196:199]\n"
    "s_add_u32 m0, s59, 32768\n"
    "s_add_u32 s62, s63, 768\n"
    "v_mfma_f32_16x16x32_bf16 v[196:199], v[100:103], a[20:23], v[196:199]\n"
    "buffer_load_dwordx4 v244, s[44:47], s62 offen offset:0 lds\n"
    "v_mfma_f32_16x16x32_bf16 v[196:199], v[104:107], a[36:39], v[196:199]\n"
    "ds_read_b64_tr_b16 v[64:65], v232 offset:49152\n"
    "v_add_u32 v234, 98304, v32\n"
    "v_mfma_f32_16x16x32_bf16 v[196:199], v[108:111], a[52:55], v[196:199]\n"
    "ds_read_b64_tr_b16 v[66:67], v232 offset:51200\n"
    "v_xor_b32 v235, 128, v234\n"
    "v_mfma_f32_16x16x32_bf16 v[200:203], v[88:91], v[120:123], 0\n"
    "ds_read_b64_tr_b16 v[68:69], v232 offset:53248\n"
    "v_xor_b32 v217, 32, v216\n"
    "v_mfma_f32_16x16x32_bf16 v[200:203], v[92:95], v[136:139], v[200:203]\n"
    "buffer_load_dwordx4 v245, s[44:47], s62 offen offset:1024 lds\n"
    "v_mfma_f32_16x16x32_bf16 v[200:203], v[96:99], a[8:11], v[200:203]\n"
    "ds_read_b64_tr_b16 v[70:71], v232 offset:55296\n"
    "v_xor_b32 v218, 64, v216\n"
    "v_mfma_f32_16x16x32_bf16 v[200:203], v[100:103], a[24:27], v[200:203]\n"
    "ds_read_b64_tr_b16 v[72:73], v232 offset:57344\n"
    "v_xor_b32 v219, 96, v216\n"
    "v_mfma_f32_16x16x32_bf16 v[200:203], v[104:107], a[40:43], v[200:203]\n"
    "ds_read_b64_tr_b16 v[74:75], v232 offset:59392\n"
    "v_add_u32 v225, 3072, v224\n"
    "v_mfma_f32_16x16x32_bf16 v[200:203], v[108:111], a[56:59], v[200:203]\n"
    "buffer_load_dwordx4 v246, s[44:47], s62 offen offset:2048 lds\n"
    "v_mfma_f32_16x16x32_bf16 v[204:207], v[88:91], v[124:127], 0\n"
    "ds_read_b64_tr_b16 v[76:77], v232 offset:61440\n"
    "v_add_u32 v226, 6144, v224\n"
    "v_mfma_f32_16x16x32_bf16 v[204:207], v[92:95], v[140:143], v[204:207]\n"
    "ds_read_b64_tr_b16 v[78:79], v232 offset:63488\n"
    "v_add_u32 v227, 9216, v224\n"
    "v_mfma_f32_16x16x32_bf16 v[204:207], v[96:99], a[12:15], v[204:207]\n"
    "v_add_u32 v253, 6144, v252\n"
    "v_add_u32 v254, 12288, v252\n"
    "v_mfma_f32_16x16x32_bf16 v[204:207], v[100:103], a[28:31], v[204:207]\n"
    "buffer_load_dwordx4 v247, s[44:47], s62 offen offset:3072 lds\n"
    "v_mfma_f32_16x16x32_bf16 v[204:207], v[104:107], a[44:47], v[204:207]\n"
    "s_waitcnt vmcnt(16)\n"
    "v_mfma_f32_16x16x32_bf16 v[204:207], v[108:111], a[60:63], v[204:207]\n"
    "s_waitcnt lgkmcnt(0)\n"
    "s_barrier\n"
    "v_mfma_f32_16x16x32_bf16 v[192:195], v[48:51], a[64:67], v[192:195]\n"
    "s_add_u32 m0, s59, 49152\n"
    "v_add_u32 v255, 18432, v252\n"
    "v_mov_b32 v208, 0x3f803f80\n"
    "s_add_u32 s62, s63, 1049344\n"
    "v_mfma_f32_16x16x32_bf16 v[192:195], v[52:55], a[80:83], v[192:195]\n"
    "buffer_load_dwordx4 v244, s[44:47], s62 offen offset:0 lds\n"
    "v_mov_b32 v209, 0x3f803f80\n"
    "v_mfma_f32_16x16x32_bf16 v[192:195], v[56:59], a[96:99], v[192:195]\n"
    "ds_read_b64_tr_b16 v[80:81], v233 offset:0\n"
    "v_mov_b32 v210, 0x3f803f80\n"
    "v_cvt_pk_bf16_f32 v160, v176, v177\n"
    "v_mfma_f32_16x16x32_bf16 v[192:195], v[60:63], a[112:115], v[192:195]\n"
    "ds_read_b64_tr_b16 v[82:83], v233 offset:2048\n"
    "v_mov_b32 v211, 0x3f803f80\n"
    "v_cvt_pk_bf16_f32 v161, v178, v179\n"
    "v_mfma_f32_16x16x32_bf16 v[192:195], v[64:67], a[192:195], v[192:195]\n"
    "s_mov_b64 s[72:73], %[wo]\n"
    "s_and_b32 s73, s73, 0xffff\n"
    "ds_read_b64_tr_b16 v[84:85], v233 offset:4096\n"
    "v_cvt_pk_bf16_f32 v162, v180, v181\n"
    "v_mfma_f32_16x16x32_bf16 v[192:195], v[68:71], a[208:211], v[192:195]\n"
    "buffer_load_dwordx4 v245, s[44:47], s62 offen offset:1024 lds\n"
    "v_mfma_f32_16x16x32_bf16 v[192:195], v[72:75], a[224:227], v[192:195]\n"
    "ds_read_b64_tr_b16 v[86:87], v233 offset:6144\n"
    "v_cvt_pk_bf16_f32 v163, v182, v183\n"
    "v_mfma_f32_16x16x32_bf16 v[192:195], v[76:79], a[240:243], v[192:195]\n"
    "ds_read_b64_tr_b16 v[88:89], v233 offset:8192\n"
    "v_cvt_pk_bf16_f32 v164, v184, v185\n"
    "v_mfma_f32_16x16x32_bf16 v[196:199], v[48:51], a[68:71], v[196:199]\n"
    "ds_read_b64_tr_b16 v[90:91], v233 offset:10240\n"
    "v_cvt_pk_bf16_f32 v165, v186, v187\n"
    "v_mfma_f32_16x16x32_bf16 v[196:199], v[52:55], a[84:87], v[196:199]\n"
    "buffer_load_dwordx4 v246, s[44:47], s62 offen offset:2048 lds\n"
    "v_mfma_f32_16x16x32_bf16 v[196:199], v[56:59], a[100:103], v[196:199]\n"
    "ds_read_b64_tr_b16 v[92:93], v233 offset:12288\n"
    "v_cvt_pk_bf16_f32 v166, v188, v189\n"
    "v_mfma_f32_16x16x32_bf16 v[196:199], v[60:63], a[116:119], v[196:199]\n"
    "ds_read_b64_tr_b16 v[94:95], v233 offset:14336\n"
    "v_cvt_pk_bf16_f32 v167, v190, v191\n"
    "v_mfma_f32_16x16x32_bf16 v[196:199], v[64:67], a[196:199], v[196:199]\n"
    "s_mov_b32 s74, 3670016\n"
    "s_mov_b32 s60, 0x40004000\n"
    "v_mfma_f32_16x16x32_bf16 v[196:199], v[68:71], a[212:215], v[196:199]\n"
    "buffer_load_dwordx4 v247, s[44:47], s62 offen offset:3072 lds\n"
    "v_mfma_f32_16x16x32_bf16 v[196:199], v[72:75], a[228:231], v[196:199]\n"
    "s_lshl_b32 s84, s66, 4\n"
    "s_waitcnt vmcnt(16)\n"
    "v_mfma_f32_16x16x32_bf16 v[196:199], v[76:79], a[244:247], v[196:199]\n"
    "s_waitcnt lgkmcnt(0)\n"
    "s_barrier\n"
    "v_mfma_f32_16x16x32_bf16 v[200:203], v[48:51], a[72:75], v[200:203]\n"
    "s_add_u32 m0, s59, 65536\n"
    "s_add_u32 s62, s63, 2097920\n"
    "v_mfma_f32_16x16x32_bf16 v[200:203], v[52:55], a[88:91], v[200:203]\n"
    "buffer_load_dwordx4 v244, s[44:47], s62 offen offset:0 lds\n"
    "v_mfma_f32_16x16x32_bf16 v[200:203], v[56:59], a[104:107], v[200:203]\n"
    "s_add_u32 s84, s84, %[wv]\n"
    "ds_read_b64_tr_b16 v[96:97], v233 offset:16384\n"
    "v_mfma_f32_16x16x32_bf16 v[200:203], v[60:63], a[120:123], v[200:203]\n"
    "s_mul_i32 s84, s84, 28672\n"
    "ds_read_b64_tr_b16 v[98:99], v233 offset:18432\n"
    "v_mfma_f32_16x16x32_bf16 v[200:203], v[64:67], a[200:203], v[200:203]\n"
    "ds_read_b64_tr_b16 v[100:101], v233 offset:20480\n"
    "v_mfma_f32_16x16x32_bf16 v[200:203], v[68:71], a[216:219], v[200:203]\n"
    "buffer_load_dwordx4 v245, s[44:47], s62 offen offset:1024 lds\n"
    "v_mfma_f32_16x16x32_bf16 v[200:203], v[72:75], a[232:235], v[200:203]\n"
    "ds_read_b64_tr_b16 v[102:103], v233 offset:22528\n"
    "v_mfma_f32_16x16x32_bf16 v[200:203], v[76:79], a[248:251], v[200:203]\n"
    "ds_read_b64_tr_b16 v[104:105], v233 offset:24576\n"
    "v_mfma_f32_16x16x32_bf16 v[204:207], v[48:51], a[76:79], v[204:207]\n"
    "ds_read_b64_tr_b16 v[106:107], v233 offset:26624\n"
    "v_mfma_f32_16x16x32_bf16 v[204:207], v[52:55], a[92:95], v[204:207]\n"
    "buffer_load_dwordx4 v246, s[44:47], s62 offen offset:2048 lds\n"
    "v_mfma_f32_16x16x32_bf16 v[204:207], v[56:59], a[108:111], v[204:207]\n"
    "ds_read_b64_tr_b16 v[108:109], v233 offset:28672\n"
    "v_mfma_f32_16x16x32_bf16 v[204:207], v[60:63], a[124:127], v[204:207]\n"
    "ds_read_b64_tr_b16 v[110:111], v233 offset:30720\n"
    "v_mfma_f32_16x16x32_bf16 v[204:207], v[64:67], a[204:207], v[204:207]\n"
    "v_mfma_f32_16x16x32_bf16 v[204:207], v[68:71], a[220:223], v[204:207]\n"
    "buffer_load_dwordx4 v247, s[44:47], s62 offen offset:3072 lds\n"
    "v_mfma_f32_16x16x32_bf16 v[204:207], v[72:75], a[236:239], v[204:207]\n"
    "s_waitcnt vmcnt(16)\n"
    "v_mfma_f32_16x16x32_bf16 v[204:207], v[76:79], a[252:255], v[204:207]\n"
    "s_waitcnt lgkmcnt(0)\n"
    "s_barrier\n"
    "v_mfma_f32_16x16x32_bf16 v[0:3], v[80:83], a[0:3], 0\n"
    "s_add_u32 m0, s59, 81920\n"
    "s_add_u32 s62, s63, 384\n"
    "v_mfma_f32_16x16x32_bf16 v[0:3], v[84:87], a[16:19], v[0:3]\n"
    "buffer_load_dwordx4 v244, s[44:47], s62 offen offset:0 lds\n"
    "v_mfma_f32_16x16x32_bf16 v[0:3], v[88:91], a[32:35], v[0:3]\n"
    "ds_read_b64_tr_b16 v[48:49], v232 offset:0\n"
    "v_cvt_pk_bf16_f32 v168, v192, v193\n"
    "v_mfma_f32_16x16x32_bf16 v[0:3], v[92:95], a[48:51], v[0:3]\n"
    "ds_read_b64_tr_b16 v[50:51], v232 offset:2048\n"
    "v_cvt_pk_bf16_f32 v169, v194, v195\n"
    "v_mfma_f32_16x16x32_bf16 v[0:3], v[96:99], a[64:67], v[0:3]\n"
    "ds_read_b64_tr_b16 v[52:53], v232 offset:4096\n"
    "v_cvt_pk_bf16_f32 v170, v196, v197\n"
    "v_mfma_f32_16x16x32_bf16 v[0:3], v[100:103], a[80:83], v[0:3]\n"
    "buffer_load_dwordx4 v245, s[44:47], s62 offen offset:1024 lds\n"
    "v_mfma_f32_16x16x32_bf16 v[0:3], v[104:107], a[96:99], v[0:3]\n"
    "ds_read_b64_tr_b16 v[54:55], v232 offset:6144\n"
    "v_cvt_pk_bf16_f32 v171, v198, v199\n"
    "v_mfma_f32_16x16x32_bf16 v[0:3], v[108:111], a[112:115], v[0:3]\n"
    "ds_read_b64_tr_b16 v[56:57], v232 offset:8192\n"
    "v_cvt_pk_bf16_f32 v172, v200, v201\n"
    "v_mfma_f32_16x16x32_bf16 v[4:7], v[80:83], a[4:7], 0\n"
    "ds_read_b64_tr_b16 v[58:59], v232 offset:10240\n"
    "v_cvt_pk_bf16_f32 v173, v202, v203\n"
    "v_mfma_f32_16x16x32_bf16 v[4:7], v[84:87], a[20:23], v[4:7]\n"
    "buffer_load_dwordx4 v246, s[44:47], s62 offen offset:2048 lds\n"
    "v_mfma_f32_16x16x32_bf16 v[4:7], v[88:91], a[36:39], v[4:7]\n"
    "ds_read_b64_tr_b16 v[60:61], v232 offset:12288\n"
    "v_cvt_pk_bf16_f32 v174, v204, v205\n"
    "v_mfma_f32_16x16x32_bf16 v[4:7], v[92:95], a[52:55], v[4:7]\n"
    "ds_read_b64_tr_b16 v[62:63], v232 offset:14336\n"
    "v_cvt_pk_bf16_f32 v175, v206, v207\n"
    "v_mfma_f32_16x16x32_bf16 v[4:7], v[96:99], a[68:71], v[4:7]\n"
    "v_mfma_f32_16x16x32_bf16 v[4:7], v[100:103], a[84:87], v[4:7]\n"
    "buffer_load_dwordx4 v247, s[44:47], s62 offen offset:3072 lds\n"
    "v_mfma_f32_16x16x32_bf16 v[4:7], v[104:107], a[100:103], v[4:7]\n"
    "s_waitcnt vmcnt(16)\n"
    "v_mfma_f32_16x16x32_bf16 v[4:7], v[108:111], a[116:119], v[4:7]\n"
    "s_waitcnt lgkmcnt(0)\n"
    "s_barrier\n"
    "v_mfma_f32_16x16x32_bf16 v[8:11], v[80:83], a[8:11], 0\n"
    "s_add_u32 m0, s59, 0\n"
    "s_add_u32 s62, s63, 1048960\n"
    "v_mfma_f32_16x16x32_bf16 v[8:11], v[84:87], a[24:27], v[8:11]\n"
    "buffer_load_dwordx4 v244, s[44:47], s62 offen offset:0 lds\n"
    "v_mfma_f32_16x16x32_bf16 v[8:11], v[88:91], a[40:43], v[8:11]\n"
    "ds_read_b64_tr_b16 v[64:65], v232 offset:16384\n"
    "v_mfma_f32_16x16x32_bf16 v[8:11], v[92:95], a[56:59], v[8:11]\n"
    "ds_read_b64_tr_b16 v[66:67], v232 offset:18432\n"
    "v_mfma_f32_16x16x32_bf16 v[8:11], v[96:99], a[72:75], v[8:11]\n"
    "ds_read_b64_tr_b16 v[68:69], v232 offset:20480\n"
    "v_mfma_f32_16x16x32_bf16 v[8:11], v[100:103], a[88:91], v[8:11]\n"
    "buffer_load_dwordx4 v245, s[44:47], s62 offen offset:1024 lds\n"
    "v_mfma_f32_16x16x32_bf16 v[8:11], v[104:107], a[104:107], v[8:11]\n"
    "ds_read_b64_tr_b16 v[70:71], v232 offset:22528\n"
    "v_mfma_f32_16x16x32_bf16 v[8:11], v[108:111], a[120:123], v[8:11]\n"
    "ds_read_b64_tr_b16 v[72:73], v232 offset:24576\n"
    "v_mfma_f32_16x16x32_bf16 v[12:15], v[80:83], a[12:15], 0\n"
    "ds_read_b64_tr_b16 v[74:75], v232 offset:26624\n"
    "v_mfma_f32_16x16x32_bf16 v[12:15], v[84:87], a[28:31], v[12:15]\n"
    "buffer_load_dwordx4 v246, s[44:47], s62 offen offset:2048 lds\n"
    "v_mfma_f32_16x16x32_bf16 v[12:15], v[88:91], a[44:47], v[12:15]\n"
    "ds_read_b64_tr_b16 v[76:77], v232 offset:28672\n"
    "v_mfma_f32_16x16x32_bf16 v[12:15], v[92:95], a[60:63], v[12:15]\n"
    "ds_read_b64_tr_b16 v[78:79], v232 offset:30720\n"
    "v_mfma_f32_16x16x32_bf16 v[12:15], v[96:99], a[76:79], v[12:15]\n"
    "v_mfma_f32_16x16x32_bf16 v[12:15], v[100:103], a[92:95], v[12:15]\n"
    "buffer_load_dwordx4 v247, s[44:47], s62 offen offset:3072 lds\n"
    "v_mfma_f32_16x16x32_bf16 v[12:15], v[104:107], a[108:111], v[12:15]\n"
    "s_waitcnt vmcnt(16)\n"
    "v_mfma_f32_16x16x32_bf16 v[12:15], v[108:111], a[124:127], v[12:15]\n"
    "s_waitcnt lgkmcnt(0)\n"
    "s_barrier\n"
    "v_mfma_f32_16x16x32_bf16 v[0:3], v[48:51], a[192:195], v[0:3]\n"
    "s_add_u32 m0, s59, 16384\n"
    "s_add_u32 s62, s63, 2097536\n"
    "v_mfma_f32_16x16x32_bf16 v[0:3], v[52:55], a[208:211], v[0:3]\n"
    "buffer_load_dwordx4 v244, s[44:47], s62 offen offset:0 lds\n"
    "v_mfma_f32_16x16x32_bf16 v[0:3], v[56:59], a[224:227], v[0:3]\n"
    "ds_read_b64_tr_b16 v[80:81], v232 offset:32768\n"
    "v_mfma_f32_16x16x32_bf16 v[0:3], v[60:63], a[240:243], v[0:3]\n"
    "ds_read_b64_tr_b16 v[82:83], v232 offset:34816\n"
    "v_mfma_f32_16x16x32_bf16 v[0:3], v[64:67], v[112:115], v[0:3]\n"
    "ds_read_b64_tr_b16 v[84:85], v232 offset:36864\n"
    "v_mfma_f32_16x16x32_bf16 v[0:3], v[68:71], v[128:131], v[0:3]\n"
    "buffer_load_dwordx4 v245, s[44:47], s62 offen offset:1024 lds\n"
    "v_mfma_f32_16x16x32_bf16 v[4:7], v[48:51], a[196:199], v[4:7]\n"
    "ds_read_b64_tr_b16 v[86:87], v232 offset:38912\n"
    "v_mfma_f32_16x16x32_bf16 v[4:7], v[52:55], a[212:215], v[4:7]\n"
    "ds_read_b64_tr_b16 v[88:89], v232 offset:40960\n"
    "v_mfma_f32_16x16x32_bf16 v[4:7], v[56:59], a[228:231], v[4:7]\n"
    "ds_read_b64_tr_b16 v[90:91], v232 offset:43008\n"
    "v_mfma_f32_16x16x32_bf16 v[4:7], v[60:63], a[244:247], v[4:7]\n"
    "buffer_load_dwordx4 v246, s[44:47], s62 offen offset:2048 lds\n"
    "v_mfma_f32_16x16x32_bf16 v[4:7], v[64:67], v[116:119], v[4:7]\n"
    "ds_read_b64_tr_b16 v[92:93], v232 offset:45056\n"
    "v_mfma_f32_16x16x32_bf16 v[4:7], v[68:71], v[132:135], v[4:7]\n"
    "ds_read_b64_tr_b16 v[94:95], v232 offset:47104\n"
    "v_mfma_f32_16x16x32_bf16 v[8:11], v[48:51], a[200:203], v[8:11]\n"
    "v_mfma_f32_16x16x32_bf16 v[8:11], v[52:55], a[216:219], v[8:11]\n"
    "buffer_load_dwordx4 v247, s[44:47], s62 offen offset:3072 lds\n"
    "v_mfma_f32_16x16x32_bf16 v[8:11], v[56:59], a[232:235], v[8:11]\n"
    "s_waitcnt vmcnt(16)\n"
    "v_mfma_f32_16x16x32_bf16 v[8:11], v[60:63], a[248:251], v[8:11]\n"
    "s_waitcnt lgkmcnt(0)\n"
    "s_barrier\n"
    "v_mfma_f32_16x16x32_bf16 v[8:11], v[64:67], v[120:123], v[8:11]\n"
    "s_add_u32 m0, s59, 32768\n"
    "s_add_u32 s62, s70, 3146112\n"
    "v_mfma_f32_16x16x32_bf16 v[8:11], v[68:71], v[136:139], v[8:11]\n"
    "buffer_load_dwordx4 v244, s[44:47], s62 offen offset:0 lds\n"
    "v_mfma_f32_16x16x32_bf16 v[12:15], v[48:51], a[204:207], v[12:15]\n"
    "ds_read_b64_tr_b16 v[96:97], v232 offset:49152\n"
    "v_mfma_f32_16x16x32_bf16 v[12:15], v[52:55], a[220:223], v[12:15]\n"
    "ds_read_b64_tr_b16 v[98:99], v232 offset:51200\n"
    "v_mfma_f32_16x16x32_bf16 v[12:15], v[56:59], a[236:239], v[12:15]\n"
    "ds_read_b64_tr_b16 v[100:101], v232 offset:53248\n"
    "v_mfma_f32_16x16x32_bf16 v[12:15], v[60:63], a[252:255], v[12:15]\n"
    "buffer_load_dwordx4 v245, s[44:47], s62 offen offset:1024 lds\n"
    "v_mfma_f32_16x16x32_bf16 v[12:15], v[64:67], v[124:127], v[12:15]\n"
    "ds_read_b64_tr_b16 v[102:103], v232 offset:55296\n"
    "v_mfma_f32_16x16x32_bf16 v[12:15], v[68:71], v[140:143], v[12:15]\n"
    "ds_read_b64_tr_b16 v[104:105], v232 offset:57344\n"
    "v_mfma_f32_16x16x32_bf16 v[16:19], v[72:75], v[112:115], 0\n"
    "ds_read_b64_tr_b16 v[106:107], v232 offset:59392\n"
    "v_mfma_f32_16x16x32_bf16 v[16:19], v[76:79], v[128:131], v[16:19]\n"
    "buffer_load_dwordx4 v246, s[44:47], s62 offen offset:2048 lds\n"
    "v_mfma_f32_16x16x32_bf16 v[20:23], v[72:75], v[116:119], 0\n"
    "ds_read_b64_tr_b16 v[108:109], v232 offset:61440\n"
    "v_mfma_f32_16x16x32_bf16 v[20:23], v[76:79], v[132:135], v[20:23]\n"
    "ds_read_b64_tr_b16 v[110:111], v232 offset:63488\n"
    "v_mfma_f32_16x16x32_bf16 v[24:27], v[72:75], v[120:123], 0\n"
    "v_mfma_f32_16x16x32_bf16 v[24:27], v[76:79], v[136:139], v[24:27]\n"
    "buffer_load_dwordx4 v247, s[44:47], s62 offen offset:3072 lds\n"
    "v_mfma_f32_16x16x32_bf16 v[28:31], v[72:75], v[124:127], 0\n"
    "s_waitcnt vmcnt(16)\n"
    "v_mfma_f32_16x16x32_bf16 v[28:31], v[76:79], v[140:143], v[28:31]\n"
    "s_waitcnt lgkmcnt(0)\n"
    "s_barrier\n"
    "v_mfma_f32_16x16x32_bf16 v[16:19], v[80:83], a[0:3], v[16:19]\n"
    "s_add_u32 m0, s59, 49152\n"
    "s_add_u32 s62, s63, 896\n"
    "v_mfma_f32_16x16x32_bf16 v[16:19], v[84:87], a[16:19], v[16:19]\n"
    "buffer_load_dwordx4 v244, s[44:47], s62 offen offset:0 lds\n"
    "v_mfma_f32_16x16x32_bf16 v[16:19], v[88:91], a[32:35], v[16:19]\n"
    "ds_read_b64_tr_b16 v[48:49], v233 offset:0\n"
    "v_cvt_pk_bf16_f32 v32, v0, v1\n"
    "v_mfma_f32_16x16x32_bf16 v[16:19], v[92:95], a[48:51], v[16:19]\n"
    "ds_read_b64_tr_b16 v[50:51], v233 offset:2048\n"
    "v_cvt_pk_bf16_f32 v33, v2, v3\n"
    "v_mfma_f32_16x16x32_bf16 v[16:19], v[96:99], a[64:67], v[16:19]\n"
    "ds_read_b64_tr_b16 v[52:53], v233 offset:4096\n"
    "v_cvt_pk_bf16_f32 v34, v4, v5\n"
    "v_mfma_f32_16x16x32_bf16 v[16:19], v[100:103], a[80:83], v[16:19]\n"
    "buffer_load_dwordx4 v245, s[44:47], s62 offen offset:1024 lds\n"
    "v_mfma_f32_16x16x32_bf16 v[16:19], v[104:107], a[96:99], v[16:19]\n"
    "ds_read_b64_tr_b16 v[54:55], v233 offset:6144\n"
    "v_cvt_pk_bf16_f32 v35, v6, v7\n"
    "v_mfma_f32_16x16x32_bf16 v[16:19], v[108:111], a[112:115], v[16:19]\n"
    "ds_read_b64_tr_b16 v[56:57], v233 offset:8192\n"
    "v_cvt_pk_bf16_f32 v36, v8, v9\n"
    "v_mfma_f32_16x16x32_bf16 v[20:23], v[80:83], a[4:7], v[20:23]\n"
    "ds_read_b64_tr_b16 v[58:59], v233 offset:10240\n"
    "v_cvt_pk_bf16_f32 v37, v10, v11\n"
    "v_mfma_f32_16x16x32_bf16 v[20:23], v[84:87], a[20:23], v[20:23]\n"
    "buffer_load_dwordx4 v246, s[44:47], s62 offen offset:2048 lds\n"
    "v_mfma_f32_16x16x32_bf16 v[20:23], v[88:91], a[36:39], v[20:23]\n"
    "ds_read_b64_tr_b16 v[60:61], v233 offset:12288\n"
    "v_cvt_pk_bf16_f32 v38, v12, v13\n"
    "v_mfma_f32_16x16x32_bf16 v[20:23], v[92:95], a[52:55], v[20:23]\n"
    "ds_read_b64_tr_b16 v[62:63], v233 offset:14336\n"
    "v_cvt_pk_bf16_f32 v39, v14, v15\n"
    "v_mfma_f32_16x16x32_bf16 v[20:23], v[96:99], a[68:71], v[20:23]\n"
    "v_mfma_f32_16x16x32_bf16 v[20:23], v[100:103], a[84:87], v[20:23]\n"
    "buffer_load_dwordx4 v247, s[44:47], s62 offen offset:3072 lds\n"
    "v_mfma_f32_16x16x32_bf16 v[20:23], v[104:107], a[100:103], v[20:23]\n"
    "s_waitcnt vmcnt(16)\n"
    "v_mfma_f32_16x16x32_bf16 v[20:23], v[108:111], a[116:119], v[20:23]\n"
    "s_waitcnt lgkmcnt(0)\n"
    "s_barrier\n"
    "v_mfma_f32_16x16x32_bf16 v[24:27], v[80:83], a[8:11], v[24:27]\n"
    "s_add_u32 m0, s59, 65536\n"
    "s_add_u32 s62, s63, 1049472\n"
    "v_mfma_f32_16x16x32_bf16 v[24:27], v[84:87], a[24:27], v[24:27]\n"
    "buffer_load_dwordx4 v244, s[44:47], s62 offen offset:0 lds\n"
    "v_mfma_f32_16x16x32_bf16 v[24:27], v[88:91], a[40:43], v[24:27]\n"
    "ds_read_b64_tr_b16 v[64:65], v233 offset:16384\n"
    "v_mfma_f32_16x16x32_bf16 v[24:27], v[92:95], a[56:59], v[24:27]\n"
    "ds_read_b64_tr_b16 v[66:67], v233 offset:18432\n"
    "v_mfma_f32_16x16x32_bf16 v[24:27], v[96:99], a[72:75], v[24:27]\n"
    "ds_read_b64_tr_b16 v[68:69], v233 offset:20480\n"
    "v_mfma_f32_16x16x32_bf16 v[24:27], v[100:103], a[88:91], v[24:27]\n"
    "buffer_load_dwordx4 v245, s[44:47], s62 offen offset:1024 lds\n"
    "v_mfma_f32_16x16x32_bf16 v[24:27], v[104:107], a[104:107], v[24:27]\n"
    "ds_read_b64_tr_b16 v[70:71], v233 offset:22528\n"
    "v_mfma_f32_16x16x32_bf16 v[24:27], v[108:111], a[120:123], v[24:27]\n"
    "ds_read_b64_tr_b16 v[72:73], v233 offset:24576\n"
    "v_mfma_f32_16x16x32_bf16 v[28:31], v[80:83], a[12:15], v[28:31]\n"
    "ds_read_b64_tr_b16 v[74:75], v233 offset:26624\n"
    "v_mfma_f32_16x16x32_bf16 v[28:31], v[84:87], a[28:31], v[28:31]\n"
    "buffer_load_dwordx4 v246, s[44:47], s62 offen offset:2048 lds\n"
    "v_mfma_f32_16x16x32_bf16 v[28:31], v[88:91], a[44:47], v[28:31]\n"
    "ds_read_b64_tr_b16 v[76:77], v233 offset:28672\n"
    "v_mfma_f32_16x16x32_bf16 v[28:31], v[92:95], a[60:63], v[28:31]\n"
    "ds_read_b64_tr_b16 v[78:79], v233 offset:30720\n"
    "v_mfma_f32_16x16x32_bf16 v[28:31], v[96:99], a[76:79], v[28:31]\n"
    "v_mfma_f32_16x16x32_bf16 v[28:31], v[100:103], a[92:95], v[28:31]\n"
    "buffer_load_dwordx4 v247, s[44:47], s62 offen offset:3072 lds\n"
    "v_mfma_f32_16x16x32_bf16 v[28:31], v[104:107], a[108:111], v[28:31]\n"
    "s_waitcnt vmcnt(16)\n"
    "v_mfma_f32_16x16x32_bf16 v[28:31], v[108:111], a[124:127], v[28:31]\n"
    "s_waitcnt lgkmcnt(0)\n"
    "s_barrier\n"
    "v_mfma_f32_16x16x32_bf16 v[16:19], v[48:51], a[192:195], v[16:19]\n"
    "s_add_u32 m0, s59, 81920\n"
    "s_add_u32 s62, s63, 2098048\n"
    "v_mfma_f32_16x16x32_bf16 v[16:19], v[52:55], a[208:211], v[16:19]\n"
    "buffer_load_dwordx4 v244, s[44:47], s62 offen offset:0 lds\n"
    "v_mfma_f32_16x16x32_bf16 v[16:19], v[56:59], a[224:227], v[16:19]\n"
    "ds_read_b64_tr_b16 v[80:81], v232 offset:0\n"
    "v_mfma_f32_16x16x32_bf16 v[16:19], v[60:63], a[240:243], v[16:19]\n"
    "ds_read_b64_tr_b16 v[82:83], v232 offset:2048\n"
    "v_mfma_f32_16x16x32_bf16 v[20:23], v[48:51], a[196:199], v[20:23]\n"
    "ds_read_b64_tr_b16 v[84:85], v232 offset:4096\n"
    "v_mfma_f32_16x16x32_bf16 v[20:23], v[52:55], a[212:215], v[20:23]\n"
    "buffer_load_dwordx4 v245, s[44:47], s62 offen offset:1024 lds\n"
    "v_mfma_f32_16x16x32_bf16 v[20:23], v[56:59], a[228:231], v[20:23]\n"
    "ds_read_b64_tr_b16 v[86:87], v232 offset:6144\n"
    "v_mfma_f32_16x16x32_bf16 v[20:23], v[60:63], a[244:247], v[20:23]\n"
    "ds_read_b64_tr_b16 v[88:89], v232 offset:8192\n"
    "v_mfma_f32_16x16x32_bf16 v[24:27], v[48:51], a[200:203], v[24:27]\n"
    "ds_read_b64_tr_b16 v[90:91], v232 offset:10240\n"
    "v_mfma_f32_16x16x32_bf16 v[24:27], v[52:55], a[216:219], v[24:27]\n"
    "buffer_load_dwordx4 v246, s[44:47], s62 offen offset:2048 lds\n"
    "v_mfma_f32_16x16x32_bf16 v[24:27], v[56:59], a[232:235], v[24:27]\n"
    "ds_read_b64_tr_b16 v[92:93], v232 offset:12288\n"
    "v_mfma_f32_16x16x32_bf16 v[24:27], v[60:63], a[248:251], v[24:27]\n"
    "ds_read_b64_tr_b16 v[94:95], v232 offset:14336\n"
    "v_mfma_f32_16x16x32_bf16 v[28:31], v[48:51], a[204:207], v[28:31]\n"
    "v_mfma_f32_16x16x32_bf16 v[28:31], v[52:55], a[220:223], v[28:31]\n"
    "buffer_load_dwordx4 v247, s[44:47], s62 offen offset:3072 lds\n"
    "v_mfma_f32_16x16x32_bf16 v[28:31], v[56:59], a[236:239], v[28:31]\n"
    "s_waitcnt vmcnt(16)\n"
    "v_mfma_f32_16x16x32_bf16 v[28:31], v[60:63], a[252:255], v[28:31]\n"
    "s_waitcnt lgkmcnt(0)\n"
    "s_barrier\n"
    "v_mfma_f32_16x16x32_bf16 v[176:179], v[64:67], a[0:3], 0\n"
    "ds_read_b64_tr_b16 v[96:97], v232 offset:16384\n"
    "s_add_u32 m0, s59, 0\n"
    "v_mfma_f32_16x16x32_bf16 v[176:179], v[68:71], a[16:19], v[176:179]\n"
    "buffer_load_dwordx4 v220, s[48:51], s56 offen offset:0 lds\n"
    "v_mfma_f32_16x16x32_bf16 v[176:179], v[72:75], a[32:35], v[176:179]\n"
    "ds_read_b64_tr_b16 v[98:99], v232 offset:18432\n"
    "v_mfma_f32_16x16x32_bf16 v[176:179], v[76:79], a[48:51], v[176:179]\n"
    "ds_read_b64_tr_b16 v[100:101], v232 offset:20480\n"
    "v_mfma_f32_16x16x32_bf16 v[180:183], v[64:67], a[4:7], 0\n"
    "ds_read_b64_tr_b16 v[102:103], v232 offset:22528\n"
    "v_mfma_f32_16x16x32_bf16 v[180:183], v[68:71], a[20:23], v[180:183]\n"
    "buffer_load_dwordx4 v221, s[48:51], s56 offen offset:1024 lds\n"
    "v_mfma_f32_16x16x32_bf16 v[180:183], v[72:75], a[36:39], v[180:183]\n"
    "ds_read_b64_tr_b16 v[104:105], v232 offset:24576\n"
    "v_mfma_f32_16x16x32_bf16 v[180:183], v[76:79], a[52:55], v[180:183]\n"
    "ds_read_b64_tr_b16 v[106:107], v232 offset:26624\n"
    "v_mfma_f32_16x16x32_bf16 v[184:187], v[64:67], a[8:11], 0\n"
    "ds_read_b64_tr_b16 v[108:109], v232 offset:28672\n"
    "v_mfma_f32_16x16x32_bf16 v[184:187], v[68:71], a[24:27], v[184:187]\n"
    "buffer_load_dwordx4 v222, s[48:51], s56 offen offset:2048 lds\n"
    "v_mfma_f32_16x16x32_bf16 v[184:187], v[72:75], a[40:43], v[184:187]\n"
    "ds_read_b64_tr_b16 v[110:111], v232 offset:30720\n"
    "v_mfma_f32_16x16x32_bf16 v[184:187], v[76:79], a[56:59], v[184:187]\n"
    "v_mfma_f32_16x16x32_bf16 v[188:191], v[64:67], a[12:15], 0\n"
    "v_mfma_f32_16x16x32_bf16 v[188:191], v[68:71], a[28:31], v[188:191]\n"
    "buffer_load_dwordx4 v223, s[48:51], s56 offen offset:3072 lds\n"
    "v_mfma_f32_16x16x32_bf16 v[188:191], v[72:75], a[44:47], v[188:191]\n"
    "s_waitcnt vmcnt(16)\n"
    "s_add_u32 s56, s56, 0x4000\n"
    "v_mfma_f32_16x16x32_bf16 v[188:191], v[76:79], a[60:63], v[188:191]\n"
    "s_waitcnt lgkmcnt(0)\n"
    "s_barrier\n"
    "v_mfma_f32_16x16x32_bf16 v[176:179], v[80:83], a[64:67], v[176:179]\n"
    "ds_read_b64_tr_b16 v[48:49], v232 offset:32768\n"
    "s_add_u32 m0, s59, 16384\n"
    "v_mfma_f32_16x16x32_bf16 v[176:179], v[84:87], a[80:83], v[176:179]\n"
    "buffer_load_dwordx4 v220, s[48:51], s56 offen offset:0 lds\n"
    "v_mfma_f32_16x16x32_bf16 v[176:179], v[88:91], a[96:99], v[176:179]\n"
    "ds_read_b64_tr_b16 v[50:51], v232 offset:34816\n"
    "v_cvt_pk_bf16_f32 v40, v16, v17\n"
    "v_mfma_f32_16x16x32_bf16 v[176:179], v[92:95], a[112:115], v[176:179]\n"
    "ds_read_b64_tr_b16 v[52:53], v232 offset:36864\n"
    "v_cvt_pk_bf16_f32 v41, v18, v19\n"
    "v_mfma_f32_16x16x32_bf16 v[176:179], v[96:99], a[192:195], v[176:179]\n"
    "ds_read_b64_tr_b16 v[54:55], v232 offset:38912\n"
    "v_cvt_pk_bf16_f32 v42, v20, v21\n"
    "v_mfma_f32_16x16x32_bf16 v[176:179], v[100:103], a[208:211], v[176:179]\n"
    "buffer_load_dwordx4 v221, s[48:51], s56 offen offset:1024 lds\n"
    "v_mfma_f32_16x16x32_bf16 v[176:179], v[104:107], a[224:227], v[176:179]\n"
    "ds_read_b64_tr_b16 v[56:57], v232 offset:40960\n"
    "v_cvt_pk_bf16_f32 v43, v22, v23\n"
    "v_mfma_f32_16x16x32_bf16 v[176:179], v[108:111], a[240:243], v[176:179]\n"
    "ds_read_b64_tr_b16 v[58:59], v232 offset:43008\n"
    "v_cvt_pk_bf16_f32 v44, v24, v25\n"
    "v_mfma_f32_16x16x32_bf16 v[180:183], v[80:83], a[68:71], v[180:183]\n"
    "ds_read_b64_tr_b16 v[60:61], v232 offset:45056\n"
    "v_cvt_pk_bf16_f32 v45, v26, v27\n"
    "v_mfma_f32_16x16x32_bf16 v[180:183], v[84:87], a[84:87], v[180:183]\n"
    "buffer_load_dwordx4 v222, s[48:51], s56 offen offset:2048 lds\n"
    "v_mfma_f32_16x16x32_bf16 v[180:183], v[88:91], a[100:103], v[180:183]\n"
    "ds_read_b64_tr_b16 v[62:63], v232 offset:47104\n"
    "v_cvt_pk_bf16_f32 v46, v28, v29\n"
    "v_mfma_f32_16x16x32_bf16 v[180:183], v[92:95], a[116:119], v[180:183]\n"
    "v_cvt_pk_bf16_f32 v47, v30, v31\n"
    "v_mfma_f32_16x16x32_bf16 v[180:183], v[96:99], a[196:199], v[180:183]\n"
    "v_mfma_f32_16x16x32_bf16 v[180:183], v[100:103], a[212:215], v[180:183]\n"
    "buffer_load_dwordx4 v223, s[48:51], s56 offen offset:3072 lds\n"
    "v_mfma_f32_16x16x32_bf16 v[180:183], v[104:107], a[228:231], v[180:183]\n"
    "s_waitcnt vmcnt(16)\n"
    "s_add_u32 s56, s56, 0x4000\n"
    "v_mfma_f32_16x16x32_bf16 v[180:183], v[108:111], a[244:247], v[180:183]\n"
    "s_waitcnt lgkmcnt(0)\n"
    "s_barrier\n"
    "v_mfma_f32_16x16x32_bf16 v[184:187], v[80:83], a[72:75], v[184:187]\n"
    "ds_read_b64_tr_b16 v[64:65], v232 offset:49152\n"
    "s_add_u32 m0, s59, 32768\n"
    "v_mfma_f32_16x16x32_bf16 v[184:187], v[84:87], a[88:91], v[184:187]\n"
    "buffer_load_dwordx4 v220, s[48:51], s56 offen offset:0 lds\n"
    "v_mfma_f32_16x16x32_bf16 v[184:187], v[88:91], a[104:107], v[184:187]\n"
    "ds_read_b64_tr_b16 v[66:67], v232 offset:51200\n"
    "v_mfma_f32_16x16x32_bf16 v[184:187], v[92:95], a[120:123], v[184:187]\n"
    "ds_read_b64_tr_b16 v[68:69], v232 offset:53248\n"
    "v_mfma_f32_16x16x32_bf16 v[184:187], v[96:99], a[200:203], v[184:187]\n"
    "ds_read_b64_tr_b16 v[70:71], v232 offset:55296\n"
    "v_mfma_f32_16x16x32_bf16 v[184:187], v[100:103], a[216:219], v[184:187]\n"
    "buffer_load_dwordx4 v221, s[48:51], s56 offen offset:1024 lds\n"
    "v_mfma_f32_16x16x32_bf16 v[184:187], v[104:107], a[232:235], v[184:187]\n"
    "ds_read_b64_tr_b16 v[72:73], v232 offset:57344\n"
    "v_mfma_f32_16x16x32_bf16 v[184:187], v[108:111], a[248:251], v[184:187]\n"
    "ds_read_b64_tr_b16 v[74:75], v232 offset:59392\n"
    "v_mfma_f32_16x16x32_bf16 v[188:191], v[80:83], a[76:79], v[188:191]\n"
    "ds_read_b64_tr_b16 v[76:77], v232 offset:61440\n"
    "v_mfma_f32_16x16x32_bf16 v[188:191], v[84:87], a[92:95], v[188:191]\n"
    "buffer_load_dwordx4 v222, s[48:51], s56 offen offset:2048 lds\n"
    "v_mfma_f32_16x16x32_bf16 v[188:191], v[88:91], a[108:111], v[188:191]\n"
    "ds_read_b64_tr_b16 v[78:79], v232 offset:63488\n"
    "v_mfma_f32_16x16x32_bf16 v[188:191], v[92:95], a[124:127], v[188:191]\n"
    "v_mfma_f32_16x16x32_bf16 v[188:191], v[96:99], a[204:207], v[188:191]\n"
    "v_mfma_f32_16x16x32_bf16 v[188:191], v[100:103], a[220:223], v[188:191]\n"
    "buffer_load_dwordx4 v223, s[48:51], s56 offen offset:3072 lds\n"
    "v_mfma_f32_16x16x32_bf16 v[188:191], v[104:107], a[236:239], v[188:191]\n"
    "s_waitcnt vmcnt(16)\n"
    "s_add_u32 s56, s56, 0x4000\n"
    "v_mfma_f32_16x16x32_bf16 v[188:191], v[108:111], a[252:255], v[188:191]\n"
    "s_waitcnt lgkmcnt(0)\n"
    "s_barrier\n"
    "v_mfma_f32_16x16x32_bf16 v[176:179], v[48:51], v[112:115], v[176:179]\n"
    "ds_read_b64_tr_b16 v[80:81], v233 offset:0\n"
    "s_add_u32 m0, s59, 49152\n"
    "ds_write_b64 v234, v[144:145] offset:0\n"
    "v_mfma_f32_16x16x32_bf16 v[176:179], v[52:55], v[128:131], v[176:179]\n"
    "ds_write_b64 v234, v[146:147] offset:8192\n"
    "buffer_load_dwordx4 v220, s[48:51], s56 offen offset:0 lds\n"
    "v_mfma_f32_16x16x32_bf16 v[180:183], v[48:51], v[116:119], v[180:183]\n"
    "ds_read_b64_tr_b16 v[82:83], v233 offset:2048\n"
    "ds_write_b64 v234, v[148:149] offset:16384\n"
    "v_mfma_f32_16x16x32_bf16 v[180:183], v[52:55], v[132:135], v[180:183]\n"
    "ds_read_b64_tr_b16 v[84:85], v233 offset:4096\n"
    "ds_write_b64 v234, v[150:151] offset:24576\n"
    "v_mfma_f32_16x16x32_bf16 v[184:187], v[48:51], v[120:123], v[184:187]\n"
    "ds_read_b64_tr_b16 v[86:87], v233 offset:6144\n"
    "ds_write_b64 v234, v[152:153] offset:32768\n"
    "v_mfma_f32_16x16x32_bf16 v[184:187], v[52:55], v[136:139], v[184:187]\n"
    "ds_write_b64 v234, v[154:155] offset:40960\n"
    "buffer_load_dwordx4 v221, s[48:51], s56 offen offset:1024 lds\n"
    "v_mfma_f32_16x16x32_bf16 v[188:191], v[48:51], v[124:127], v[188:191]\n"
    "ds_read_b64_tr_b16 v[88:89], v233 offset:8192\n"
    "ds_write_b64 v234, v[156:157] offset:49152\n"
    "v_mfma_f32_16x16x32_bf16 v[188:191], v[52:55], v[140:143], v[188:191]\n"
    "ds_read_b64_tr_b16 v[90:91], v233 offset:10240\n"
    "ds_write_b64 v234, v[158:159] offset:57344\n"
    "v_mfma_f32_16x16x32_bf16 v[192:195], v[56:59], v[112:115], 0\n"
    "ds_read_b64_tr_b16 v[92:93], v233 offset:12288\n"
    "ds_write_b64 v235, v[160:161] offset:0\n"
    "v_mfma_f32_16x16x32_bf16 v[192:195], v[60:63], v[128:131], v[192:195]\n"
    "ds_write_b64 v235, v[162:163] offset:8192\n"
    "buffer_load_dwordx4 v222, s[48:51], s56 offen offset:2048 lds\n"
    "v_mfma_f32_16x16x32_bf16 v[192:195], v[64:67], a[0:3], v[192:195]\n"
    "ds_read_b64_tr_b16 v[94:95], v233 offset:14336\n"
    "v_mfma_f32_16x16x32_bf16 v[192:195], v[68:71], a[16:19], v[192:195]\n"
    "ds_write_b64 v235, v[164:165] offset:16384\n"
    "v_mfma_f32_16x16x32_bf16 v[192:195], v[72:75], a[32:35], v[192:195]\n"
    "ds_write_b64 v235, v[166:167] offset:24576\n"
    "v_mfma_f32_16x16x32_bf16 v[192:195], v[76:79], a[48:51], v[192:195]\n"
    "buffer_load_dwordx4 v223, s[48:51], s56 offen offset:3072 lds\n"
    "v_mfma_f32_16x16x32_bf16 v[196:199], v[56:59], v[116:119], 0\n"
    "s_waitcnt vmcnt(16)\n"
    "s_add_u32 s56, s56, 0x4000\n"
    "s_and_b32 s56, s56, 0xfffff\n"
    "v_mfma_f32_16x16x32_bf16 v[196:199], v[60:63], v[132:135], v[196:199]\n"
    "s_waitcnt lgkmcnt(0)\n"
    "s_barrier\n"
    "v_mfma_f32_16x16x32_bf16 v[196:199], v[64:67], a[4:7], v[196:199]\n"
    "ds_read_b64_tr_b16 v[96:97], v233 offset:16384\n"
    "ds_write_b64 v235, v[168:169] offset:32768\n"
    "v_mfma_f32_16x16x32_bf16 v[196:199], v[68:71], a[20:23], v[196:199]\n"
    "ds_read_b64_tr_b16 v[98:99], v233 offset:18432\n"
    "ds_write_b64 v235, v[170:171] offset:40960\n"
    "v_mfma_f32_16x16x32_bf16 v[196:199], v[72:75], a[36:39], v[196:199]\n"
    "ds_read_b64_tr_b16 v[100:101], v233 offset:20480\n"
    "ds_write_b64 v235, v[172:173] offset:49152\n"
    "v_mfma_f32_16x16x32_bf16 v[196:199], v[76:79], a[52:55], v[196:199]\n"
    "ds_read_b64_tr_b16 v[102:103], v233 offset:22528\n"
    "ds_write_b64 v235, v[174:175] offset:57344\n"
    "v_mfma_f32_16x16x32_bf16 v[200:203], v[56:59], v[120:123], 0\n"
    "ds_read_b64_tr_b16 v[104:105], v233 offset:24576\n"
    "ds_write_b64 v234, v[32:33] offset:256\n"
    "v_mfma_f32_16x16x32_bf16 v[200:203], v[60:63], v[136:139], v[200:203]\n"
    "ds_read_b64_tr_b16 v[106:107], v233 offset:26624\n"
    "ds_write_b64 v234, v[34:35] offset:8448\n"
    "v_mfma_f32_16x16x32_bf16 v[200:203], v[64:67], a[8:11], v[200:203]\n"
    "ds_read_b64_tr_b16 v[108:109], v233 offset:28672\n"
    "ds_write_b64 v234, v[36:37] offset:16640\n"
    "v_mfma_f32_16x16x32_bf16 v[200:203], v[68:71], a[24:27], v[200:203]\n"
    "ds_read_b64_tr_b16 v[110:111], v233 offset:30720\n"
    "v_mfma_f32_16x16x32_bf16 v[200:203], v[72:75], a[40:43], v[200:203]\n"
    "ds_write_b64 v234, v[38:39] offset:24832\n"
    "v_mfma_f32_16x16x32_bf16 v[200:203], v[76:79], a[56:59], v[200:203]\n"
    "ds_write_b64 v234, v[40:41] offset:33024\n"
    "v_mfma_f32_16x16x32_bf16 v[204:207], v[56:59], v[124:127], 0\n"
    "ds_write_b64 v234, v[42:43] offset:41216\n"
    "v_mfma_f32_16x16x32_bf16 v[204:207], v[60:63], v[140:143], v[204:207]\n"
    "ds_write_b64 v234, v[44:45] offset:49408\n"
    "v_mfma_f32_16x16x32_bf16 v[204:207], v[64:67], a[12:15], v[204:207]\n"
    "ds_write_b64 v234, v[46:47] offset:57600\n"
    "v_mfma_f32_16x16x32_bf16 v[204:207], v[68:71], a[28:31], v[204:207]\n"
    "v_mfma_f32_16x16x32_bf16 v[204:207], v[72:75], a[44:47], v[204:207]\n"
    "s_waitcnt vmcnt(12)\n"
    "v_mfma_f32_16x16x32_bf16 v[204:207], v[76:79], a[60:63], v[204:207]\n"
    "s_waitcnt lgkmcnt(0)\n"
    "s_barrier\n"
    "v_mfma_f32_16x16x32_bf16 v[192:195], v[80:83], a[64:67], v[192:195]\n"
    "ds_read_b128 v[112:115], v212 offset:0\n"
    "ds_read_b128 v[116:119], v213 offset:0\n"
    "v_mfma_f32_16x16x32_bf16 v[192:195], v[84:87], a[80:83], v[192:195]\n"
    "ds_read_b128 v[120:123], v214 offset:0\n"
    "ds_read_b128 v[124:127], v215 offset:0\n"
    "v_mfma_f32_16x16x32_bf16 v[192:195], v[88:91], a[96:99], v[192:195]\n"
    "ds_read_b128 v[128:131], v212 offset:256\n"
    "v_cvt_pk_bf16_f32 v0, v176, v177\n"
    "v_mfma_f32_16x16x32_bf16 v[192:195], v[92:95], a[112:115], v[192:195]\n"
    "ds_read_b128 v[132:135], v213 offset:256\n"
    "v_cvt_pk_bf16_f32 v1, v178, v179\n"
    "v_mfma_f32_16x16x32_bf16 v[192:195], v[96:99], a[192:195], v[192:195]\n"
    "ds_read_b128 v[136:139], v214 offset:256\n"
    "v_cvt_pk_bf16_f32 v2, v180, v181\n"
    "v_mfma_f32_16x16x32_bf16 v[192:195], v[100:103], a[208:211], v[192:195]\n"
    "ds_read_b128 v[140:143], v215 offset:256\n"
    "v_cvt_pk_bf16_f32 v3, v182, v183\n"
    "v_mfma_f32_16x16x32_bf16 v[192:195], v[104:107], a[224:227], v[192:195]\n"
    "ds_read_b128 a[128:131], v236 offset:0\n"
    "v_cvt_pk_bf16_f32 v4, v184, v185\n"
    "v_mfma_f32_16x16x32_bf16 v[192:195], v[108:111], a[240:243], v[192:195]\n"
    "ds_read_b128 a[160:163], v236 offset:8192\n"
    "v_cvt_pk_bf16_f32 v5, v186, v187\n"
    "v_mfma_f32_16x16x32_bf16 v[196:199], v[80:83], a[68:71], v[196:199]\n"
    "ds_read_b128 a[132:135], v237 offset:0\n"
    "v_cvt_pk_bf16_f32 v6, v188, v189\n"
    "v_mfma_f32_16x16x32_bf16 v[196:199], v[84:87], a[84:87], v[196:199]\n"
    "ds_read_b128 a[164:167], v237 offset:8192\n"
    "v_cvt_pk_bf16_f32 v7, v190, v191\n"
    "v_mfma_f32_16x16x32_bf16 v[196:199], v[88:91], a[100:103], v[196:199]\n"
    "ds_read_b128 a[136:139], v238 offset:0\n"
    "ds_write_b64 v235, v[0:1] offset:256\n"
    "v_mfma_f32_16x16x32_bf16 v[196:199], v[92:95], a[116:119], v[196:199]\n"
    "ds_read_b128 a[168:171], v238 offset:8192\n"
    "v_mfma_f32_16x16x32_bf16 v[196:199], v[96:99], a[196:199], v[196:199]\n"
    "ds_write_b64 v235, v[2:3] offset:8448\n"
    "v_mfma_f32_16x16x32_bf16 v[196:199], v[100:103], a[212:215], v[196:199]\n"
    "ds_write_b64 v235, v[4:5] offset:16640\n"
    "v_mfma_f32_16x16x32_bf16 v[196:199], v[104:107], a[228:231], v[196:199]\n"
    "s_waitcnt vmcnt(8)\n"
    "ds_write_b64 v235, v[6:7] offset:24832\n"
    "v_mfma_f32_16x16x32_bf16 v[196:199], v[108:111], a[244:247], v[196:199]\n"
    "s_waitcnt lgkmcnt(3)\n"
    "s_barrier\n"
    "v_mfma_f32_16x16x32_bf16 v[200:203], v[80:83], a[72:75], v[200:203]\n"
    "ds_read_b128 v[144:147], v212 offset:8192\n"
    "ds_read_b128 v[148:151], v213 offset:8192\n"
    "v_mfma_f32_16x16x32_bf16 v[200:203], v[84:87], a[88:91], v[200:203]\n"
    "ds_read_b128 v[152:155], v214 offset:8192\n"
    "ds_read_b128 v[156:159], v215 offset:8192\n"
    "v_mfma_f32_16x16x32_bf16 v[200:203], v[88:91], a[104:107], v[200:203]\n"
    "ds_read_b128 v[160:163], v212 offset:8448\n"
    "v_mfma_f32_16x16x32_bf16 v[200:203], v[92:95], a[120:123], v[200:203]\n"
    "ds_read_b128 v[164:167], v213 offset:8448\n"
    "v_mfma_f32_16x16x32_bf16 v[200:203], v[96:99], a[200:203], v[200:203]\n"
    "ds_read_b128 v[168:171], v214 offset:8448\n"
    "v_mfma_f32_16x16x32_bf16 v[200:203], v[100:103], a[216:219], v[200:203]\n"
    "ds_read_b128 v[172:175], v215 offset:8448\n"
    "v_mfma_f32_16x16x32_bf16 v[200:203], v[104:107], a[232:235], v[200:203]\n"
    "ds_read_b128 a[140:143], v239 offset:0\n"
    "v_mfma_f32_16x16x32_bf16 v[200:203], v[108:111], a[248:251], v[200:203]\n"
    "ds_read_b128 a[172:175], v239 offset:8192\n"
    "v_mfma_f32_16x16x32_bf16 v[204:207], v[80:83], a[76:79], v[204:207]\n"
    "ds_read_b128 a[144:147], v236 offset:256\n"
    "v_mfma_f32_16x16x32_bf16 v[204:207], v[84:87], a[92:95], v[204:207]\n"
    "ds_read_b128 a[176:179], v236 offset:8448\n"
    "v_mfma_f32_16x16x32_bf16 v[204:207], v[88:91], a[108:111], v[204:207]\n"
    "ds_read_b128 a[148:151], v237 offset:256\n"
    "v_mfma_f32_16x16x32_bf16 v[204:207], v[92:95], a[124:127], v[204:207]\n"
    "ds_read_b128 a[180:183], v237 offset:8448\n"
    "v_mfma_f32_16x16x32_bf16 v[204:207], v[96:99], a[204:207], v[204:207]\n"
    "v_mfma_f32_16x16x32_bf16 v[204:207], v[100:103], a[220:223], v[204:207]\n"
    "v_mfma_f32_16x16x32_bf16 v[204:207], v[104:107], a[236:239], v[204:207]\n"
    "s_waitcnt vmcnt(4)\n"
    "v_mfma_f32_16x16x32_bf16 v[204:207], v[108:111], a[252:255], v[204:207]\n"
    "s_waitcnt lgkmcnt(0)\n"
    "s_barrier\n"
    "v_mfma_f32_16x16x32_bf16 v[80:83], v[112:115], a[128:131], 0\n"
    "v_accvgpr_write_b32 a0, 0\n"
    "v_accvgpr_write_b32 a1, 0\n"
    "v_accvgpr_write_b32 a2, 0\n"
    "v_mfma_f32_16x16x32_bf16 v[80:83], v[116:119], a[132:135], v[80:83]\n"
    "v_accvgpr_write_b32 a3, 0\n"
    "v_accvgpr_write_b32 a4, 0\n"
    "v_accvgpr_write_b32 a5, 0\n"
    "v_mfma_f32_16x16x32_bf16 v[80:83], v[120:123], a[136:139], v[80:83]\n"
    "s_nop 0\n"
    "v_accvgpr_write_b32 a6, 0\n"
    "v_accvgpr_write_b32 a7, 0\n"
    "v_cvt_pk_bf16_f32 v8, v192, v193\n"
    "v_mfma_f32_16x16x32_bf16 v[80:83], v[124:127], a[140:143], v[80:83]\n"
    "v_accvgpr_write_b32 a8, 0\n"
    "v_cvt_pk_bf16_f32 v9, v194, v195\n"
    "v_cvt_pk_bf16_f32 v10, v196, v197\n"
    "v_mfma_f32_16x16x32_bf16 v[80:83], v[128:131], a[144:147], v[80:83]\n"
    "v_accvgpr_write_b32 a9, 0\n"
    "v_cvt_pk_bf16_f32 v11, v198, v199\n"
    "v_cvt_pk_bf16_f32 v12, v200, v201\n"
    "v_mfma_f32_16x16x32_bf16 v[80:83], v[132:135], a[148:151], v[80:83]\n"
    "v_accvgpr_write_b32 a10, 0\n"
    "v_cvt_pk_bf16_f32 v13, v202, v203\n"
    "v_cvt_pk_bf16_f32 v14, v204, v205\n"
    "v_mfma_f32_16x16x32_bf16 v[84:87], v[112:115], a[160:163], 0\n"
    "v_accvgpr_write_b32 a11, 0\n"
    "v_accvgpr_write_b32 a12, 0\n"
    "v_cvt_pk_bf16_f32 v15, v206, v207\n"
    "v_mfma_f32_16x16x32_bf16 v[84:87], v[116:119], a[164:167], v[84:87]\n"
    "ds_write_b64 v235, v[8:9] offset:33024\n"
    "v_mfma_f32_16x16x32_bf16 v[84:87], v[120:123], a[168:171], v[84:87]\n"
    "ds_write_b64 v235, v[10:11] offset:41216\n"
    "v_mfma_f32_16x16x32_bf16 v[84:87], v[124:127], a[172:175], v[84:87]\n"
    "ds_write_b64 v235, v[12:13] offset:49408\n"
    "v_mfma_f32_16x16x32_bf16 v[84:87], v[128:131], a[176:179], v[84:87]\n"
    "s_add_u32 m0, s59, 65536\n"
    "ds_write_b64 v235, v[14:15] offset:57600\n"
    "v_mfma_f32_16x16x32_bf16 v[84:87], v[132:135], a[180:183], v[84:87]\n"
    "buffer_load_dwordx4 v224, s[88:91], s57 offen offset:0 lds\n"
    "v_mfma_f32_16x16x32_bf16 v[88:91], v[144:147], a[128:131], 0\n"
    "buffer_load_dwordx4 v225, s[88:91], s57 offen offset:1024 lds\n"
    "v_mfma_f32_16x16x32_bf16 v[88:91], v[148:151], a[132:135], v[88:91]\n"
    "s_waitcnt lgkmcnt(0)\n"
    "v_accvgpr_write_b32 a13, 0\n"
    "s_barrier\n"
    "v_mfma_f32_16x16x32_bf16 v[88:91], v[152:155], a[136:139], v[88:91]\n"
    "buffer_load_dwordx4 v226, s[88:91], s57 offen offset:2048 lds\n"
    "ds_read_b128 a[152:155], v238 offset:256\n"
    "v_mfma_f32_16x16x32_bf16 v[88:91], v[156:159], a[140:143], v[88:91]\n"
    "ds_read_b128 a[184:187], v238 offset:8448\n"
    "v_accvgpr_write_b32 a14, 0\n"
    "v_mfma_f32_16x16x32_bf16 v[88:91], v[160:163], a[144:147], v[88:91]\n"
    "ds_read_b128 a[156:159], v239 offset:256\n"
    "v_accvgpr_write_b32 a15, 0\n"
    "v_mfma_f32_16x16x32_bf16 v[88:91], v[164:167], a[148:151], v[88:91]\n"
    "ds_read_b128 a[188:191], v239 offset:8448\n"
    "v_accvgpr_write_b32 a16, 0\n"
    "v_mfma_f32_16x16x32_bf16 v[92:95], v[144:147], a[160:163], 0\n"
    "ds_read_b128 v[112:115], v212 offset:16384\n"
    "v_accvgpr_write_b32 a17, 0\n"
    "v_mfma_f32_16x16x32_bf16 v[92:95], v[148:151], a[164:167], v[92:95]\n"
    "ds_read_b128 v[116:119], v213 offset:16384\n"
    "v_accvgpr_write_b32 a18, 0\n"
    "v_mfma_f32_16x16x32_bf16 v[92:95], v[152:155], a[168:171], v[92:95]\n"
    "ds_read_b128 v[120:123], v214 offset:16384\n"
    "v_accvgpr_write_b32 a19, 0\n"
    "v_mfma_f32_16x16x32_bf16 v[92:95], v[156:159], a[172:175], v[92:95]\n"
    "ds_read_b128 v[124:127], v215 offset:16384\n"
    "v_accvgpr_write_b32 a20, 0\n"
    "v_mfma_f32_16x16x32_bf16 v[92:95], v[160:163], a[176:179], v[92:95]\n"
    "ds_read_b128 v[128:131], v212 offset:16640\n"
    "v_accvgpr_write_b32 a21, 0\n"
    "v_mfma_f32_16x16x32_bf16 v[92:95], v[164:167], a[180:183], v[92:95]\n"
    "s_waitcnt lgkmcnt(5)\n"
    "ds_read_b128 v[132:135], v213 offset:16640\n"
    "v_accvgpr_write_b32 a22, 0\n"
    "v_accvgpr_write_b32 a23, 0\n"
    "v_mfma_f32_16x16x32_bf16 v[80:83], v[136:139], a[152:155], v[80:83]\n"
    "ds_read_b128 v[144:147], v212 offset:24576\n"
    "v_accvgpr_write_b32 a24, 0\n"
    "v_mfma_f32_16x16x32_bf16 v[80:83], v[140:143], a[156:159], v[80:83]\n"
    "ds_read_b128 v[148:151], v213 offset:24576\n"
    "v_accvgpr_write_b32 a25, 0\n"
    "v_mfma_f32_16x16x32_bf16 v[84:87], v[136:139], a[184:187], v[84:87]\n"
    "ds_read_b128 v[152:155], v214 offset:24576\n"
    "v_accvgpr_write_b32 a26, 0\n"
    "v_mfma_f32_16x16x32_bf16 v[84:87], v[140:143], a[188:191], v[84:87]\n"
    "ds_read_b128 v[156:159], v215 offset:24576\n"
    "v_accvgpr_write_b32 a27, 0\n"
    "v_mfma_f32_16x16x32_bf16 v[88:91], v[168:171], a[152:155], v[88:91]\n"
    "ds_read_b128 v[136:139], v214 offset:16640\n"
    "v_accvgpr_write_b32 a28, 0\n"
    "v_mfma_f32_16x16x32_bf16 v[88:91], v[172:175], a[156:159], v[88:91]\n"
    "ds_read_b128 v[140:143], v215 offset:16640\n"
    "v_accvgpr_write_b32 a29, 0\n"
    "v_mfma_f32_16x16x32_bf16 v[92:95], v[168:171], a[184:187], v[92:95]\n"
    "buffer_load_dwordx4 v227, s[88:91], s57 offen offset:3072 lds\n"
    "v_mfma_f32_16x16x32_bf16 v[92:95], v[172:175], a[188:191], v[92:95]\n"
    "s_waitcnt lgkmcnt(2)\n"
    "s_barrier\n"
    "v_mfma_f32_16x16x32_bf16 v[96:99], v[112:115], a[128:131], 0\n"
    "s_cmp_lt_u32 %[wv], 2\n"
    "s_cselect_b64 s[44:45], %[wg], %[wu]\n"
    "s_add_u32 m0, s59, 0\n"
    "ds_read_b128 v[160:163], v212 offset:24832\n"
    "v_mfma_f32_16x16x32_bf16 v[96:99], v[116:119], a[132:135], v[96:99]\n"
    "s_lshr_b32 s61, s67, 2\n"
    "s_lshl_b32 s61, s61, 3\n"
    "s_add_u32 s61, s61, s66\n"
    "ds_read_b128 v[164:167], v213 offset:24832\n"
    "v_mfma_f32_16x16x32_bf16 v[96:99], v[120:123], a[136:139], v[96:99]\n"
    "s_cmp_lt_u32 s61, 28\n"
    "s_cselect_b32 s46, 1605632, 0\n"
    "ds_read_b128 v[168:171], v214 offset:24832\n"
    "v_mfma_f32_16x16x32_bf16 v[96:99], v[124:127], a[140:143], v[96:99]\n"
    "s_lshl_b32 s64, s61, 7\n"
    "ds_read_b128 v[172:175], v215 offset:24832\n"
    "v_max3_f32 v230, v80, v81, v82\n"
    "v_max_f32 v230, v230, v83\n"
    "v_mfma_f32_16x16x32_bf16 v[96:99], v[128:131], a[144:147], v[96:99]\n"
    "buffer_load_dwordx4 v220, s[48:51], s56 offen offset:0 lds\n"
    "v_max3_f32 v231, v84, v85, v86\n"
    "v_mfma_f32_16x16x32_bf16 v[96:99], v[132:135], a[148:151], v[96:99]\n"
    "s_waitcnt lgkmcnt(4)\n"
    "buffer_load_dwordx4 v221, s[48:51], s56 offen offset:1024 lds\n"
    "v_max_f32 v231, v231, v87\n"
    "v_mfma_f32_16x16x32_bf16 v[96:99], v[136:139], a[152:155], v[96:99]\n"
    "buffer_load_dwordx4 v222, s[48:51], s56 offen offset:2048 lds\n"
    "v_max3_f32 v230, v230, v88, v89\n"
    "v_mfma_f32_16x16x32_bf16 v[96:99], v[140:143], a[156:159], v[96:99]\n"
    "buffer_load_dwordx4 v223, s[48:51], s56 offen offset:3072 lds\n"
    "v_max3_f32 v230, v230, v90, v91\n"
    "v_mfma_f32_16x16x32_bf16 v[100:103], v[112:115], a[160:163], 0\n"
    "s_add_u32 s56, s56, 0x4000\n"
    "v_max3_f32 v231, v231, v92, v93\n"
    "v_max3_f32 v231, v231, v94, v95\n"
    "s_add_u32 s57, s57, 128\n"
    "v_mfma_f32_16x16x32_bf16 v[100:103], v[116:119], a[164:167], v[100:103]\n"
    "s_and_b32 s61, s67, 3\n"
    "v_accvgpr_write_b32 a30, 0\n"
    "v_accvgpr_write_b32 a31, 0\n"
    "s_add_u32 m0, s59, 81920\n"
    "v_mfma_f32_16x16x32_bf16 v[100:103], v[120:123], a[168:171], v[100:103]\n"
    "buffer_load_dwordx4 v224, s[88:91], s57 offen offset:0 lds\n"
    "v_accvgpr_write_b32 a32, 0\n"
    "v_mfma_f32_16x16x32_bf16 v[100:103], v[124:127], a[172:175], v[100:103]\n"
    "buffer_load_dwordx4 v225, s[88:91], s57 offen offset:1024 lds\n"
    "v_accvgpr_write_b32 a33, 0\n"
    "v_mfma_f32_16x16x32_bf16 v[100:103], v[128:131], a[176:179], v[100:103]\n"
    "s_lshl_b32 s61, s61, 5\n"
    "ds_read_b128 v[112:115], v212 offset:32768\n"
    "v_accvgpr_write_b32 a34, 0\n"
    "v_accvgpr_write_b32 a35, 0\n"
    "v_mfma_f32_16x16x32_bf16 v[100:103], v[132:135], a[180:183], v[100:103]\n"
    "s_add_u32 s64, s64, s61\n"
    "ds_read_b128 v[116:119], v213 offset:32768\n"
    "v_accvgpr_write_b32 a36, 0\n"
    "v_accvgpr_write_b32 a37, 0\n"
    "v_mfma_f32_16x16x32_bf16 v[100:103], v[136:139], a[184:187], v[100:103]\n"
    "s_mul_i32 s70, s64, 448\n"
    "ds_read_b128 v[120:123], v214 offset:32768\n"
    "v_accvgpr_write_b32 a38, 0\n"
    "v_accvgpr_write_b32 a39, 0\n"
    "v_mfma_f32_16x16x32_bf16 v[100:103], v[140:143], a[188:191], v[100:103]\n"
    "s_waitcnt lgkmcnt(3)\n"
    "ds_read_b128 v[124:127], v215 offset:32768\n"
    "v_accvgpr_write_b32 a40, 0\n"
    "v_accvgpr_write_b32 a41, 0\n"
    "v_mfma_f32_16x16x32_bf16 v[104:107], v[144:147], a[128:131], 0\n"
    "s_and_b32 s61, %[wv], 1\n"
    "s_mul_i32 s61, s61, 114688\n"
    "ds_read_b128 v[128:131], v212 offset:33024\n"
    "v_accvgpr_write_b32 a42, 0\n"
    "v_mfma_f32_16x16x32_bf16 v[104:107], v[148:151], a[132:135], v[104:107]\n"
    "s_add_u32 s64, s64, s61\n"
    "s_lshr_b32 s61, s67, 1\n"
    "ds_read_b128 v[132:135], v213 offset:33024\n"
    "v_accvgpr_write_b32 a43, 0\n"
    "v_mfma_f32_16x16x32_bf16 v[104:107], v[152:155], a[136:139], v[104:107]\n"
    "s_and_b32 s61, s61, 4\n"
    "s_add_u32 s61, s61, s66\n"
    "ds_read_b128 v[136:139], v214 offset:33024\n"
    "v_accvgpr_write_b32 a44, 0\n"
    "v_mfma_f32_16x16x32_bf16 v[104:107], v[156:159], a[140:143], v[104:107]\n"
    "s_and_b32 s61, s61, 7\n"
    "s_lshl_b32 s68, s67, 2\n"
    "ds_read_b128 v[140:143], v215 offset:33024\n"
    "v_accvgpr_write_b32 a45, 0\n"
    "v_mfma_f32_16x16x32_bf16 v[104:107], v[160:163], a[144:147], v[104:107]\n"
    "buffer_load_dwordx4 v226, s[88:91], s57 offen offset:2048 lds\n"
    "v_mfma_f32_16x16x32_bf16 v[104:107], v[164:167], a[148:151], v[104:107]\n"
    "buffer_load_dwordx4 v227, s[88:91], s57 offen offset:3072 lds\n"
    "v_mfma_f32_16x16x32_bf16 v[104:107], v[168:171], a[152:155], v[104:107]\n"
    "s_and_b32 s96, s68, 0x18\n"
    "s_add_u32 s61, s61, s96\n"
    "v_accvgpr_write_b32 a46, 0\n"
    "v_accvgpr_write_b32 a47, 0\n"
    "v_mfma_f32_16x16x32_bf16 v[104:107], v[172:175], a[156:159], v[104:107]\n"
    "s_add_u32 s61, s61, 14\n"
    "s_and_b32 s68, s68, 4\n"
    "v_accvgpr_write_b32 a48, 0\n"
    "v_accvgpr_write_b32 a49, 0\n"
    "v_mfma_f32_16x16x32_bf16 v[108:111], v[144:147], a[160:163], 0\n"
    "v_accvgpr_write_b32 a50, 0\n"
    "v_accvgpr_write_b32 a51, 0\n"
    "v_mfma_f32_16x16x32_bf16 v[108:111], v[148:151], a[164:167], v[108:111]\n"
    "v_accvgpr_write_b32 a52, 0\n"
    "v_accvgpr_write_b32 a53, 0\n"
    "v_mfma_f32_16x16x32_bf16 v[108:111], v[152:155], a[168:171], v[108:111]\n"
    "v_accvgpr_write_b32 a54, 0\n"
    "v_accvgpr_write_b32 a55, 0\n"
    "v_mfma_f32_16x16x32_bf16 v[108:111], v[156:159], a[172:175], v[108:111]\n"
    "v_accvgpr_write_b32 a56, 0\n"
    "v_accvgpr_write_b32 a57, 0\n"
    "v_mfma_f32_16x16x32_bf16 v[108:111], v[160:163], a[176:179], v[108:111]\n"
    "ds_read_b128 v[144:147], v212 offset:40960\n"
    "v_accvgpr_write_b32 a58, 0\n"
    "v_mfma_f32_16x16x32_bf16 v[108:111], v[164:167], a[180:183], v[108:111]\n"
    "ds_read_b128 v[148:151], v213 offset:40960\n"
    "v_accvgpr_write_b32 a59, 0\n"
    "v_mfma_f32_16x16x32_bf16 v[108:111], v[168:171], a[184:187], v[108:111]\n"
    "s_waitcnt vmcnt(12)\n"
    "ds_read_b128 v[152:155], v214 offset:40960\n"
    "v_accvgpr_write_b32 a60, 0\n"
    "v_accvgpr_write_b32 a61, 0\n"
    "v_mfma_f32_16x16x32_bf16 v[108:111], v[172:175], a[188:191], v[108:111]\n"
    "s_waitcnt lgkmcnt(3)\n"
    "ds_read_b128 v[156:159], v215 offset:40960\n"
    "s_barrier\n"
    "v_mfma_f32_16x16x32_bf16 v[0:3], v[112:115], a[128:131], 0\n"
    "s_add_u32 m0, s59, 16384\n"
    "ds_read_b128 v[160:163], v212 offset:41216\n"
    "v_accvgpr_write_b32 a62, 0\n"
    "v_accvgpr_write_b32 a63, 0\n"
    "v_mfma_f32_16x16x32_bf16 v[0:3], v[116:119], a[132:135], v[0:3]\n"
    "s_add_u32 s68, s68, %[wv]\n"
    "s_lshl_b32 s68, s68, 4\n"
    "ds_read_b128 v[164:167], v213 offset:41216\n"
    "v_accvgpr_write_b32 a64, 0\n"
    "v_mfma_f32_16x16x32_bf16 v[0:3], v[120:123], a[136:139], v[0:3]\n"
    "s_lshl_b32 s96, s61, 7\n"
    "s_add_u32 s96, s96, s68\n"
    "ds_read_b128 v[168:171], v214 offset:41216\n"
    "v_accvgpr_write_b32 a65, 0\n"
    "v_mfma_f32_16x16x32_bf16 v[0:3], v[124:127], a[140:143], v[0:3]\n"
    "s_lshl_b32 s68, s96, 1\n"
    "ds_read_b128 v[172:175], v215 offset:41216\n"
    "v_max3_f32 v230, v230, v96, v97\n"
    "v_max3_f32 v230, v230, v98, v99\n"
    "v_mfma_f32_16x16x32_bf16 v[0:3], v[128:131], a[144:147], v[0:3]\n"
    "buffer_load_dwordx4 v220, s[48:51], s56 offen offset:0 lds\n"
    "v_max3_f32 v231, v231, v100, v101\n"
    "v_mfma_f32_16x16x32_bf16 v[0:3], v[132:135], a[148:151], v[0:3]\n"
    "buffer_load_dwordx4 v221, s[48:51], s56 offen offset:1024 lds\n"
    "v_max3_f32 v231, v231, v102, v103\n"
    "v_mfma_f32_16x16x32_bf16 v[0:3], v[136:139], a[152:155], v[0:3]\n"
    "buffer_load_dwordx4 v222, s[48:51], s56 offen offset:2048 lds\n"
    "v_max3_f32 v230, v230, v104, v105\n"
    "v_mfma_f32_16x16x32_bf16 v[0:3], v[140:143], a[156:159], v[0:3]\n"
    "buffer_load_dwordx4 v223, s[48:51], s56 offen offset:3072 lds\n"
    "v_mfma_f32_16x16x32_bf16 v[4:7], v[112:115], a[160:163], 0\n"
    "s_add_u32 s56, s56, 0x4000\n"
    "v_max3_f32 v230, v230, v106, v107\n"
    "v_max3_f32 v231, v231, v108, v109\n"
    "s_add_u32 s57, s57, 128\n"
    "v_mfma_f32_16x16x32_bf16 v[4:7], v[116:119], a[164:167], v[4:7]\n"
    "s_add_u32 s68, s68, 4096\n"
    "v_max3_f32 v231, v231, v110, v111\n"
    "s_add_u32 m0, s59, 98304\n"
    "v_mfma_f32_16x16x32_bf16 v[4:7], v[120:123], a[168:171], v[4:7]\n"
    "buffer_load_dwordx4 v224, s[88:91], s57 offen offset:0 lds\n"
    "v_mfma_f32_16x16x32_bf16 v[4:7], v[124:127], a[172:175], v[4:7]\n"
    "buffer_load_dwordx4 v225, s[88:91], s57 offen offset:1024 lds\n"
    "v_mfma_f32_16x16x32_bf16 v[4:7], v[128:131], a[176:179], v[4:7]\n"
    "s_cmp_lt_u32 s61, 42\n"
    "s_cselect_b32 s97, 2408448, 0\n"
    "s_cselect_b32 s86, s68, 0x7f000000\n"
    "ds_read_b128 v[112:115], v212 offset:49152\n"
    "v_max3_f32 v230, v230, v0, v1\n"
    "v_mfma_f32_16x16x32_bf16 v[4:7], v[132:135], a[180:183], v[4:7]\n"
    "s_sub_u32 s61, s61, 28\n"
    "ds_read_b128 v[116:119], v213 offset:49152\n"
    "v_max3_f32 v230, v230, v2, v3\n"
    "v_mfma_f32_16x16x32_bf16 v[4:7], v[136:139], a[184:187], v[4:7]\n"
    "s_cmp_lt_u32 s61, 7\n"
    "s_cselect_b32 s82, 1.0, 0\n"
    "ds_read_b128 v[120:123], v214 offset:49152\n"
    "v_accvgpr_write_b32 a66, 0\n"
    "v_mfma_f32_16x16x32_bf16 v[4:7], v[140:143], a[188:191], v[4:7]\n"
    "s_waitcnt lgkmcnt(3)\n"
    "ds_read_b128 v[124:127], v215 offset:49152\n"
    "v_accvgpr_write_b32 a67, 0\n"
    "v_accvgpr_write_b32 a68, 0\n"
    "v_mfma_f32_16x16x32_bf16 v[8:11], v[144:147], a[128:131], 0\n"
    "s_movk_i32 s98, 5376\n"
    "ds_read_b128 v[128:131], v212 offset:49408\n"
    "v_accvgpr_write_b32 a69, 0\n"
    "v_mfma_f32_16x16x32_bf16 v[8:11], v[148:151], a[132:135], v[8:11]\n"
    "s_cmp_lt_u32 s67, 16\n"
    "s_cselect_b64 s[44:45], s[44:45], %[wmod]\n"
    "s_cselect_b32 s46, s46, s97\n"
    "ds_read_b128 v[132:135], v213 offset:49408\n"
    "v_accvgpr_write_b32 a70, 0\n"
    "v_mfma_f32_16x16x32_bf16 v[8:11], v[152:155], a[136:139], v[8:11]\n"
    "s_cmp_lt_u32 s67, 16\n"
    "s_cselect_b32 s64, s64, s96\n"
    "s_cselect_b32 s86, 0x7f000000, s86\n"
    "ds_read_b128 v[136:139], v214 offset:49408\n"
    "v_accvgpr_write_b32 a71, 0\n"
    "v_mfma_f32_16x16x32_bf16 v[8:11], v[156:159], a[140:143], v[8:11]\n"
    "s_cmp_lt_u32 s67, 16\n"
    "s_cselect_b32 s98, 3584, s98\n"
    "s_cselect_b32 s99, 1, 0\n"
    "ds_read_b128 v[140:143], v215 offset:49408\n"
    "v_max3_f32 v231, v231, v4, v5\n"
    "v_mfma_f32_16x16x32_bf16 v[8:11], v[160:163], a[144:147], v[8:11]\n"
    "buffer_load_dwordx4 v226, s[88:91], s57 offen offset:2048 lds\n"
    "v_mfma_f32_16x16x32_bf16 v[8:11], v[164:167], a[148:151], v[8:11]\n"
    "buffer_load_dwordx4 v227, s[88:91], s57 offen offset:3072 lds\n"
    "v_mfma_f32_16x16x32_bf16 v[8:11], v[168:171], a[152:155], v[8:11]\n"
    "s_and_b32 s45, s45, 0xffff\n"
    "v_accvgpr_write_b32 a72, 0\n"
    "v_max3_f32 v231, v231, v6, v7\n"
    "v_mfma_f32_16x16x32_bf16 v[8:11], v[172:175], a[156:159], v[8:11]\n"
    "s_lshl_b32 s87, s98, 6\n"
    "v_lshrrev_b32 v40, s99, v250\n"
    "v_accvgpr_write_b32 a73, 0\n"
    "v_accvgpr_write_b32 a74, 0\n"
    "v_mfma_f32_16x16x32_bf16 v[12:15], v[144:147], a[160:163], 0\n"
    "v_mul_u32_u24 v40, s98, v40\n"
    "v_accvgpr_write_b32 a75, 0\n"
    "v_accvgpr_write_b32 a76, 0\n"
    "v_mfma_f32_16x16x32_bf16 v[12:15], v[148:151], a[164:167], v[12:15]\n"
    "v_and_b32 v249, s99, v250\n"
    "v_accvgpr_write_b32 a77, 0\n"
    "v_accvgpr_write_b32 a78, 0\n"
    "v_mfma_f32_16x16x32_bf16 v[12:15], v[152:155], a[168:171], v[12:15]\n"
    "v_lshl_add_u32 v249, v249, 4, v40\n"
    "v_accvgpr_write_b32 a79, 0\n"
    "v_accvgpr_write_b32 a80, 0\n"
    "v_mfma_f32_16x16x32_bf16 v[12:15], v[156:159], a[172:175], v[12:15]\n"
    "v_accvgpr_write_b32 a81, 0\n"
    "v_max3_f32 v230, v230, v8, v9\n"
    "v_mfma_f32_16x16x32_bf16 v[12:15], v[160:163], a[176:179], v[12:15]\n"
    "ds_read_b128 v[144:147], v212 offset:57344\n"
    "v_accvgpr_write_b32 a82, 0\n"
    "v_mfma_f32_16x16x32_bf16 v[12:15], v[164:167], a[180:183], v[12:15]\n"
    "ds_read_b128 v[148:151], v213 offset:57344\n"
    "v_accvgpr_write_b32 a83, 0\n"
    "v_mfma_f32_16x16x32_bf16 v[12:15], v[168:171], a[184:187], v[12:15]\n"
    "s_waitcnt vmcnt(12)\n"
    "ds_read_b128 v[152:155], v214 offset:57344\n"
    "v_accvgpr_write_b32 a84, 0\n"
    "v_accvgpr_write_b32 a85, 0\n"
    "v_mfma_f32_16x16x32_bf16 v[12:15], v[172:175], a[188:191], v[12:15]\n"
    "s_waitcnt lgkmcnt(3)\n"
    "ds_read_b128 v[156:159], v215 offset:57344\n"
    "v_max3_f32 v230, v230, v10, v11\n"
    "s_barrier\n"
    "v_accvgpr_write_b32 a86, 0\n"
    "v_accvgpr_write_b32 a87, 0\n"
    "v_accvgpr_write_b32 a88, 0\n"
    "v_accvgpr_write_b32 a89, 0\n"
    "v_accvgpr_write_b32 a90, 0\n"
    "v_accvgpr_write_b32 a91, 0\n"
    "v_accvgpr_write_b32 a92, 0\n"
    "v_accvgpr_write_b32 a93, 0\n"
    "v_max3_f32 v231, v231, v12, v13\n"
    "v_max3_f32 v231, v231, v14, v15\n"
    "v_mov_b32 v40, v230\n"
    "v_mov_b32 v41, v231\n"
    "s_nop 0\n"
    "v_permlane32_swap_b32 v230, v40\n"
    "v_permlane32_swap_b32 v231, v41\n"
    "v_max_f32 v230, v230, v40\n"
    "v_max_f32 v231, v231, v41\n"
    "v_mov_b32 v40, v230\n"
    "v_mov_b32 v41, v231\n"
    "s_nop 0\n"
    "v_permlane16_swap_b32 v230, v40\n"
    "v_permlane16_swap_b32 v231, v41\n"
    "v_max_f32 v230, v230, v40\n"
    "v_max_f32 v231, v231, v41\n"
    "v_sub_f32 v32, 0xc2c80000, v230\n"
    "v_sub_f32 v33, 0xc2c80000, v230\n"
    "v_sub_f32 v34, 0xc2c80000, v230\n"
    "v_sub_f32 v35, 0xc2c80000, v230\n"
    "v_sub_f32 v36, 0xc2c80000, v231\n"
    "v_sub_f32 v37, 0xc2c80000, v231\n"
    "v_sub_f32 v38, 0xc2c80000, v231\n"
    "v_sub_f32 v39, 0xc2c80000, v231\n"
    "v_mfma_f32_16x16x32_bf16 v[16:19], v[112:115], a[128:131], v[32:35]\n"
    "s_add_u32 m0, s59, 32768\n"
    "ds_read_b128 v[160:163], v212 offset:57600\n"
    "v_add_f32 v80, v80, v32\n"
    "v_add_f32 v81, v81, v32\n"
    "v_add_f32 v82, v82, v32\n"
    "v_add_f32 v83, v83, v32\n"
    "v_exp_f32 v80, v80\n"
    "v_mfma_f32_16x16x32_bf16 v[16:19], v[116:119], a[132:135], v[16:19]\n"
    "s_lshl_b32 s65, %[wv], 10\n"
    "ds_read_b128 v[164:167], v213 offset:57600\n"
    "v_exp_f32 v81, v81\n"
    "v_exp_f32 v82, v82\n"
    "v_exp_f32 v83, v83\n"
    "v_add_f32 v84, v84, v36\n"
    "v_mfma_f32_16x16x32_bf16 v[16:19], v[120:123], a[136:139], v[16:19]\n"
    "s_add_u32 s65, s65, 131072\n"
    "ds_read_b128 v[168:171], v214 offset:57600\n"
    "v_add_f32 v85, v85, v36\n"
    "v_add_f32 v86, v86, v36\n"
    "v_add_f32 v87, v87, v36\n"
    "v_cvt_pk_bf16_f32 v48, v80, v81\n"
    "v_cvt_pk_bf16_f32 v49, v82, v83\n"
    "v_exp_f32 v84, v84\n"
    "v_mfma_f32_16x16x32_bf16 v[16:19], v[124:127], a[140:143], v[16:19]\n"
    "s_mov_b32 s58, 7\n"
    "ds_read_b128 v[172:175], v215 offset:57600\n"
    "v_exp_f32 v85, v85\n"
    "v_exp_f32 v86, v86\n"
    "v_exp_f32 v87, v87\n"
    "v_mfma_f32_16x16x32_bf16 v[16:19], v[128:131], a[144:147], v[16:19]\n"
    "buffer_load_dwordx4 v220, s[48:51], s56 offen offset:0 lds\n"
    "v_add_f32 v88, v88, v32\n"
    "v_add_f32 v89, v89, v32\n"
    "v_add_f32 v90, v90, v32\n"
    "v_add_f32 v91, v91, v32\n"
    "v_cvt_pk_bf16_f32 v64, v84, v85\n"
    "v_mfma_f32_16x16x32_bf16 v[16:19], v[132:135], a[148:151], v[16:19]\n"
    "buffer_load_dwordx4 v221, s[48:51], s56 offen offset:1024 lds\n"
    "v_cvt_pk_bf16_f32 v65, v86, v87\n"
    "v_exp_f32 v88, v88\n"
    "v_exp_f32 v89, v89\n"
    "v_mfma_f32_16x16x32_bf16 v[16:19], v[136:139], a[152:155], v[16:19]\n"
    "buffer_load_dwordx4 v222, s[48:51], s56 offen offset:2048 lds\n"
    "v_exp_f32 v90, v90\n"
    "v_exp_f32 v91, v91\n"
    "v_add_f32 v92, v92, v36\n"
    "v_mfma_f32_16x16x32_bf16 v[16:19], v[140:143], a[156:159], v[16:19]\n"
    "buffer_load_dwordx4 v223, s[48:51], s56 offen offset:3072 lds\n"
    "v_add_f32 v93, v93, v36\n"
    "v_add_f32 v94, v94, v36\n"
    "v_add_f32 v95, v95, v36\n"
    "v_cvt_pk_bf16_f32 v50, v88, v89\n"
    "v_cvt_pk_bf16_f32 v51, v90, v91\n"
    "v_mfma_f32_16x16x32_bf16 v[20:23], v[112:115], a[160:163], v[36:39]\n"
    "s_add_u32 s56, s56, 0x4000\n"
    "v_exp_f32 v92, v92\n"
    "v_exp_f32 v93, v93\n"
    "v_exp_f32 v94, v94\n"
    "v_exp_f32 v95, v95\n"
    "v_mfma_f32_16x16x32_bf16 v[20:23], v[116:119], a[164:167], v[20:23]\n"
    "s_nop 0\n"
    "v_cvt_pk_bf16_f32 v66, v92, v93\n"
    "v_cvt_pk_bf16_f32 v67, v94, v95\n"
    "v_add_f32 v96, v96, v32\n"
    "v_add_f32 v97, v97, v32\n"
    "v_add_f32 v98, v98, v32\n"
    "v_add_f32 v99, v99, v32\n"
    "v_exp_f32 v96, v96\n"
    "v_exp_f32 v97, v97\n"
    "v_mfma_f32_16x16x32_bf16 v[20:23], v[120:123], a[168:171], v[20:23]\n"
    "v_exp_f32 v98, v98\n"
    "v_exp_f32 v99, v99\n"
    "v_add_f32 v100, v100, v36\n"
    "v_add_f32 v101, v101, v36\n"
    "v_add_f32 v102, v102, v36\n"
    "v_add_f32 v103, v103, v36\n"
    "v_cvt_pk_bf16_f32 v52, v96, v97\n"
    "v_mfma_f32_16x16x32_bf16 v[20:23], v[124:127], a[172:175], v[20:23]\n"
    "v_cvt_pk_bf16_f32 v53, v98, v99\n"
    "v_exp_f32 v100, v100\n"
    "v_exp_f32 v101, v101\n"
    "v_exp_f32 v102, v102\n"
    "v_exp_f32 v40, v16\n"
    "v_mfma_f32_16x16x32_bf16 v[20:23], v[128:131], a[176:179], v[20:23]\n"
    "ds_read_b128 v[112:115], v212 offset:0\n"
    "v_exp_f32 v103, v103\n"
    "v_add_f32 v104, v104, v32\n"
    "v_add_f32 v105, v105, v32\n"
    "v_add_f32 v106, v106, v32\n"
    "v_add_f32 v107, v107, v32\n"
    "v_cvt_pk_bf16_f32 v68, v100, v101\n"
    "v_exp_f32 v41, v17\n"
    "v_mfma_f32_16x16x32_bf16 v[20:23], v[132:135], a[180:183], v[20:23]\n"
    "ds_read_b128 v[116:119], v213 offset:0\n"
    "v_cvt_pk_bf16_f32 v69, v102, v103\n"
    "v_exp_f32 v104, v104\n"
    "v_exp_f32 v105, v105\n"
    "v_exp_f32 v106, v106\n"
    "v_exp_f32 v42, v18\n"
    "v_mfma_f32_16x16x32_bf16 v[20:23], v[136:139], a[184:187], v[20:23]\n"
    "ds_read_b128 v[120:123], v214 offset:0\n"
    "v_exp_f32 v107, v107\n"
    "v_add_f32 v108, v108, v36\n"
    "v_add_f32 v109, v109, v36\n"
    "v_add_f32 v110, v110, v36\n"
    "v_add_f32 v111, v111, v36\n"
    "v_cvt_pk_bf16_f32 v54, v104, v105\n"
    "v_cvt_pk_bf16_f32 v55, v106, v107\n"
    "v_exp_f32 v43, v19\n"
    "v_mfma_f32_16x16x32_bf16 v[20:23], v[140:143], a[188:191], v[20:23]\n"
    "s_waitcnt lgkmcnt(3)\n"
    "ds_read_b128 v[124:127], v215 offset:0\n"
    "v_exp_f32 v108, v108\n"
    "v_exp_f32 v109, v109\n"
    "v_exp_f32 v110, v110\n"
    "v_exp_f32 v111, v111\n"
    "v_mfma_f32_16x16x32_bf16 v[24:27], v[144:147], a[128:131], v[32:35]\n"
    "s_nop 0\n"
    "ds_read_b128 v[128:131], v212 offset:256\n"
    "v_cvt_pk_bf16_f32 v70, v108, v109\n"
    "v_cvt_pk_bf16_f32 v71, v110, v111\n"
    "v_add_f32 v0, v0, v32\n"
    "v_add_f32 v1, v1, v32\n"
    "v_add_f32 v2, v2, v32\n"
    "v_add_f32 v3, v3, v32\n"
    "v_cvt_pk_bf16_f32 v60, v40, v41\n"
    "v_mfma_f32_16x16x32_bf16 v[24:27], v[148:151], a[132:135], v[24:27]\n"
    "ds_read_b128 v[132:135], v213 offset:256\n"
    "v_exp_f32 v0, v0\n"
    "v_exp_f32 v1, v1\n"
    "v_exp_f32 v2, v2\n"
    "v_cvt_pk_bf16_f32 v61, v42, v43\n"
    "v_mfma_f32_16x16x32_bf16 v[24:27], v[152:155], a[136:139], v[24:27]\n"
    "ds_read_b128 v[136:139], v214 offset:256\n"
    "v_exp_f32 v3, v3\n"
    "v_add_f32 v4, v4, v36\n"
    "v_add_f32 v5, v5, v36\n"
    "v_add_f32 v6, v6, v36\n"
    "v_add_f32 v7, v7, v36\n"
    "v_or_b32 v228, v60, v61\n"
    "v_mfma_f32_16x16x32_bf16 v[24:27], v[156:159], a[140:143], v[24:27]\n"
    "ds_read_b128 v[140:143], v215 offset:256\n"
    "v_cvt_pk_bf16_f32 v56, v0, v1\n"
    "v_cvt_pk_bf16_f32 v57, v2, v3\n"
    "v_exp_f32 v4, v4\n"
    "v_exp_f32 v5, v5\n"
    "v_exp_f32 v44, v20\n"
    "v_mfma_f32_16x16x32_bf16 v[24:27], v[160:163], a[144:147], v[24:27]\n"
    "ds_read_b64_tr_b16 v[176:177], v216 offset:0\n"
    "v_exp_f32 v6, v6\n"
    "v_exp_f32 v7, v7\n"
    "v_add_f32 v8, v8, v32\n"
    "v_add_f32 v9, v9, v32\n"
    "v_exp_f32 v45, v21\n"
    "v_mfma_f32_16x16x32_bf16 v[24:27], v[164:167], a[148:151], v[24:27]\n"
    "ds_read_b64_tr_b16 v[178:179], v216 offset:2048\n"
    "v_add_f32 v10, v10, v32\n"
    "v_add_f32 v11, v11, v32\n"
    "v_cvt_pk_bf16_f32 v72, v4, v5\n"
    "v_cvt_pk_bf16_f32 v73, v6, v7\n"
    "v_exp_f32 v8, v8\n"
    "v_exp_f32 v46, v22\n"
    "v_mfma_f32_16x16x32_bf16 v[24:27], v[168:171], a[152:155], v[24:27]\n"
    "ds_read_b64_tr_b16 v[180:181], v216 offset:4096\n"
    "v_exp_f32 v9, v9\n"
    "v_exp_f32 v10, v10\n"
    "v_exp_f32 v11, v11\n"
    "v_add_f32 v12, v12, v36\n"
    "v_exp_f32 v47, v23\n"
    "v_mfma_f32_16x16x32_bf16 v[24:27], v[172:175], a[156:159], v[24:27]\n"
    "ds_read_b64_tr_b16 v[182:183], v216 offset:6144\n"
    "v_add_f32 v13, v13, v36\n"
    "v_add_f32 v14, v14, v36\n"
    "v_add_f32 v15, v15, v36\n"
    "v_cvt_pk_bf16_f32 v58, v8, v9\n"
    "v_cvt_pk_bf16_f32 v59, v10, v11\n"
    "v_exp_f32 v12, v12\n"
    "v_mfma_f32_16x16x32_bf16 v[28:31], v[144:147], a[160:163], v[36:39]\n"
    "ds_read_b64_tr_b16 v[184:185], v216 offset:8192\n"
    "v_exp_f32 v13, v13\n"
    "v_exp_f32 v14, v14\n"
    "v_exp_f32 v15, v15\n"
    "v_cvt_pk_bf16_f32 v76, v44, v45\n"
    "v_mfma_f32_16x16x32_bf16 v[28:31], v[148:151], a[164:167], v[28:31]\n"
    "s_nop 0\n"
    "ds_read_b64_tr_b16 v[186:187], v216 offset:10240\n"
    "v_cvt_pk_bf16_f32 v74, v12, v13\n"
    "v_cvt_pk_bf16_f32 v75, v14, v15\n"
    "v_accvgpr_write_b32 a94, 0\n"
    "v_accvgpr_write_b32 a95, 0\n"
    "v_accvgpr_write_b32 a96, 0\n"
    "v_accvgpr_write_b32 a97, 0\n"
    "v_cvt_pk_bf16_f32 v77, v46, v47\n"
    "v_mfma_f32_16x16x32_bf16 v[28:31], v[152:155], a[168:171], v[28:31]\n"
    "ds_read_b64_tr_b16 v[188:189], v216 offset:12288\n"
    "v_accvgpr_write_b32 a98, 0\n"
    "v_accvgpr_write_b32 a99, 0\n"
    "v_accvgpr_write_b32 a100, 0\n"
    "v_accvgpr_write_b32 a101, 0\n"
    "v_accvgpr_write_b32 a102, 0\n"
    "v_accvgpr_write_b32 a103, 0\n"
    "v_or3_b32 v228, v228, v76, v77\n"
    "v_mfma_f32_16x16x32_bf16 v[28:31], v[156:159], a[172:175], v[28:31]\n"
    "ds_read_b64_tr_b16 v[190:191], v216 offset:14336\n"
    "v_accvgpr_write_b32 a104, 0\n"
    "v_accvgpr_write_b32 a105, 0\n"
    "v_accvgpr_write_b32 a106, 0\n"
    "v_accvgpr_write_b32 a107, 0\n"
    "v_accvgpr_write_b32 a108, 0\n"
    "v_accvgpr_write_b32 a109, 0\n"
    "v_exp_f32 v40, v24\n"
    "v_mfma_f32_16x16x32_bf16 v[28:31], v[160:163], a[176:179], v[28:31]\n"
    "ds_read_b128 v[144:147], v212 offset:8192\n"
    "v_accvgpr_write_b32 a110, 0\n"
    "v_accvgpr_write_b32 a111, 0\n"
    "v_accvgpr_write_b32 a112, 0\n"
    "v_accvgpr_write_b32 a113, 0\n"
    "v_accvgpr_write_b32 a114, 0\n"
    "v_accvgpr_write_b32 a115, 0\n"
    "v_exp_f32 v41, v25\n"
    "v_mfma_f32_16x16x32_bf16 v[28:31], v[164:167], a[180:183], v[28:31]\n"
    "ds_read_b128 v[148:151], v213 offset:8192\n"
    "v_accvgpr_write_b32 a116, 0\n"
    "v_accvgpr_write_b32 a117, 0\n"
    "v_accvgpr_write_b32 a118, 0\n"
    "v_accvgpr_write_b32 a119, 0\n"
    "v_accvgpr_write_b32 a120, 0\n"
    "v_accvgpr_write_b32 a121, 0\n"
    "v_exp_f32 v42, v26\n"
    "v_mfma_f32_16x16x32_bf16 v[28:31], v[168:171], a[184:187], v[28:31]\n"
    "s_waitcnt vmcnt(8)\n"
    "ds_read_b128 v[152:155], v214 offset:8192\n"
    "v_accvgpr_write_b32 a122, 0\n"
    "v_accvgpr_write_b32 a123, 0\n"
    "v_accvgpr_write_b32 a124, 0\n"
    "v_accvgpr_write_b32 a125, 0\n"
    "v_accvgpr_write_b32 a126, 0\n"
    "v_accvgpr_write_b32 a127, 0\n"
    "v_exp_f32 v43, v27\n"
    "v_mfma_f32_16x16x32_bf16 v[28:31], v[172:175], a[188:191], v[28:31]\n"
    "s_waitcnt lgkmcnt(11)\n"
    "ds_read_b128 v[156:159], v215 offset:8192\n"
    "v_accvgpr_write_b32 a192, 0\n"
    "v_accvgpr_write_b32 a193, 0\n"
    "v_accvgpr_write_b32 a194, 0\n"
    "v_accvgpr_write_b32 a195, 0\n"
    "v_accvgpr_write_b32 a196, 0\n"
    "v_accvgpr_write_b32 a197, 0\n"
    "v_accvgpr_write_b32 a198, 0\n"
    "v_accvgpr_write_b32 a199, 0\n"
    "s_barrier\n"
    "v_cvt_pk_bf16_f32 v62, v40, v41\n"
    "v_cvt_pk_bf16_f32 v63, v42, v43\n"
    "v_or3_b32 v228, v228, v62, v63\n"
    "s_add_u32 m0, s59, 49152\n"
    "s_nop 2\n"
    "v_exp_f32 v44, v28\n"
    "v_exp_f32 v45, v29\n"
    "v_exp_f32 v46, v30\n"
    "v_exp_f32 v47, v31\n"
    ".p2align 6\n"
    "fb_Lloop:\n"
    "v_mfma_f32_16x16x32_bf16 v[0:3], v[112:115], a[128:131], v[32:35]\n"
    "ds_read_b64_tr_b16 v[192:193], v217 offset:0\n"
    "v_cvt_pk_bf16_f32 v78, v44, v45\n"
    "v_mfma_f32_16x16x32_bf16 v[0:3], v[116:119], a[132:135], v[0:3]\n"
    "ds_read_b64_tr_b16 v[194:195], v217 offset:2048\n"
    "v_cvt_pk_bf16_f32 v79, v46, v47\n"
    "v_mfma_f32_16x16x32_bf16 v[0:3], v[120:123], a[136:139], v[0:3]\n"
    "ds_read_b64_tr_b16 v[196:197], v217 offset:4096\n"
    "v_or3_b32 v228, v228, v78, v79\n"
    "v_mfma_f32_16x16x32_bf16 v[0:3], v[124:127], a[140:143], v[0:3]\n"
    "ds_read_b64_tr_b16 v[198:199], v217 offset:6144\n"
    "v_and_b32 v228, s60, v228\n"
    "v_mfma_f32_16x16x32_bf16 v[0:3], v[128:131], a[144:147], v[0:3]\n"
    "buffer_load_dwordx4 v220, s[48:51], s56 offen offset:0 lds\n"
    "v_mfma_f32_16x16x32_bf16 v[0:3], v[132:135], a[148:151], v[0:3]\n"
    "ds_read_b64_tr_b16 v[200:201], v217 offset:8192\n"
    "v_cmp_ne_u32 vcc, 0, v228\n"
    "v_mfma_f32_16x16x32_bf16 v[0:3], v[136:139], a[152:155], v[0:3]\n"
    "s_waitcnt lgkmcnt(5)\n"
    "ds_read_b64_tr_b16 v[202:203], v217 offset:10240\n"
    "v_mfma_f32_16x16x32_bf16 v[0:3], v[140:143], a[156:159], v[0:3]\n"
    "s_cbranch_vccnz fb_Lslow4\n"
    "fb_Lback4:\n"
    "ds_read_b64_tr_b16 v[204:205], v217 offset:12288\n"
    "ds_read_b64_tr_b16 v[206:207], v217 offset:14336\n"
    "v_mfma_f32_16x16x32_bf16 a[0:3], v[176:179], v[48:51], a[0:3]\n"
    "ds_read_b128 v[160:163], v212 offset:8448\n"
    "v_mfma_f32_16x16x32_bf16 a[0:3], v[180:183], v[52:55], a[0:3]\n"
    "ds_read_b128 v[164:167], v213 offset:8448\n"
    "v_mfma_f32_16x16x32_bf16 a[0:3], v[184:187], v[56:59], a[0:3]\n"
    "buffer_load_dwordx4 v221, s[48:51], s56 offen offset:1024 lds\n"
    "v_mfma_f32_16x16x32_bf16 a[0:3], v[188:191], v[60:63], a[0:3]\n"
    "ds_read_b128 v[168:171], v214 offset:8448\n"
    "ds_read_b128 v[172:175], v215 offset:8448\n"
    "v_mfma_f32_16x16x32_bf16 a[4:7], v[176:179], v[64:67], a[4:7]\n"
    "v_exp_f32 v40, v0\n"
    "v_mfma_f32_16x16x32_bf16 a[4:7], v[180:183], v[68:71], a[4:7]\n"
    "v_exp_f32 v41, v1\n"
    "v_mfma_f32_16x16x32_bf16 a[4:7], v[184:187], v[72:75], a[4:7]\n"
    "v_exp_f32 v42, v2\n"
    "v_mfma_f32_16x16x32_bf16 a[4:7], v[188:191], v[76:79], a[4:7]\n"
    "v_exp_f32 v43, v3\n"
    "v_mfma_f32_16x16x32_bf16 v[4:7], v[112:115], a[160:163], v[36:39]\n"
    "ds_read_b64_tr_b16 v[176:177], v218 offset:0\n"
    "v_cvt_pk_bf16_f32 v80, v40, v41\n"
    "v_mfma_f32_16x16x32_bf16 v[4:7], v[116:119], a[164:167], v[4:7]\n"
    "ds_read_b64_tr_b16 v[178:179], v218 offset:2048\n"
    "v_cvt_pk_bf16_f32 v81, v42, v43\n"
    "v_mfma_f32_16x16x32_bf16 v[4:7], v[120:123], a[168:171], v[4:7]\n"
    "ds_read_b64_tr_b16 v[180:181], v218 offset:4096\n"
    "v_or_b32 v228, v80, v81\n"
    "v_mfma_f32_16x16x32_bf16 v[4:7], v[124:127], a[172:175], v[4:7]\n"
    "ds_read_b64_tr_b16 v[182:183], v218 offset:6144\n"
    "v_mfma_f32_16x16x32_bf16 v[4:7], v[128:131], a[176:179], v[4:7]\n"
    "buffer_load_dwordx4 v222, s[48:51], s56 offen offset:2048 lds\n"
    "v_mfma_f32_16x16x32_bf16 v[4:7], v[132:135], a[180:183], v[4:7]\n"
    "ds_read_b64_tr_b16 v[184:185], v218 offset:8192\n"
    "v_mfma_f32_16x16x32_bf16 v[4:7], v[136:139], a[184:187], v[4:7]\n"
    "s_waitcnt lgkmcnt(5)\n"
    "ds_read_b64_tr_b16 v[186:187], v218 offset:10240\n"
    "v_mfma_f32_16x16x32_bf16 v[4:7], v[140:143], a[188:191], v[4:7]\n"
    "ds_read_b64_tr_b16 v[188:189], v218 offset:12288\n"
    "v_mfma_f32_16x16x32_bf16 a[8:11], v[192:195], v[48:51], a[8:11]\n"
    "ds_read_b64_tr_b16 v[190:191], v218 offset:14336\n"
    "v_mfma_f32_16x16x32_bf16 a[8:11], v[196:199], v[52:55], a[8:11]\n"
    "ds_read_b128 v[112:115], v212 offset:16384\n"
    "v_mfma_f32_16x16x32_bf16 a[8:11], v[200:203], v[56:59], a[8:11]\n"
    "buffer_load_dwordx4 v223, s[48:51], s56 offen offset:3072 lds\n"
    "v_mfma_f32_16x16x32_bf16 a[8:11], v[204:207], v[60:63], a[8:11]\n"
    "ds_read_b128 v[116:119], v213 offset:16384\n"
    "v_mfma_f32_16x16x32_bf16 a[12:15], v[192:195], v[64:67], a[12:15]\n"
    "s_add_u32 s57, s57, 128\n"
    "v_exp_f32 v44, v4\n"
    "v_mfma_f32_16x16x32_bf16 a[12:15], v[196:199], v[68:71], a[12:15]\n"
    "v_exp_f32 v45, v5\n"
    "v_mfma_f32_16x16x32_bf16 a[12:15], v[200:203], v[72:75], a[12:15]\n"
    "s_add_u32 m0, s59, 114688\n"
    "v_exp_f32 v46, v6\n"
    "v_mfma_f32_16x16x32_bf16 a[12:15], v[204:207], v[76:79], a[12:15]\n"
    "s_add_u32 s56, s56, 0x4000\n"
    "v_exp_f32 v47, v7\n"
    "v_mfma_f32_16x16x32_bf16 a[192:195], v[208:211], v[48:51], a[192:195]\n"
    "ds_read_b128 v[120:123], v214 offset:16384\n"
    "v_mfma_f32_16x16x32_bf16 a[192:195], v[208:211], v[52:55], a[192:195]\n"
    "ds_read_b128 v[124:127], v215 offset:16384\n"
    "v_mfma_f32_16x16x32_bf16 a[192:195], v[208:211], v[56:59], a[192:195]\n"
    "ds_read_b64_tr_b16 v[192:193], v219 offset:0\n"
    "v_mfma_f32_16x16x32_bf16 a[192:195], v[208:211], v[60:63], a[192:195]\n"
    "ds_read_b64_tr_b16 v[194:195], v219 offset:2048\n"
    "v_mfma_f32_16x16x32_bf16 v[8:11], v[144:147], a[128:131], v[32:35]\n"
    "ds_read_b64_tr_b16 v[196:197], v219 offset:4096\n"
    "v_cvt_pk_bf16_f32 v96, v44, v45\n"
    "v_mfma_f32_16x16x32_bf16 v[8:11], v[148:151], a[132:135], v[8:11]\n"
    "ds_read_b64_tr_b16 v[198:199], v219 offset:6144\n"
    "v_cvt_pk_bf16_f32 v97, v46, v47\n"
    "v_mfma_f32_16x16x32_bf16 v[8:11], v[152:155], a[136:139], v[8:11]\n"
    "ds_read_b64_tr_b16 v[200:201], v219 offset:8192\n"
    "v_or3_b32 v228, v228, v96, v97\n"
    "v_mfma_f32_16x16x32_bf16 v[8:11], v[156:159], a[140:143], v[8:11]\n"
    "ds_read_b64_tr_b16 v[202:203], v219 offset:10240\n"
    "v_mfma_f32_16x16x32_bf16 v[8:11], v[160:163], a[144:147], v[8:11]\n"
    "buffer_load_dwordx4 v224, s[88:91], s57 offen offset:0 lds\n"
    "v_mfma_f32_16x16x32_bf16 v[8:11], v[164:167], a[148:151], v[8:11]\n"
    "ds_read_b64_tr_b16 v[204:205], v219 offset:12288\n"
    "v_mfma_f32_16x16x32_bf16 v[8:11], v[168:171], a[152:155], v[8:11]\n"
    "s_waitcnt lgkmcnt(7)\n"
    "ds_read_b64_tr_b16 v[206:207], v219 offset:14336\n"
    "v_mfma_f32_16x16x32_bf16 v[8:11], v[172:175], a[156:159], v[8:11]\n"
    "ds_read_b128 v[128:131], v212 offset:16640\n"
    "v_mfma_f32_16x16x32_bf16 a[16:19], v[176:179], v[48:51], a[16:19]\n"
    "ds_read_b128 v[132:135], v213 offset:16640\n"
    "v_mfma_f32_16x16x32_bf16 a[16:19], v[180:183], v[52:55], a[16:19]\n"
    "ds_read_b128 v[136:139], v214 offset:16640\n"
    "v_mfma_f32_16x16x32_bf16 a[16:19], v[184:187], v[56:59], a[16:19]\n"
    "buffer_load_dwordx4 v225, s[88:91], s57 offen offset:1024 lds\n"
    "v_mfma_f32_16x16x32_bf16 a[16:19], v[188:191], v[60:63], a[16:19]\n"
    "ds_read_b128 v[140:143], v215 offset:16640\n"
    "v_mfma_f32_16x16x32_bf16 a[20:23], v[176:179], v[64:67], a[20:23]\n"
    "s_and_b32 s56, s56, 0xfffff\n"
    "v_exp_f32 v40, v8\n"
    "v_mfma_f32_16x16x32_bf16 a[20:23], v[180:183], v[68:71], a[20:23]\n"
    "v_exp_f32 v41, v9\n"
    "v_mfma_f32_16x16x32_bf16 a[20:23], v[184:187], v[72:75], a[20:23]\n"
    "v_exp_f32 v42, v10\n"
    "v_mfma_f32_16x16x32_bf16 a[20:23], v[188:191], v[76:79], a[20:23]\n"
    "v_exp_f32 v43, v11\n"
    "v_mfma_f32_16x16x32_bf16 v[12:15], v[144:147], a[160:163], v[36:39]\n"
    "ds_read_b64_tr_b16 v[176:177], v216 offset:16384\n"
    "v_cvt_pk_bf16_f32 v82, v40, v41\n"
    "v_mfma_f32_16x16x32_bf16 v[12:15], v[148:151], a[164:167], v[12:15]\n"
    "ds_read_b64_tr_b16 v[178:179], v216 offset:18432\n"
    "v_cvt_pk_bf16_f32 v83, v42, v43\n"
    "v_mfma_f32_16x16x32_bf16 v[12:15], v[152:155], a[168:171], v[12:15]\n"
    "ds_read_b64_tr_b16 v[180:181], v216 offset:20480\n"
    "v_or3_b32 v228, v228, v82, v83\n"
    "v_mfma_f32_16x16x32_bf16 v[12:15], v[156:159], a[172:175], v[12:15]\n"
    "ds_read_b64_tr_b16 v[182:183], v216 offset:22528\n"
    "v_mfma_f32_16x16x32_bf16 v[12:15], v[160:163], a[176:179], v[12:15]\n"
    "buffer_load_dwordx4 v226, s[88:91], s57 offen offset:2048 lds\n"
    "v_mfma_f32_16x16x32_bf16 v[12:15], v[164:167], a[180:183], v[12:15]\n"
    "ds_read_b64_tr_b16 v[184:185], v216 offset:24576\n"
    "v_mfma_f32_16x16x32_bf16 v[12:15], v[168:171], a[184:187], v[12:15]\n"
    "s_waitcnt lgkmcnt(5)\n"
    "ds_read_b64_tr_b16 v[186:187], v216 offset:26624\n"
    "v_mfma_f32_16x16x32_bf16 v[12:15], v[172:175], a[188:191], v[12:15]\n"
    "ds_read_b64_tr_b16 v[188:189], v216 offset:28672\n"
    "ds_read_b64_tr_b16 v[190:191], v216 offset:30720\n"
    "v_mfma_f32_16x16x32_bf16 a[24:27], v[192:195], v[48:51], a[24:27]\n"
    "ds_read_b128 v[144:147], v212 offset:24576\n"
    "v_mfma_f32_16x16x32_bf16 a[24:27], v[196:199], v[52:55], a[24:27]\n"
    "ds_read_b128 v[148:151], v213 offset:24576\n"
    "v_mfma_f32_16x16x32_bf16 a[24:27], v[200:203], v[56:59], a[24:27]\n"
    "buffer_load_dwordx4 v227, s[88:91], s57 offen offset:3072 lds\n"
    "v_mfma_f32_16x16x32_bf16 a[24:27], v[204:207], v[60:63], a[24:27]\n"
    "ds_read_b128 v[152:155], v214 offset:24576\n"
    "ds_read_b128 v[156:159], v215 offset:24576\n"
    "v_mfma_f32_16x16x32_bf16 a[28:31], v[192:195], v[64:67], a[28:31]\n"
    "s_add_u32 m0, s59, 0\n"
    "v_exp_f32 v44, v12\n"
    "v_mfma_f32_16x16x32_bf16 a[28:31], v[196:199], v[68:71], a[28:31]\n"
    "v_exp_f32 v45, v13\n"
    "v_mfma_f32_16x16x32_bf16 a[28:31], v[200:203], v[72:75], a[28:31]\n"
    "s_waitcnt vmcnt(8)\n"
    "v_exp_f32 v46, v14\n"
    "v_mfma_f32_16x16x32_bf16 a[28:31], v[204:207], v[76:79], a[28:31]\n"
    "s_barrier\n"
    "v_exp_f32 v47, v15\n"
    "v_mfma_f32_16x16x32_bf16 v[16:19], v[112:115], a[128:131], v[32:35]\n"
    "ds_read_b64_tr_b16 v[192:193], v217 offset:16384\n"
    "v_cvt_pk_bf16_f32 v98, v44, v45\n"
    "v_mfma_f32_16x16x32_bf16 v[16:19], v[116:119], a[132:135], v[16:19]\n"
    "ds_read_b64_tr_b16 v[194:195], v217 offset:18432\n"
    "v_cvt_pk_bf16_f32 v99, v46, v47\n"
    "v_mfma_f32_16x16x32_bf16 v[16:19], v[120:123], a[136:139], v[16:19]\n"
    "ds_read_b64_tr_b16 v[196:197], v217 offset:20480\n"
    "v_or3_b32 v228, v228, v98, v99\n"
    "v_mfma_f32_16x16x32_bf16 v[16:19], v[124:127], a[140:143], v[16:19]\n"
    "ds_read_b64_tr_b16 v[198:199], v217 offset:22528\n"
    "v_and_b32 v228, s60, v228\n"
    "v_mfma_f32_16x16x32_bf16 v[16:19], v[128:131], a[144:147], v[16:19]\n"
    "buffer_load_dwordx4 v220, s[48:51], s56 offen offset:0 lds\n"
    "v_mfma_f32_16x16x32_bf16 v[16:19], v[132:135], a[148:151], v[16:19]\n"
    "ds_read_b64_tr_b16 v[200:201], v217 offset:24576\n"
    "v_cmp_ne_u32 vcc, 0, v228\n"
    "v_mfma_f32_16x16x32_bf16 v[16:19], v[136:139], a[152:155], v[16:19]\n"
    "s_waitcnt lgkmcnt(5)\n"
    "ds_read_b64_tr_b16 v[202:203], v217 offset:26624\n"
    "v_mfma_f32_16x16x32_bf16 v[16:19], v[140:143], a[156:159], v[16:19]\n"
    "s_cbranch_vccnz fb_Lslow5\n"
    "fb_Lback5:\n"
    "ds_read_b64_tr_b16 v[204:205], v217 offset:28672\n"
    "ds_read_b64_tr_b16 v[206:207], v217 offset:30720\n"
    "v_mfma_f32_16x16x32_bf16 a[32:35], v[176:179], v[48:51], a[32:35]\n"
    "ds_read_b128 v[160:163], v212 offset:24832\n"
    "v_mfma_f32_16x16x32_bf16 a[32:35], v[180:183], v[52:55], a[32:35]\n"
    "ds_read_b128 v[164:167], v213 offset:24832\n"
    "v_mfma_f32_16x16x32_bf16 a[32:35], v[184:187], v[56:59], a[32:35]\n"
    "buffer_load_dwordx4 v221, s[48:51], s56 offen offset:1024 lds\n"
    "v_mfma_f32_16x16x32_bf16 a[32:35], v[188:191], v[60:63], a[32:35]\n"
    "ds_read_b128 v[168:171], v214 offset:24832\n"
    "ds_read_b128 v[172:175], v215 offset:24832\n"
    "v_mfma_f32_16x16x32_bf16 a[36:39], v[176:179], v[64:67], a[36:39]\n"
    "v_exp_f32 v40, v16\n"
    "v_mfma_f32_16x16x32_bf16 a[36:39], v[180:183], v[68:71], a[36:39]\n"
    "v_exp_f32 v41, v17\n"
    "v_mfma_f32_16x16x32_bf16 a[36:39], v[184:187], v[72:75], a[36:39]\n"
    "v_exp_f32 v42, v18\n"
    "v_mfma_f32_16x16x32_bf16 a[36:39], v[188:191], v[76:79], a[36:39]\n"
    "v_exp_f32 v43, v19\n"
    "v_mfma_f32_16x16x32_bf16 v[20:23], v[112:115], a[160:163], v[36:39]\n"
    "ds_read_b64_tr_b16 v[176:177], v218 offset:16384\n"
    "v_cvt_pk_bf16_f32 v84, v40, v41\n"
    "v_mfma_f32_16x16x32_bf16 v[20:23], v[116:119], a[164:167], v[20:23]\n"
    "ds_read_b64_tr_b16 v[178:179], v218 offset:18432\n"
    "v_cvt_pk_bf16_f32 v85, v42, v43\n"
    "v_mfma_f32_16x16x32_bf16 v[20:23], v[120:123], a[168:171], v[20:23]\n"
    "ds_read_b64_tr_b16 v[180:181], v218 offset:20480\n"
    "v_or_b32 v228, v84, v85\n"
    "v_mfma_f32_16x16x32_bf16 v[20:23], v[124:127], a[172:175], v[20:23]\n"
    "ds_read_b64_tr_b16 v[182:183], v218 offset:22528\n"
    "v_mfma_f32_16x16x32_bf16 v[20:23], v[128:131], a[176:179], v[20:23]\n"
    "buffer_load_dwordx4 v222, s[48:51], s56 offen offset:2048 lds\n"
    "v_mfma_f32_16x16x32_bf16 v[20:23], v[132:135], a[180:183], v[20:23]\n"
    "ds_read_b64_tr_b16 v[184:185], v218 offset:24576\n"
    "v_mfma_f32_16x16x32_bf16 v[20:23], v[136:139], a[184:187], v[20:23]\n"
    "s_waitcnt lgkmcnt(5)\n"
    "ds_read_b64_tr_b16 v[186:187], v218 offset:26624\n"
    "v_mfma_f32_16x16x32_bf16 v[20:23], v[140:143], a[188:191], v[20:23]\n"
    "ds_read_b64_tr_b16 v[188:189], v218 offset:28672\n"
    "ds_read_b64_tr_b16 v[190:191], v218 offset:30720\n"
    "v_mfma_f32_16x16x32_bf16 a[40:43], v[192:195], v[48:51], a[40:43]\n"
    "ds_read_b128 v[112:115], v212 offset:32768\n"
    "v_mfma_f32_16x16x32_bf16 a[40:43], v[196:199], v[52:55], a[40:43]\n"
    "ds_read_b128 v[116:119], v213 offset:32768\n"
    "v_mfma_f32_16x16x32_bf16 a[40:43], v[200:203], v[56:59], a[40:43]\n"
    "buffer_load_dwordx4 v223, s[48:51], s56 offen offset:3072 lds\n"
    "v_mfma_f32_16x16x32_bf16 a[40:43], v[204:207], v[60:63], a[40:43]\n"
    "ds_read_b128 v[120:123], v214 offset:32768\n"
    "ds_read_b128 v[124:127], v215 offset:32768\n"
    "v_mfma_f32_16x16x32_bf16 a[44:47], v[192:195], v[64:67], a[44:47]\n"
    "s_add_u32 s57, s57, 65152\n"
    "v_exp_f32 v44, v20\n"
    "v_mfma_f32_16x16x32_bf16 a[44:47], v[196:199], v[68:71], a[44:47]\n"
    "s_and_b32 s57, s57, 0xfffff\n"
    "v_exp_f32 v45, v21\n"
    "v_mfma_f32_16x16x32_bf16 a[44:47], v[200:203], v[72:75], a[44:47]\n"
    "s_add_u32 m0, s59, 65536\n"
    "v_exp_f32 v46, v22\n"
    "v_mfma_f32_16x16x32_bf16 a[44:47], v[204:207], v[76:79], a[44:47]\n"
    "s_add_u32 s56, s56, 0x4000\n"
    "v_exp_f32 v47, v23\n"
    "v_mfma_f32_16x16x32_bf16 v[24:27], v[144:147], a[128:131], v[32:35]\n"
    "ds_read_b64_tr_b16 v[192:193], v219 offset:16384\n"
    "v_cvt_pk_bf16_f32 v100, v44, v45\n"
    "v_mfma_f32_16x16x32_bf16 v[24:27], v[148:151], a[132:135], v[24:27]\n"
    "ds_read_b64_tr_b16 v[194:195], v219 offset:18432\n"
    "v_cvt_pk_bf16_f32 v101, v46, v47\n"
    "v_mfma_f32_16x16x32_bf16 v[24:27], v[152:155], a[136:139], v[24:27]\n"
    "ds_read_b64_tr_b16 v[196:197], v219 offset:20480\n"
    "v_or3_b32 v228, v228, v100, v101\n"
    "v_mfma_f32_16x16x32_bf16 v[24:27], v[156:159], a[140:143], v[24:27]\n"
    "ds_read_b64_tr_b16 v[198:199], v219 offset:22528\n"
    "v_mfma_f32_16x16x32_bf16 v[24:27], v[160:163], a[144:147], v[24:27]\n"
    "buffer_load_dwordx4 v224, s[88:91], s57 offen offset:0 lds\n"
    "v_mfma_f32_16x16x32_bf16 v[24:27], v[164:167], a[148:151], v[24:27]\n"
    "ds_read_b64_tr_b16 v[200:201], v219 offset:24576\n"
    "v_mfma_f32_16x16x32_bf16 v[24:27], v[168:171], a[152:155], v[24:27]\n"
    "s_waitcnt lgkmcnt(5)\n"
    "ds_read_b64_tr_b16 v[202:203], v219 offset:26624\n"
    "v_mfma_f32_16x16x32_bf16 v[24:27], v[172:175], a[156:159], v[24:27]\n"
    "ds_read_b64_tr_b16 v[204:205], v219 offset:28672\n"
    "ds_read_b64_tr_b16 v[206:207], v219 offset:30720\n"
    "v_mfma_f32_16x16x32_bf16 a[48:51], v[176:179], v[48:51], a[48:51]\n"
    "ds_read_b128 v[128:131], v212 offset:33024\n"
    "v_mfma_f32_16x16x32_bf16 a[48:51], v[180:183], v[52:55], a[48:51]\n"
    "ds_read_b128 v[132:135], v213 offset:33024\n"
    "v_mfma_f32_16x16x32_bf16 a[48:51], v[184:187], v[56:59], a[48:51]\n"
    "buffer_load_dwordx4 v225, s[88:91], s57 offen offset:1024 lds\n"
    "v_mfma_f32_16x16x32_bf16 a[48:51], v[188:191], v[60:63], a[48:51]\n"
    "ds_read_b128 v[136:139], v214 offset:33024\n"
    "ds_read_b128 v[140:143], v215 offset:33024\n"
    "v_mfma_f32_16x16x32_bf16 a[52:55], v[176:179], v[64:67], a[52:55]\n"
    "v_exp_f32 v40, v24\n"
    "v_mfma_f32_16x16x32_bf16 a[52:55], v[180:183], v[68:71], a[52:55]\n"
    "v_exp_f32 v41, v25\n"
    "v_mfma_f32_16x16x32_bf16 a[52:55], v[184:187], v[72:75], a[52:55]\n"
    "v_exp_f32 v42, v26\n"
    "v_mfma_f32_16x16x32_bf16 a[52:55], v[188:191], v[76:79], a[52:55]\n"
    "v_exp_f32 v43, v27\n"
    "v_mfma_f32_16x16x32_bf16 v[28:31], v[144:147], a[160:163], v[36:39]\n"
    "ds_read_b64_tr_b16 v[176:177], v216 offset:32768\n"
    "v_cvt_pk_bf16_f32 v86, v40, v41\n"
    "v_mfma_f32_16x16x32_bf16 v[28:31], v[148:151], a[164:167], v[28:31]\n"
    "ds_read_b64_tr_b16 v[178:179], v216 offset:34816\n"
    "v_cvt_pk_bf16_f32 v87, v42, v43\n"
    "v_mfma_f32_16x16x32_bf16 v[28:31], v[152:155], a[168:171], v[28:31]\n"
    "ds_read_b64_tr_b16 v[180:181], v216 offset:36864\n"
    "v_or3_b32 v228, v228, v86, v87\n"
    "v_mfma_f32_16x16x32_bf16 v[28:31], v[156:159], a[172:175], v[28:31]\n"
    "ds_read_b64_tr_b16 v[182:183], v216 offset:38912\n"
    "v_mfma_f32_16x16x32_bf16 v[28:31], v[160:163], a[176:179], v[28:31]\n"
    "buffer_load_dwordx4 v226, s[88:91], s57 offen offset:2048 lds\n"
    "v_mfma_f32_16x16x32_bf16 v[28:31], v[164:167], a[180:183], v[28:31]\n"
    "ds_read_b64_tr_b16 v[184:185], v216 offset:40960\n"
    "v_mfma_f32_16x16x32_bf16 v[28:31], v[168:171], a[184:187], v[28:31]\n"
    "s_waitcnt lgkmcnt(5)\n"
    "ds_read_b64_tr_b16 v[186:187], v216 offset:43008\n"
    "v_mfma_f32_16x16x32_bf16 v[28:31], v[172:175], a[188:191], v[28:31]\n"
    "ds_read_b64_tr_b16 v[188:189], v216 offset:45056\n"
    "ds_read_b64_tr_b16 v[190:191], v216 offset:47104\n"
    "v_mfma_f32_16x16x32_bf16 a[56:59], v[192:195], v[48:51], a[56:59]\n"
    "ds_read_b128 v[144:147], v212 offset:40960\n"
    "v_mfma_f32_16x16x32_bf16 a[56:59], v[196:199], v[52:55], a[56:59]\n"
    "ds_read_b128 v[148:151], v213 offset:40960\n"
    "v_mfma_f32_16x16x32_bf16 a[56:59], v[200:203], v[56:59], a[56:59]\n"
    "buffer_load_dwordx4 v227, s[88:91], s57 offen offset:3072 lds\n"
    "v_mfma_f32_16x16x32_bf16 a[56:59], v[204:207], v[60:63], a[56:59]\n"
    "ds_read_b128 v[152:155], v214 offset:40960\n"
    "ds_read_b128 v[156:159], v215 offset:40960\n"
    "v_mfma_f32_16x16x32_bf16 a[60:63], v[192:195], v[64:67], a[60:63]\n"
    "s_add_u32 m0, s59, 16384\n"
    "v_exp_f32 v44, v28\n"
    "v_mfma_f32_16x16x32_bf16 a[60:63], v[196:199], v[68:71], a[60:63]\n"
    "v_exp_f32 v45, v29\n"
    "v_mfma_f32_16x16x32_bf16 a[60:63], v[200:203], v[72:75], a[60:63]\n"
    "s_waitcnt vmcnt(8)\n"
    "v_exp_f32 v46, v30\n"
    "v_mfma_f32_16x16x32_bf16 a[60:63], v[204:207], v[76:79], a[60:63]\n"
    "s_barrier\n"
    "v_exp_f32 v47, v31\n"
    "v_mfma_f32_16x16x32_bf16 v[0:3], v[112:115], a[128:131], v[32:35]\n"
    "ds_read_b64_tr_b16 v[192:193], v217 offset:32768\n"
    "v_cvt_pk_bf16_f32 v102, v44, v45\n"
    "v_mfma_f32_16x16x32_bf16 v[0:3], v[116:119], a[132:135], v[0:3]\n"
    "ds_read_b64_tr_b16 v[194:195], v217 offset:34816\n"
    "v_cvt_pk_bf16_f32 v103, v46, v47\n"
    "v_mfma_f32_16x16x32_bf16 v[0:3], v[120:123], a[136:139], v[0:3]\n"
    "ds_read_b64_tr_b16 v[196:197], v217 offset:36864\n"
    "v_or3_b32 v228, v228, v102, v103\n"
    "v_mfma_f32_16x16x32_bf16 v[0:3], v[124:127], a[140:143], v[0:3]\n"
    "ds_read_b64_tr_b16 v[198:199], v217 offset:38912\n"
    "v_and_b32 v228, s60, v228\n"
    "v_mfma_f32_16x16x32_bf16 v[0:3], v[128:131], a[144:147], v[0:3]\n"
    "buffer_load_dwordx4 v220, s[48:51], s56 offen offset:0 lds\n"
    "v_mfma_f32_16x16x32_bf16 v[0:3], v[132:135], a[148:151], v[0:3]\n"
    "ds_read_b64_tr_b16 v[200:201], v217 offset:40960\n"
    "v_cmp_ne_u32 vcc, 0, v228\n"
    "v_mfma_f32_16x16x32_bf16 v[0:3], v[136:139], a[152:155], v[0:3]\n"
    "s_waitcnt lgkmcnt(5)\n"
    "ds_read_b64_tr_b16 v[202:203], v217 offset:43008\n"
    "v_mfma_f32_16x16x32_bf16 v[0:3], v[140:143], a[156:159], v[0:3]\n"
    "s_cbranch_vccnz fb_Lslow6\n"
    "fb_Lback6:\n"
    "ds_read_b64_tr_b16 v[204:205], v217 offset:45056\n"
    "ds_read_b64_tr_b16 v[206:207], v217 offset:47104\n"
    "v_mfma_f32_16x16x32_bf16 a[64:67], v[176:179], v[48:51], a[64:67]\n"
    "ds_read_b128 v[160:163], v212 offset:41216\n"
    "v_mfma_f32_16x16x32_bf16 a[64:67], v[180:183], v[52:55], a[64:67]\n"
    "ds_read_b128 v[164:167], v213 offset:41216\n"
    "v_mfma_f32_16x16x32_bf16 a[64:67], v[184:187], v[56:59], a[64:67]\n"
    "buffer_load_dwordx4 v221, s[48:51], s56 offen offset:1024 lds\n"
    "v_mfma_f32_16x16x32_bf16 a[64:67], v[188:191], v[60:63], a[64:67]\n"
    "ds_read_b128 v[168:171], v214 offset:41216\n"
    "ds_read_b128 v[172:175], v215 offset:41216\n"
    "v_mfma_f32_16x16x32_bf16 a[68:71], v[176:179], v[64:67], a[68:71]\n"
    "v_exp_f32 v40, v0\n"
    "v_mfma_f32_16x16x32_bf16 a[68:71], v[180:183], v[68:71], a[68:71]\n"
    "v_exp_f32 v41, v1\n"
    "v_mfma_f32_16x16x32_bf16 a[68:71], v[184:187], v[72:75], a[68:71]\n"
    "v_exp_f32 v42, v2\n"
    "v_mfma_f32_16x16x32_bf16 a[68:71], v[188:191], v[76:79], a[68:71]\n"
    "v_exp_f32 v43, v3\n"
    "v_mfma_f32_16x16x32_bf16 v[4:7], v[112:115], a[160:163], v[36:39]\n"
    "ds_read_b64_tr_b16 v[176:177], v218 offset:32768\n"
    "v_cvt_pk_bf16_f32 v88, v40, v41\n"
    "v_mfma_f32_16x16x32_bf16 v[4:7], v[116:119], a[164:167], v[4:7]\n"
    "ds_read_b64_tr_b16 v[178:179], v218 offset:34816\n"
    "v_cvt_pk_bf16_f32 v89, v42, v43\n"
    "v_mfma_f32_16x16x32_bf16 v[4:7], v[120:123], a[168:171], v[4:7]\n"
    "ds_read_b64_tr_b16 v[180:181], v218 offset:36864\n"
    "v_or_b32 v228, v88, v89\n"
    "v_mfma_f32_16x16x32_bf16 v[4:7], v[124:127], a[172:175], v[4:7]\n"
    "ds_read_b64_tr_b16 v[182:183], v218 offset:38912\n"
    "v_mfma_f32_16x16x32_bf16 v[4:7], v[128:131], a[176:179], v[4:7]\n"
    "buffer_load_dwordx4 v222, s[48:51], s56 offen offset:2048 lds\n"
    "v_mfma_f32_16x16x32_bf16 v[4:7], v[132:135], a[180:183], v[4:7]\n"
    "ds_read_b64_tr_b16 v[184:185], v218 offset:40960\n"
    "v_mfma_f32_16x16x32_bf16 v[4:7], v[136:139], a[184:187], v[4:7]\n"
    "s_waitcnt lgkmcnt(5)\n"
    "ds_read_b64_tr_b16 v[186:187], v218 offset:43008\n"
    "v_mfma_f32_16x16x32_bf16 v[4:7], v[140:143], a[188:191], v[4:7]\n"
    "ds_read_b64_tr_b16 v[188:189], v218 offset:45056\n"
    "ds_read_b64_tr_b16 v[190:191], v218 offset:47104\n"
    "v_mfma_f32_16x16x32_bf16 a[72:75], v[192:195], v[48:51], a[72:75]\n"
    "ds_read_b128 v[112:115], v212 offset:49152\n"
    "v_mfma_f32_16x16x32_bf16 a[72:75], v[196:199], v[52:55], a[72:75]\n"
    "ds_read_b128 v[116:119], v213 offset:49152\n"
    "v_mfma_f32_16x16x32_bf16 a[72:75], v[200:203], v[56:59], a[72:75]\n"
    "buffer_load_dwordx4 v223, s[48:51], s56 offen offset:3072 lds\n"
    "v_mfma_f32_16x16x32_bf16 a[72:75], v[204:207], v[60:63], a[72:75]\n"
    "ds_read_b128 v[120:123], v214 offset:49152\n"
    "ds_read_b128 v[124:127], v215 offset:49152\n"
    "v_mfma_f32_16x16x32_bf16 a[76:79], v[192:195], v[64:67], a[76:79]\n"
    "s_add_u32 s57, s57, 128\n"
    "v_exp_f32 v44, v4\n"
    "v_mfma_f32_16x16x32_bf16 a[76:79], v[196:199], v[68:71], a[76:79]\n"
    "v_exp_f32 v45, v5\n"
    "v_mfma_f32_16x16x32_bf16 a[76:79], v[200:203], v[72:75], a[76:79]\n"
    "s_add_u32 m0, s59, 81920\n"
    "v_exp_f32 v46, v6\n"
    "v_mfma_f32_16x16x32_bf16 a[76:79], v[204:207], v[76:79], a[76:79]\n"
    "s_add_u32 s56, s56, 0x4000\n"
    "v_exp_f32 v47, v7\n"
    "v_mfma_f32_16x16x32_bf16 a[196:199], v[208:211], v[64:67], a[196:199]\n"
    "s_mov_b32 m0, s65\n"
    "s_add_u32 s65, s65, 4096\n"
    "v_mfma_f32_16x16x32_bf16 a[196:199], v[208:211], v[68:71], a[196:199]\n"
    "buffer_load_dwordx4 v249, s[44:47], s64 offen lds\n"
    "v_mfma_f32_16x16x32_bf16 a[196:199], v[208:211], v[72:75], a[196:199]\n"
    "s_add_u32 m0, s59, 81920\n"
    "s_add_u32 s64, s64, s87\n"
    "v_mfma_f32_16x16x32_bf16 a[196:199], v[208:211], v[76:79], a[196:199]\n"
    "v_mfma_f32_16x16x32_bf16 v[8:11], v[144:147], a[128:131], v[32:35]\n"
    "ds_read_b64_tr_b16 v[192:193], v219 offset:32768\n"
    "v_cvt_pk_bf16_f32 v104, v44, v45\n"
    "v_mfma_f32_16x16x32_bf16 v[8:11], v[148:151], a[132:135], v[8:11]\n"
    "ds_read_b64_tr_b16 v[194:195], v219 offset:34816\n"
    "v_cvt_pk_bf16_f32 v105, v46, v47\n"
    "v_mfma_f32_16x16x32_bf16 v[8:11], v[152:155], a[136:139], v[8:11]\n"
    "ds_read_b64_tr_b16 v[196:197], v219 offset:36864\n"
    "v_or3_b32 v228, v228, v104, v105\n"
    "v_mfma_f32_16x16x32_bf16 v[8:11], v[156:159], a[140:143], v[8:11]\n"
    "ds_read_b64_tr_b16 v[198:199], v219 offset:38912\n"
    "v_mfma_f32_16x16x32_bf16 v[8:11], v[160:163], a[144:147], v[8:11]\n"
    "buffer_load_dwordx4 v224, s[88:91], s57 offen offset:0 lds\n"
    "v_mfma_f32_16x16x32_bf16 v[8:11], v[164:167], a[148:151], v[8:11]\n"
    "ds_read_b64_tr_b16 v[200:201], v219 offset:40960\n"
    "v_mfma_f32_16x16x32_bf16 v[8:11], v[168:171], a[152:155], v[8:11]\n"
    "s_waitcnt lgkmcnt(5)\n"
    "ds_read_b64_tr_b16 v[202:203], v219 offset:43008\n"
    "v_mfma_f32_16x16x32_bf16 v[8:11], v[172:175], a[156:159], v[8:11]\n"
    "ds_read_b64_tr_b16 v[204:205], v219 offset:45056\n"
    "ds_read_b64_tr_b16 v[206:207], v219 offset:47104\n"
    "v_mfma_f32_16x16x32_bf16 a[80:83], v[176:179], v[48:51], a[80:83]\n"
    "ds_read_b128 v[128:131], v212 offset:49408\n"
    "v_mfma_f32_16x16x32_bf16 a[80:83], v[180:183], v[52:55], a[80:83]\n"
    "ds_read_b128 v[132:135], v213 offset:49408\n"
    "v_mfma_f32_16x16x32_bf16 a[80:83], v[184:187], v[56:59], a[80:83]\n"
    "buffer_load_dwordx4 v225, s[88:91], s57 offen offset:1024 lds\n"
    "v_mfma_f32_16x16x32_bf16 a[80:83], v[188:191], v[60:63], a[80:83]\n"
    "ds_read_b128 v[136:139], v214 offset:49408\n"
    "ds_read_b128 v[140:143], v215 offset:49408\n"
    "v_mfma_f32_16x16x32_bf16 a[84:87], v[176:179], v[64:67], a[84:87]\n"
    "v_exp_f32 v40, v8\n"
    "v_mfma_f32_16x16x32_bf16 a[84:87], v[180:183], v[68:71], a[84:87]\n"
    "v_exp_f32 v41, v9\n"
    "v_mfma_f32_16x16x32_bf16 a[84:87], v[184:187], v[72:75], a[84:87]\n"
    "v_exp_f32 v42, v10\n"
    "v_mfma_f32_16x16x32_bf16 a[84:87], v[188:191], v[76:79], a[84:87]\n"
    "v_exp_f32 v43, v11\n"
    "v_mfma_f32_16x16x32_bf16 v[12:15], v[144:147], a[160:163], v[36:39]\n"
    "ds_read_b64_tr_b16 v[176:177], v216 offset:49152\n"
    "v_cvt_pk_bf16_f32 v90, v40, v41\n"
    "v_mfma_f32_16x16x32_bf16 v[12:15], v[148:151], a[164:167], v[12:15]\n"
    "ds_read_b64_tr_b16 v[178:179], v216 offset:51200\n"
    "v_cvt_pk_bf16_f32 v91, v42, v43\n"
    "v_mfma_f32_16x16x32_bf16 v[12:15], v[152:155], a[168:171], v[12:15]\n"
    "ds_read_b64_tr_b16 v[180:181], v216 offset:53248\n"
    "v_or3_b32 v228, v228, v90, v91\n"
    "v_mfma_f32_16x16x32_bf16 v[12:15], v[156:159], a[172:175], v[12:15]\n"
    "ds_read_b64_tr_b16 v[182:183], v216 offset:55296\n"
    "v_mfma_f32_16x16x32_bf16 v[12:15], v[160:163], a[176:179], v[12:15]\n"
    "buffer_load_dwordx4 v226, s[88:91], s57 offen offset:2048 lds\n"
    "v_mfma_f32_16x16x32_bf16 v[12:15], v[164:167], a[180:183], v[12:15]\n"
    "ds_read_b64_tr_b16 v[184:185], v216 offset:57344\n"
    "v_mfma_f32_16x16x32_bf16 v[12:15], v[168:171], a[184:187], v[12:15]\n"
    "s_waitcnt lgkmcnt(5)\n"
    "ds_read_b64_tr_b16 v[186:187], v216 offset:59392\n"
    "v_mfma_f32_16x16x32_bf16 v[12:15], v[172:175], a[188:191], v[12:15]\n"
    "ds_read_b64_tr_b16 v[188:189], v216 offset:61440\n"
    "ds_read_b64_tr_b16 v[190:191], v216 offset:63488\n"
    "v_mfma_f32_16x16x32_bf16 a[88:91], v[192:195], v[48:51], a[88:91]\n"
    "ds_read_b128 v[144:147], v212 offset:57344\n"
    "v_mfma_f32_16x16x32_bf16 a[88:91], v[196:199], v[52:55], a[88:91]\n"
    "ds_read_b128 v[148:151], v213 offset:57344\n"
    "v_mfma_f32_16x16x32_bf16 a[88:91], v[200:203], v[56:59], a[88:91]\n"
    "buffer_load_dwordx4 v227, s[88:91], s57 offen offset:3072 lds\n"
    "v_mfma_f32_16x16x32_bf16 a[88:91], v[204:207], v[60:63], a[88:91]\n"
    "ds_read_b128 v[152:155], v214 offset:57344\n"
    "ds_read_b128 v[156:159], v215 offset:57344\n"
    "v_mfma_f32_16x16x32_bf16 a[92:95], v[192:195], v[64:67], a[92:95]\n"
    "s_add_u32 m0, s59, 32768\n"
    "v_exp_f32 v44, v12\n"
    "v_mfma_f32_16x16x32_bf16 a[92:95], v[196:199], v[68:71], a[92:95]\n"
    "v_exp_f32 v45, v13\n"
    "v_mfma_f32_16x16x32_bf16 a[92:95], v[200:203], v[72:75], a[92:95]\n"
    "s_waitcnt vmcnt(9)\n"
    "v_exp_f32 v46, v14\n"
    "v_mfma_f32_16x16x32_bf16 a[92:95], v[204:207], v[76:79], a[92:95]\n"
    "s_barrier\n"
    "v_exp_f32 v47, v15\n"
    "v_mfma_f32_16x16x32_bf16 v[16:19], v[112:115], a[128:131], v[32:35]\n"
    "ds_read_b64_tr_b16 v[192:193], v217 offset:49152\n"
    "v_cvt_pk_bf16_f32 v106, v44, v45\n"
    "v_mfma_f32_16x16x32_bf16 v[16:19], v[116:119], a[132:135], v[16:19]\n"
    "ds_read_b64_tr_b16 v[194:195], v217 offset:51200\n"
    "v_cvt_pk_bf16_f32 v107, v46, v47\n"
    "v_mfma_f32_16x16x32_bf16 v[16:19], v[120:123], a[136:139], v[16:19]\n"
    "ds_read_b64_tr_b16 v[196:197], v217 offset:53248\n"
    "v_or3_b32 v228, v228, v106, v107\n"
    "v_mfma_f32_16x16x32_bf16 v[16:19], v[124:127], a[140:143], v[16:19]\n"
    "ds_read_b64_tr_b16 v[198:199], v217 offset:55296\n"
    "v_and_b32 v228, s60, v228\n"
    "v_mfma_f32_16x16x32_bf16 v[16:19], v[128:131], a[144:147], v[16:19]\n"
    "buffer_load_dwordx4 v220, s[48:51], s56 offen offset:0 lds\n"
    "v_mfma_f32_16x16x32_bf16 v[16:19], v[132:135], a[148:151], v[16:19]\n"
    "ds_read_b64_tr_b16 v[200:201], v217 offset:57344\n"
    "v_cmp_ne_u32 vcc, 0, v228\n"
    "v_mfma_f32_16x16x32_bf16 v[16:19], v[136:139], a[152:155], v[16:19]\n"
    "s_waitcnt lgkmcnt(5)\n"
    "ds_read_b64_tr_b16 v[202:203], v217 offset:59392\n"
    "v_mfma_f32_16x16x32_bf16 v[16:19], v[140:143], a[156:159], v[16:19]\n"
    "s_cbranch_vccnz fb_Lslow7\n"
    "fb_Lback7:\n"
    "ds_read_b64_tr_b16 v[204:205], v217 offset:61440\n"
    "ds_read_b64_tr_b16 v[206:207], v217 offset:63488\n"
    "v_mfma_f32_16x16x32_bf16 a[96:99], v[176:179], v[48:51], a[96:99]\n"
    "ds_read_b128 v[160:163], v212 offset:57600\n"
    "v_mfma_f32_16x16x32_bf16 a[96:99], v[180:183], v[52:55], a[96:99]\n"
    "ds_read_b128 v[164:167], v213 offset:57600\n"
    "v_mfma_f32_16x16x32_bf16 a[96:99], v[184:187], v[56:59], a[96:99]\n"
    "buffer_load_dwordx4 v221, s[48:51], s56 offen offset:1024 lds\n"
    "v_mfma_f32_16x16x32_bf16 a[96:99], v[188:191], v[60:63], a[96:99]\n"
    "ds_read_b128 v[168:171], v214 offset:57600\n"
    "ds_read_b128 v[172:175], v215 offset:57600\n"
    "v_mfma_f32_16x16x32_bf16 a[100:103], v[176:179], v[64:67], a[100:103]\n"
    "v_exp_f32 v40, v16\n"
    "v_mfma_f32_16x16x32_bf16 a[100:103], v[180:183], v[68:71], a[100:103]\n"
    "v_exp_f32 v41, v17\n"
    "v_mfma_f32_16x16x32_bf16 a[100:103], v[184:187], v[72:75], a[100:103]\n"
    "v_exp_f32 v42, v18\n"
    "v_mfma_f32_16x16x32_bf16 a[100:103], v[188:191], v[76:79], a[100:103]\n"
    "v_exp_f32 v43, v19\n"
    "v_mfma_f32_16x16x32_bf16 v[20:23], v[112:115], a[160:163], v[36:39]\n"
    "ds_read_b64_tr_b16 v[176:177], v218 offset:49152\n"
    "v_cvt_pk_bf16_f32 v92, v40, v41\n"
    "v_mfma_f32_16x16x32_bf16 v[20:23], v[116:119], a[164:167], v[20:23]\n"
    "ds_read_b64_tr_b16 v[178:179], v218 offset:51200\n"
    "v_cvt_pk_bf16_f32 v93, v42, v43\n"
    "v_mfma_f32_16x16x32_bf16 v[20:23], v[120:123], a[168:171], v[20:23]\n"
    "ds_read_b64_tr_b16 v[180:181], v218 offset:53248\n"
    "v_or_b32 v228, v92, v93\n"
    "v_mfma_f32_16x16x32_bf16 v[20:23], v[124:127], a[172:175], v[20:23]\n"
    "ds_read_b64_tr_b16 v[182:183], v218 offset:55296\n"
    "v_mfma_f32_16x16x32_bf16 v[20:23], v[128:131], a[176:179], v[20:23]\n"
    "buffer_load_dwordx4 v222, s[48:51], s56 offen offset:2048 lds\n"
    "v_mfma_f32_16x16x32_bf16 v[20:23], v[132:135], a[180:183], v[20:23]\n"
    "ds_read_b64_tr_b16 v[184:185], v218 offset:57344\n"
    "v_mfma_f32_16x16x32_bf16 v[20:23], v[136:139], a[184:187], v[20:23]\n"
    "s_waitcnt lgkmcnt(5)\n"
    "ds_read_b64_tr_b16 v[186:187], v218 offset:59392\n"
    "v_mfma_f32_16x16x32_bf16 v[20:23], v[140:143], a[188:191], v[20:23]\n"
    "ds_read_b64_tr_b16 v[188:189], v218 offset:61440\n"
    "ds_read_b64_tr_b16 v[190:191], v218 offset:63488\n"
    "v_mfma_f32_16x16x32_bf16 a[104:107], v[192:195], v[48:51], a[104:107]\n"
    "ds_read_b128 v[112:115], v212 offset:0\n"
    "v_mfma_f32_16x16x32_bf16 a[104:107], v[196:199], v[52:55], a[104:107]\n"
    "ds_read_b128 v[116:119], v213 offset:0\n"
    "v_mfma_f32_16x16x32_bf16 a[104:107], v[200:203], v[56:59], a[104:107]\n"
    "buffer_load_dwordx4 v223, s[48:51], s56 offen offset:3072 lds\n"
    "v_mfma_f32_16x16x32_bf16 a[104:107], v[204:207], v[60:63], a[104:107]\n"
    "ds_read_b128 v[120:123], v214 offset:0\n"
    "ds_read_b128 v[124:127], v215 offset:0\n"
    "v_mfma_f32_16x16x32_bf16 a[108:111], v[192:195], v[64:67], a[108:111]\n"
    "s_add_u32 s57, s57, 128\n"
    "v_exp_f32 v44, v20\n"
    "v_mfma_f32_16x16x32_bf16 a[108:111], v[196:199], v[68:71], a[108:111]\n"
    "v_exp_f32 v45, v21\n"
    "v_mfma_f32_16x16x32_bf16 a[108:111], v[200:203], v[72:75], a[108:111]\n"
    "s_add_u32 m0, s59, 98304\n"
    "v_exp_f32 v46, v22\n"
    "v_mfma_f32_16x16x32_bf16 a[108:111], v[204:207], v[76:79], a[108:111]\n"
    "s_add_u32 s56, s56, 0x4000\n"
    "v_exp_f32 v47, v23\n"
    "v_mfma_f32_16x16x32_bf16 v[24:27], v[144:147], a[128:131], v[32:35]\n"
    "ds_read_b64_tr_b16 v[192:193], v219 offset:49152\n"
    "v_cvt_pk_bf16_f32 v108, v44, v45\n"
    "v_mfma_f32_16x16x32_bf16 v[24:27], v[148:151], a[132:135], v[24:27]\n"
    "ds_read_b64_tr_b16 v[194:195], v219 offset:51200\n"
    "v_cvt_pk_bf16_f32 v109, v46, v47\n"
    "v_mfma_f32_16x16x32_bf16 v[24:27], v[152:155], a[136:139], v[24:27]\n"
    "ds_read_b64_tr_b16 v[196:197], v219 offset:53248\n"
    "v_or3_b32 v228, v228, v108, v109\n"
    "v_mfma_f32_16x16x32_bf16 v[24:27], v[156:159], a[140:143], v[24:27]\n"
    "ds_read_b64_tr_b16 v[198:199], v219 offset:55296\n"
    "v_mfma_f32_16x16x32_bf16 v[24:27], v[160:163], a[144:147], v[24:27]\n"
    "buffer_load_dwordx4 v224, s[88:91], s57 offen offset:0 lds\n"
    "v_mfma_f32_16x16x32_bf16 v[24:27], v[164:167], a[148:151], v[24:27]\n"
    "ds_read_b64_tr_b16 v[200:201], v219 offset:57344\n"
    "v_mfma_f32_16x16x32_bf16 v[24:27], v[168:171], a[152:155], v[24:27]\n"
    "s_waitcnt lgkmcnt(5)\n"
    "ds_read_b64_tr_b16 v[202:203], v219 offset:59392\n"
    "v_mfma_f32_16x16x32_bf16 v[24:27], v[172:175], a[156:159], v[24:27]\n"
    "ds_read_b64_tr_b16 v[204:205], v219 offset:61440\n"
    "ds_read_b64_tr_b16 v[206:207], v219 offset:63488\n"
    "v_mfma_f32_16x16x32_bf16 a[112:115], v[176:179], v[48:51], a[112:115]\n"
    "ds_read_b128 v[128:131], v212 offset:256\n"
    "v_mfma_f32_16x16x32_bf16 a[112:115], v[180:183], v[52:55], a[112:115]\n"
    "ds_read_b128 v[132:135], v213 offset:256\n"
    "v_mfma_f32_16x16x32_bf16 a[112:115], v[184:187], v[56:59], a[112:115]\n"
    "buffer_load_dwordx4 v225, s[88:91], s57 offen offset:1024 lds\n"
    "v_mfma_f32_16x16x32_bf16 a[112:115], v[188:191], v[60:63], a[112:115]\n"
    "ds_read_b128 v[136:139], v214 offset:256\n"
    "ds_read_b128 v[140:143], v215 offset:256\n"
    "v_mfma_f32_16x16x32_bf16 a[116:119], v[176:179], v[64:67], a[116:119]\n"
    "v_exp_f32 v40, v24\n"
    "v_mfma_f32_16x16x32_bf16 a[116:119], v[180:183], v[68:71], a[116:119]\n"
    "v_exp_f32 v41, v25\n"
    "v_mfma_f32_16x16x32_bf16 a[116:119], v[184:187], v[72:75], a[116:119]\n"
    "v_exp_f32 v42, v26\n"
    "v_mfma_f32_16x16x32_bf16 a[116:119], v[188:191], v[76:79], a[116:119]\n"
    "v_exp_f32 v43, v27\n"
    "v_mfma_f32_16x16x32_bf16 v[28:31], v[144:147], a[160:163], v[36:39]\n"
    "ds_read_b64_tr_b16 v[176:177], v216 offset:0\n"
    "v_cvt_pk_bf16_f32 v94, v40, v41\n"
    "v_mfma_f32_16x16x32_bf16 v[28:31], v[148:151], a[164:167], v[28:31]\n"
    "ds_read_b64_tr_b16 v[178:179], v216 offset:2048\n"
    "v_cvt_pk_bf16_f32 v95, v42, v43\n"
    "v_mfma_f32_16x16x32_bf16 v[28:31], v[152:155], a[168:171], v[28:31]\n"
    "ds_read_b64_tr_b16 v[180:181], v216 offset:4096\n"
    "v_or3_b32 v228, v228, v94, v95\n"
    "v_mfma_f32_16x16x32_bf16 v[28:31], v[156:159], a[172:175], v[28:31]\n"
    "ds_read_b64_tr_b16 v[182:183], v216 offset:6144\n"
    "v_mfma_f32_16x16x32_bf16 v[28:31], v[160:163], a[176:179], v[28:31]\n"
    "buffer_load_dwordx4 v226, s[88:91], s57 offen offset:2048 lds\n"
    "v_mfma_f32_16x16x32_bf16 v[28:31], v[164:167], a[180:183], v[28:31]\n"
    "ds_read_b64_tr_b16 v[184:185], v216 offset:8192\n"
    "v_mfma_f32_16x16x32_bf16 v[28:31], v[168:171], a[184:187], v[28:31]\n"
    "s_waitcnt lgkmcnt(5)\n"
    "ds_read_b64_tr_b16 v[186:187], v216 offset:10240\n"
    "v_mfma_f32_16x16x32_bf16 v[28:31], v[172:175], a[188:191], v[28:31]\n"
    "ds_read_b64_tr_b16 v[188:189], v216 offset:12288\n"
    "ds_read_b64_tr_b16 v[190:191], v216 offset:14336\n"
    "v_mfma_f32_16x16x32_bf16 a[120:123], v[192:195], v[48:51], a[120:123]\n"
    "ds_read_b128 v[144:147], v212 offset:8192\n"
    "v_mfma_f32_16x16x32_bf16 a[120:123], v[196:199], v[52:55], a[120:123]\n"
    "ds_read_b128 v[148:151], v213 offset:8192\n"
    "v_mfma_f32_16x16x32_bf16 a[120:123], v[200:203], v[56:59], a[120:123]\n"
    "buffer_load_dwordx4 v227, s[88:91], s57 offen offset:3072 lds\n"
    "v_mfma_f32_16x16x32_bf16 a[120:123], v[204:207], v[60:63], a[120:123]\n"
    "ds_read_b128 v[152:155], v214 offset:8192\n"
    "ds_read_b128 v[156:159], v215 offset:8192\n"
    "v_mfma_f32_16x16x32_bf16 a[124:127], v[192:195], v[64:67], a[124:127]\n"
    "s_add_u32 m0, s59, 49152\n"
    "v_exp_f32 v44, v28\n"
    "v_mfma_f32_16x16x32_bf16 a[124:127], v[196:199], v[68:71], a[124:127]\n"
    "v_exp_f32 v45, v29\n"
    "v_mfma_f32_16x16x32_bf16 a[124:127], v[200:203], v[72:75], a[124:127]\n"
    "s_waitcnt vmcnt(8)\n"
    "v_exp_f32 v46, v30\n"
    "v_mfma_f32_16x16x32_bf16 a[124:127], v[204:207], v[76:79], a[124:127]\n"
    "s_barrier\n"
    "v_exp_f32 v47, v31\n"
    "v_mfma_f32_16x16x32_bf16 v[0:3], v[112:115], a[128:131], v[32:35]\n"
    "ds_read_b64_tr_b16 v[192:193], v217 offset:0\n"
    "v_cvt_pk_bf16_f32 v110, v44, v45\n"
    "v_mfma_f32_16x16x32_bf16 v[0:3], v[116:119], a[132:135], v[0:3]\n"
    "ds_read_b64_tr_b16 v[194:195], v217 offset:2048\n"
    "v_cvt_pk_bf16_f32 v111, v46, v47\n"
    "v_mfma_f32_16x16x32_bf16 v[0:3], v[120:123], a[136:139], v[0:3]\n"
    "ds_read_b64_tr_b16 v[196:197], v217 offset:4096\n"
    "v_or3_b32 v228, v228, v110, v111\n"
    "v_mfma_f32_16x16x32_bf16 v[0:3], v[124:127], a[140:143], v[0:3]\n"
    "ds_read_b64_tr_b16 v[198:199], v217 offset:6144\n"
    "v_and_b32 v228, s60, v228\n"
    "v_mfma_f32_16x16x32_bf16 v[0:3], v[128:131], a[144:147], v[0:3]\n"
    "buffer_load_dwordx4 v220, s[48:51], s56 offen offset:0 lds\n"
    "v_mfma_f32_16x16x32_bf16 v[0:3], v[132:135], a[148:151], v[0:3]\n"
    "ds_read_b64_tr_b16 v[200:201], v217 offset:8192\n"
    "v_cmp_ne_u32 vcc, 0, v228\n"
    "v_mfma_f32_16x16x32_bf16 v[0:3], v[136:139], a[152:155], v[0:3]\n"
    "s_waitcnt lgkmcnt(5)\n"
    "ds_read_b64_tr_b16 v[202:203], v217 offset:10240\n"
    "v_mfma_f32_16x16x32_bf16 v[0:3], v[140:143], a[156:159], v[0:3]\n"
    "s_cbranch_vccnz fb_Lslow8\n"
    "fb_Lback8:\n"
    "ds_read_b64_tr_b16 v[204:205], v217 offset:12288\n"
    "ds_read_b64_tr_b16 v[206:207], v217 offset:14336\n"
    "v_mfma_f32_16x16x32_bf16 a[0:3], v[176:179], v[80:83], a[0:3]\n"
    "ds_read_b128 v[160:163], v212 offset:8448\n"
    "v_mfma_f32_16x16x32_bf16 a[0:3], v[180:183], v[84:87], a[0:3]\n"
    "ds_read_b128 v[164:167], v213 offset:8448\n"
    "v_mfma_f32_16x16x32_bf16 a[0:3], v[184:187], v[88:91], a[0:3]\n"
    "buffer_load_dwordx4 v221, s[48:51], s56 offen offset:1024 lds\n"
    "v_mfma_f32_16x16x32_bf16 a[0:3], v[188:191], v[92:95], a[0:3]\n"
    "ds_read_b128 v[168:171], v214 offset:8448\n"
    "ds_read_b128 v[172:175], v215 offset:8448\n"
    "v_mfma_f32_16x16x32_bf16 a[4:7], v[176:179], v[96:99], a[4:7]\n"
    "v_exp_f32 v40, v0\n"
    "v_mfma_f32_16x16x32_bf16 a[4:7], v[180:183], v[100:103], a[4:7]\n"
    "v_exp_f32 v41, v1\n"
    "v_mfma_f32_16x16x32_bf16 a[4:7], v[184:187], v[104:107], a[4:7]\n"
    "v_exp_f32 v42, v2\n"
    "v_mfma_f32_16x16x32_bf16 a[4:7], v[188:191], v[108:111], a[4:7]\n"
    "v_exp_f32 v43, v3\n"
    "v_mfma_f32_16x16x32_bf16 v[4:7], v[112:115], a[160:163], v[36:39]\n"
    "ds_read_b64_tr_b16 v[176:177], v218 offset:0\n"
    "v_cvt_pk_bf16_f32 v48, v40, v41\n"
    "v_mfma_f32_16x16x32_bf16 v[4:7], v[116:119], a[164:167], v[4:7]\n"
    "ds_read_b64_tr_b16 v[178:179], v218 offset:2048\n"
    "v_cvt_pk_bf16_f32 v49, v42, v43\n"
    "v_mfma_f32_16x16x32_bf16 v[4:7], v[120:123], a[168:171], v[4:7]\n"
    "ds_read_b64_tr_b16 v[180:181], v218 offset:4096\n"
    "v_or_b32 v228, v48, v49\n"
    "v_mfma_f32_16x16x32_bf16 v[4:7], v[124:127], a[172:175], v[4:7]\n"
    "ds_read_b64_tr_b16 v[182:183], v218 offset:6144\n"
    "v_mfma_f32_16x16x32_bf16 v[4:7], v[128:131], a[176:179], v[4:7]\n"
    "buffer_load_dwordx4 v222, s[48:51], s56 offen offset:2048 lds\n"
    "v_mfma_f32_16x16x32_bf16 v[4:7], v[132:135], a[180:183], v[4:7]\n"
    "ds_read_b64_tr_b16 v[184:185], v218 offset:8192\n"
    "v_mfma_f32_16x16x32_bf16 v[4:7], v[136:139], a[184:187], v[4:7]\n"
    "s_waitcnt lgkmcnt(5)\n"
    "ds_read_b64_tr_b16 v[186:187], v218 offset:10240\n"
    "v_mfma_f32_16x16x32_bf16 v[4:7], v[140:143], a[188:191], v[4:7]\n"
    "ds_read_b64_tr_b16 v[188:189], v218 offset:12288\n"
    "v_mfma_f32_16x16x32_bf16 a[8:11], v[192:195], v[80:83], a[8:11]\n"
    "ds_read_b64_tr_b16 v[190:191], v218 offset:14336\n"
    "v_mfma_f32_16x16x32_bf16 a[8:11], v[196:199], v[84:87], a[8:11]\n"
    "ds_read_b128 v[112:115], v212 offset:16384\n"
    "v_mfma_f32_16x16x32_bf16 a[8:11], v[200:203], v[88:91], a[8:11]\n"
    "buffer_load_dwordx4 v223, s[48:51], s56 offen offset:3072 lds\n"
    "v_mfma_f32_16x16x32_bf16 a[8:11], v[204:207], v[92:95], a[8:11]\n"
    "ds_read_b128 v[116:119], v213 offset:16384\n"
    "v_mfma_f32_16x16x32_bf16 a[12:15], v[192:195], v[96:99], a[12:15]\n"
    "s_add_u32 s57, s57, 128\n"
    "v_exp_f32 v44, v4\n"
    "v_mfma_f32_16x16x32_bf16 a[12:15], v[196:199], v[100:103], a[12:15]\n"
    "v_exp_f32 v45, v5\n"
    "v_mfma_f32_16x16x32_bf16 a[12:15], v[200:203], v[104:107], a[12:15]\n"
    "s_add_u32 m0, s59, 114688\n"
    "v_exp_f32 v46, v6\n"
    "v_mfma_f32_16x16x32_bf16 a[12:15], v[204:207], v[108:111], a[12:15]\n"
    "s_add_u32 s56, s56, 0x4000\n"
    "v_exp_f32 v47, v7\n"
    "v_mfma_f32_16x16x32_bf16 a[192:195], v[208:211], v[80:83], a[192:195]\n"
    "ds_read_b128 v[120:123], v214 offset:16384\n"
    "v_mfma_f32_16x16x32_bf16 a[192:195], v[208:211], v[84:87], a[192:195]\n"
    "ds_read_b128 v[124:127], v215 offset:16384\n"
    "v_mfma_f32_16x16x32_bf16 a[192:195], v[208:211], v[88:91], a[192:195]\n"
    "ds_read_b64_tr_b16 v[192:193], v219 offset:0\n"
    "v_mfma_f32_16x16x32_bf16 a[192:195], v[208:211], v[92:95], a[192:195]\n"
    "ds_read_b64_tr_b16 v[194:195], v219 offset:2048\n"
    "v_mfma_f32_16x16x32_bf16 v[8:11], v[144:147], a[128:131], v[32:35]\n"
    "ds_read_b64_tr_b16 v[196:197], v219 offset:4096\n"
    "v_cvt_pk_bf16_f32 v64, v44, v45\n"
    "v_mfma_f32_16x16x32_bf16 v[8:11], v[148:151], a[132:135], v[8:11]\n"
    "ds_read_b64_tr_b16 v[198:199], v219 offset:6144\n"
    "v_cvt_pk_bf16_f32 v65, v46, v47\n"
    "v_mfma_f32_16x16x32_bf16 v[8:11], v[152:155], a[136:139], v[8:11]\n"
    "ds_read_b64_tr_b16 v[200:201], v219 offset:8192\n"
    "v_or3_b32 v228, v228, v64, v65\n"
    "v_mfma_f32_16x16x32_bf16 v[8:11], v[156:159], a[140:143], v[8:11]\n"
    "ds_read_b64_tr_b16 v[202:203], v219 offset:10240\n"
    "v_mfma_f32_16x16x32_bf16 v[8:11], v[160:163], a[144:147], v[8:11]\n"
    "buffer_load_dwordx4 v224, s[88:91], s57 offen offset:0 lds\n"
    "v_mfma_f32_16x16x32_bf16 v[8:11], v[164:167], a[148:151], v[8:11]\n"
    "ds_read_b64_tr_b16 v[204:205], v219 offset:12288\n"
    "v_mfma_f32_16x16x32_bf16 v[8:11], v[168:171], a[152:155], v[8:11]\n"
    "s_waitcnt lgkmcnt(7)\n"
    "ds_read_b64_tr_b16 v[206:207], v219 offset:14336\n"
    "v_mfma_f32_16x16x32_bf16 v[8:11], v[172:175], a[156:159], v[8:11]\n"
    "ds_read_b128 v[128:131], v212 offset:16640\n"
    "v_mfma_f32_16x16x32_bf16 a[16:19], v[176:179], v[80:83], a[16:19]\n"
    "ds_read_b128 v[132:135], v213 offset:16640\n"
    "v_mfma_f32_16x16x32_bf16 a[16:19], v[180:183], v[84:87], a[16:19]\n"
    "ds_read_b128 v[136:139], v214 offset:16640\n"
    "v_mfma_f32_16x16x32_bf16 a[16:19], v[184:187], v[88:91], a[16:19]\n"
    "buffer_load_dwordx4 v225, s[88:91], s57 offen offset:1024 lds\n"
    "v_mfma_f32_16x16x32_bf16 a[16:19], v[188:191], v[92:95], a[16:19]\n"
    "ds_read_b128 v[140:143], v215 offset:16640\n"
    "v_mfma_f32_16x16x32_bf16 a[20:23], v[176:179], v[96:99], a[20:23]\n"
    "s_and_b32 s56, s56, 0xfffff\n"
    "v_exp_f32 v40, v8\n"
    "v_mfma_f32_16x16x32_bf16 a[20:23], v[180:183], v[100:103], a[20:23]\n"
    "v_exp_f32 v41, v9\n"
    "v_mfma_f32_16x16x32_bf16 a[20:23], v[184:187], v[104:107], a[20:23]\n"
    "v_exp_f32 v42, v10\n"
    "v_mfma_f32_16x16x32_bf16 a[20:23], v[188:191], v[108:111], a[20:23]\n"
    "v_exp_f32 v43, v11\n"
    "v_mfma_f32_16x16x32_bf16 v[12:15], v[144:147], a[160:163], v[36:39]\n"
    "ds_read_b64_tr_b16 v[176:177], v216 offset:16384\n"
    "v_cvt_pk_bf16_f32 v50, v40, v41\n"
    "v_mfma_f32_16x16x32_bf16 v[12:15], v[148:151], a[164:167], v[12:15]\n"
    "ds_read_b64_tr_b16 v[178:179], v216 offset:18432\n"
    "v_cvt_pk_bf16_f32 v51, v42, v43\n"
    "v_mfma_f32_16x16x32_bf16 v[12:15], v[152:155], a[168:171], v[12:15]\n"
    "ds_read_b64_tr_b16 v[180:181], v216 offset:20480\n"
    "v_or3_b32 v228, v228, v50, v51\n"
    "v_mfma_f32_16x16x32_bf16 v[12:15], v[156:159], a[172:175], v[12:15]\n"
    "ds_read_b64_tr_b16 v[182:183], v216 offset:22528\n"
    "v_mfma_f32_16x16x32_bf16 v[12:15], v[160:163], a[176:179], v[12:15]\n"
    "buffer_load_dwordx4 v226, s[88:91], s57 offen offset:2048 lds\n"
    "v_mfma_f32_16x16x32_bf16 v[12:15], v[164:167], a[180:183], v[12:15]\n"
    "ds_read_b64_tr_b16 v[184:185], v216 offset:24576\n"
    "v_mfma_f32_16x16x32_bf16 v[12:15], v[168:171], a[184:187], v[12:15]\n"
    "s_waitcnt lgkmcnt(5)\n"
    "ds_read_b64_tr_b16 v[186:187], v216 offset:26624\n"
    "v_mfma_f32_16x16x32_bf16 v[12:15], v[172:175], a[188:191], v[12:15]\n"
    "ds_read_b64_tr_b16 v[188:189], v216 offset:28672\n"
    "ds_read_b64_tr_b16 v[190:191], v216 offset:30720\n"
    "v_mfma_f32_16x16x32_bf16 a[24:27], v[192:195], v[80:83], a[24:27]\n"
    "ds_read_b128 v[144:147], v212 offset:24576\n"
    "v_mfma_f32_16x16x32_bf16 a[24:27], v[196:199], v[84:87], a[24:27]\n"
    "ds_read_b128 v[148:151], v213 offset:24576\n"
    "v_mfma_f32_16x16x32_bf16 a[24:27], v[200:203], v[88:91], a[24:27]\n"
    "buffer_load_dwordx4 v227, s[88:91], s57 offen offset:3072 lds\n"
    "v_mfma_f32_16x16x32_bf16 a[24:27], v[204:207], v[92:95], a[24:27]\n"
    "ds_read_b128 v[152:155], v214 offset:24576\n"
    "ds_read_b128 v[156:159], v215 offset:24576\n"
    "v_mfma_f32_16x16x32_bf16 a[28:31], v[192:195], v[96:99], a[28:31]\n"
    "s_add_u32 m0, s59, 0\n"
    "v_exp_f32 v44, v12\n"
    "v_mfma_f32_16x16x32_bf16 a[28:31], v[196:199], v[100:103], a[28:31]\n"
    "v_exp_f32 v45, v13\n"
    "v_mfma_f32_16x16x32_bf16 a[28:31], v[200:203], v[104:107], a[28:31]\n"
    "s_waitcnt vmcnt(8)\n"
    "v_exp_f32 v46, v14\n"
    "v_mfma_f32_16x16x32_bf16 a[28:31], v[204:207], v[108:111], a[28:31]\n"
    "s_barrier\n"
    "v_exp_f32 v47, v15\n"
    "v_mfma_f32_16x16x32_bf16 v[16:19], v[112:115], a[128:131], v[32:35]\n"
    "ds_read_b64_tr_b16 v[192:193], v217 offset:16384\n"
    "v_cvt_pk_bf16_f32 v66, v44, v45\n"
    "v_mfma_f32_16x16x32_bf16 v[16:19], v[116:119], a[132:135], v[16:19]\n"
    "ds_read_b64_tr_b16 v[194:195], v217 offset:18432\n"
    "v_cvt_pk_bf16_f32 v67, v46, v47\n"
    "v_mfma_f32_16x16x32_bf16 v[16:19], v[120:123], a[136:139], v[16:19]\n"
    "ds_read_b64_tr_b16 v[196:197], v217 offset:20480\n"
    "v_or3_b32 v228, v228, v66, v67\n"
    "v_mfma_f32_16x16x32_bf16 v[16:19], v[124:127], a[140:143], v[16:19]\n"
    "ds_read_b64_tr_b16 v[198:199], v217 offset:22528\n"
    "v_and_b32 v228, s60, v228\n"
    "v_mfma_f32_16x16x32_bf16 v[16:19], v[128:131], a[144:147], v[16:19]\n"
    "buffer_load_dwordx4 v220, s[48:51], s56 offen offset:0 lds\n"
    "v_mfma_f32_16x16x32_bf16 v[16:19], v[132:135], a[148:151], v[16:19]\n"
    "ds_read_b64_tr_b16 v[200:201], v217 offset:24576\n"
    "v_cmp_ne_u32 vcc, 0, v228\n"
    "v_mfma_f32_16x16x32_bf16 v[16:19], v[136:139], a[152:155], v[16:19]\n"
    "s_waitcnt lgkmcnt(5)\n"
    "ds_read_b64_tr_b16 v[202:203], v217 offset:26624\n"
    "v_mfma_f32_16x16x32_bf16 v[16:19], v[140:143], a[156:159], v[16:19]\n"
    "s_cbranch_vccnz fb_Lslow9\n"
    "fb_Lback9:\n"
    "ds_read_b64_tr_b16 v[204:205], v217 offset:28672\n"
    "ds_read_b64_tr_b16 v[206:207], v217 offset:30720\n"
    "v_mfma_f32_16x16x32_bf16 a[32:35], v[176:179], v[80:83], a[32:35]\n"
    "ds_read_b128 v[160:163], v212 offset:24832\n"
    "v_mfma_f32_16x16x32_bf16 a[32:35], v[180:183], v[84:87], a[32:35]\n"
    "ds_read_b128 v[164:167], v213 offset:24832\n"
    "v_mfma_f32_16x16x32_bf16 a[32:35], v[184:187], v[88:91], a[32:35]\n"
    "buffer_load_dwordx4 v221, s[48:51], s56 offen offset:1024 lds\n"
    "v_mfma_f32_16x16x32_bf16 a[32:35], v[188:191], v[92:95], a[32:35]\n"
    "ds_read_b128 v[168:171], v214 offset:24832\n"
    "ds_read_b128 v[172:175], v215 offset:24832\n"
    "v_mfma_f32_16x16x32_bf16 a[36:39], v[176:179], v[96:99], a[36:39]\n"
    "v_exp_f32 v40, v16\n"
    "v_mfma_f32_16x16x32_bf16 a[36:39], v[180:183], v[100:103], a[36:39]\n"
    "v_exp_f32 v41, v17\n"
    "v_mfma_f32_16x16x32_bf16 a[36:39], v[184:187], v[104:107], a[36:39]\n"
    "v_exp_f32 v42, v18\n"
    "v_mfma_f32_16x16x32_bf16 a[36:39], v[188:191], v[108:111], a[36:39]\n"
    "v_exp_f32 v43, v19\n"
    "v_mfma_f32_16x16x32_bf16 v[20:23], v[112:115], a[160:163], v[36:39]\n"
    "ds_read_b64_tr_b16 v[176:177], v218 offset:16384\n"
    "v_cvt_pk_bf16_f32 v52, v40, v41\n"
    "v_mfma_f32_16x16x32_bf16 v[20:23], v[116:119], a[164:167], v[20:23]\n"
    "ds_read_b64_tr_b16 v[178:179], v218 offset:18432\n"
    "v_cvt_pk_bf16_f32 v53, v42, v43\n"
    "v_mfma_f32_16x16x32_bf16 v[20:23], v[120:123], a[168:171], v[20:23]\n"
    "ds_read_b64_tr_b16 v[180:181], v218 offset:20480\n"
    "v_or_b32 v228, v52, v53\n"
    "v_mfma_f32_16x16x32_bf16 v[20:23], v[124:127], a[172:175], v[20:23]\n"
    "ds_read_b64_tr_b16 v[182:183], v218 offset:22528\n"
    "v_mfma_f32_16x16x32_bf16 v[20:23], v[128:131], a[176:179], v[20:23]\n"
    "buffer_load_dwordx4 v222, s[48:51], s56 offen offset:2048 lds\n"
    "v_mfma_f32_16x16x32_bf16 v[20:23], v[132:135], a[180:183], v[20:23]\n"
    "ds_read_b64_tr_b16 v[184:185], v218 offset:24576\n"
    "v_mfma_f32_16x16x32_bf16 v[20:23], v[136:139], a[184:187], v[20:23]\n"
    "s_waitcnt lgkmcnt(5)\n"
    "ds_read_b64_tr_b16 v[186:187], v218 offset:26624\n"
    "v_mfma_f32_16x16x32_bf16 v[20:23], v[140:143], a[188:191], v[20:23]\n"
    "ds_read_b64_tr_b16 v[188:189], v218 offset:28672\n"
    "ds_read_b64_tr_b16 v[190:191], v218 offset:30720\n"
    "v_mfma_f32_16x16x32_bf16 a[40:43], v[192:195], v[80:83], a[40:43]\n"
    "ds_read_b128 v[112:115], v212 offset:32768\n"
    "v_mfma_f32_16x16x32_bf16 a[40:43], v[196:199], v[84:87], a[40:43]\n"
    "ds_read_b128 v[116:119], v213 offset:32768\n"
    "v_mfma_f32_16x16x32_bf16 a[40:43], v[200:203], v[88:91], a[40:43]\n"
    "buffer_load_dwordx4 v223, s[48:51], s56 offen offset:3072 lds\n"
    "v_mfma_f32_16x16x32_bf16 a[40:43], v[204:207], v[92:95], a[40:43]\n"
    "ds_read_b128 v[120:123], v214 offset:32768\n"
    "ds_read_b128 v[124:127], v215 offset:32768\n"
    "v_mfma_f32_16x16x32_bf16 a[44:47], v[192:195], v[96:99], a[44:47]\n"
    "s_add_u32 s57, s57, 65152\n"
    "v_exp_f32 v44, v20\n"
    "v_mfma_f32_16x16x32_bf16 a[44:47], v[196:199], v[100:103], a[44:47]\n"
    "s_and_b32 s57, s57, 0xfffff\n"
    "v_exp_f32 v45, v21\n"
    "v_mfma_f32_16x16x32_bf16 a[44:47], v[200:203], v[104:107], a[44:47]\n"
    "s_add_u32 m0, s59, 65536\n"
    "v_exp_f32 v46, v22\n"
    "v_mfma_f32_16x16x32_bf16 a[44:47], v[204:207], v[108:111], a[44:47]\n"
    "s_add_u32 s56, s56, 0x4000\n"
    "v_exp_f32 v47, v23\n"
    "v_mfma_f32_16x16x32_bf16 v[24:27], v[144:147], a[128:131], v[32:35]\n"
    "ds_read_b64_tr_b16 v[192:193], v219 offset:16384\n"
    "v_cvt_pk_bf16_f32 v68, v44, v45\n"
    "v_mfma_f32_16x16x32_bf16 v[24:27], v[148:151], a[132:135], v[24:27]\n"
    "ds_read_b64_tr_b16 v[194:195], v219 offset:18432\n"
    "v_cvt_pk_bf16_f32 v69, v46, v47\n"
    "v_mfma_f32_16x16x32_bf16 v[24:27], v[152:155], a[136:139], v[24:27]\n"
    "ds_read_b64_tr_b16 v[196:197], v219 offset:20480\n"
    "v_or3_b32 v228, v228, v68, v69\n"
    "v_mfma_f32_16x16x32_bf16 v[24:27], v[156:159], a[140:143], v[24:27]\n"
    "ds_read_b64_tr_b16 v[198:199], v219 offset:22528\n"
    "v_mfma_f32_16x16x32_bf16 v[24:27], v[160:163], a[144:147], v[24:27]\n"
    "buffer_load_dwordx4 v224, s[88:91], s57 offen offset:0 lds\n"
    "v_mfma_f32_16x16x32_bf16 v[24:27], v[164:167], a[148:151], v[24:27]\n"
    "ds_read_b64_tr_b16 v[200:201], v219 offset:24576\n"
    "v_mfma_f32_16x16x32_bf16 v[24:27], v[168:171], a[152:155], v[24:27]\n"
    "s_waitcnt lgkmcnt(5)\n"
    "ds_read_b64_tr_b16 v[202:203], v219 offset:26624\n"
    "v_mfma_f32_16x16x32_bf16 v[24:27], v[172:175], a[156:159], v[24:27]\n"
    "ds_read_b64_tr_b16 v[204:205], v219 offset:28672\n"
    "ds_read_b64_tr_b16 v[206:207], v219 offset:30720\n"
    "v_mfma_f32_16x16x32_bf16 a[48:51], v[176:179], v[80:83], a[48:51]\n"
    "ds_read_b128 v[128:131], v212 offset:33024\n"
    "v_mfma_f32_16x16x32_bf16 a[48:51], v[180:183], v[84:87], a[48:51]\n"
    "ds_read_b128 v[132:135], v213 offset:33024\n"
    "v_mfma_f32_16x16x32_bf16 a[48:51], v[184:187], v[88:91], a[48:51]\n"
    "buffer_load_dwordx4 v225, s[88:91], s57 offen offset:1024 lds\n"
    "v_mfma_f32_16x16x32_bf16 a[48:51], v[188:191], v[92:95], a[48:51]\n"
    "ds_read_b128 v[136:139], v214 offset:33024\n"
    "ds_read_b128 v[140:143], v215 offset:33024\n"
    "v_mfma_f32_16x16x32_bf16 a[52:55], v[176:179], v[96:99], a[52:55]\n"
    "v_exp_f32 v40, v24\n"
    "v_mfma_f32_16x16x32_bf16 a[52:55], v[180:183], v[100:103], a[52:55]\n"
    "v_exp_f32 v41, v25\n"
    "v_mfma_f32_16x16x32_bf16 a[52:55], v[184:187], v[104:107], a[52:55]\n"
    "v_exp_f32 v42, v26\n"
    "v_mfma_f32_16x16x32_bf16 a[52:55], v[188:191], v[108:111], a[52:55]\n"
    "v_exp_f32 v43, v27\n"
    "v_mfma_f32_16x16x32_bf16 v[28:31], v[144:147], a[160:163], v[36:39]\n"
    "ds_read_b64_tr_b16 v[176:177], v216 offset:32768\n"
    "v_cvt_pk_bf16_f32 v54, v40, v41\n"
    "v_mfma_f32_16x16x32_bf16 v[28:31], v[148:151], a[164:167], v[28:31]\n"
    "ds_read_b64_tr_b16 v[178:179], v216 offset:34816\n"
    "v_cvt_pk_bf16_f32 v55, v42, v43\n"
    "v_mfma_f32_16x16x32_bf16 v[28:31], v[152:155], a[168:171], v[28:31]\n"
    "ds_read_b64_tr_b16 v[180:181], v216 offset:36864\n"
    "v_or3_b32 v228, v228, v54, v55\n"
    "v_mfma_f32_16x16x32_bf16 v[28:31], v[156:159], a[172:175], v[28:31]\n"
    "ds_read_b64_tr_b16 v[182:183], v216 offset:38912\n"
    "v_mfma_f32_16x16x32_bf16 v[28:31], v[160:163], a[176:179], v[28:31]\n"
    "buffer_load_dwordx4 v226, s[88:91], s57 offen offset:2048 lds\n"
    "v_mfma_f32_16x16x32_bf16 v[28:31], v[164:167], a[180:183], v[28:31]\n"
    "ds_read_b64_tr_b16 v[184:185], v216 offset:40960\n"
    "v_mfma_f32_16x16x32_bf16 v[28:31], v[168:171], a[184:187], v[28:31]\n"
    "s_waitcnt lgkmcnt(5)\n"
    "ds_read_b64_tr_b16 v[186:187], v216 offset:43008\n"
    "v_mfma_f32_16x16x32_bf16 v[28:31], v[172:175], a[188:191], v[28:31]\n"
    "ds_read_b64_tr_b16 v[188:189], v216 offset:45056\n"
    "ds_read_b64_tr_b16 v[190:191], v216 offset:47104\n"
    "v_mfma_f32_16x16x32_bf16 a[56:59], v[192:195], v[80:83], a[56:59]\n"
    "ds_read_b128 v[144:147], v212 offset:40960\n"
    "v_mfma_f32_16x16x32_bf16 a[56:59], v[196:199], v[84:87], a[56:59]\n"
    "ds_read_b128 v[148:151], v213 offset:40960\n"
    "v_mfma_f32_16x16x32_bf16 a[56:59], v[200:203], v[88:91], a[56:59]\n"
    "buffer_load_dwordx4 v227, s[88:91], s57 offen offset:3072 lds\n"
    "v_mfma_f32_16x16x32_bf16 a[56:59], v[204:207], v[92:95], a[56:59]\n"
    "ds_read_b128 v[152:155], v214 offset:40960\n"
    "ds_read_b128 v[156:159], v215 offset:40960\n"
    "v_mfma_f32_16x16x32_bf16 a[60:63], v[192:195], v[96:99], a[60:63]\n"
    "s_add_u32 m0, s59, 16384\n"
    "v_exp_f32 v44, v28\n"
    "v_mfma_f32_16x16x32_bf16 a[60:63], v[196:199], v[100:103], a[60:63]\n"
    "v_exp_f32 v45, v29\n"
    "v_mfma_f32_16x16x32_bf16 a[60:63], v[200:203], v[104:107], a[60:63]\n"
    "s_waitcnt vmcnt(8)\n"
    "v_exp_f32 v46, v30\n"
    "v_mfma_f32_16x16x32_bf16 a[60:63], v[204:207], v[108:111], a[60:63]\n"
    "s_barrier\n"
    "v_exp_f32 v47, v31\n"
    "v_mfma_f32_16x16x32_bf16 v[0:3], v[112:115], a[128:131], v[32:35]\n"
    "ds_read_b64_tr_b16 v[192:193], v217 offset:32768\n"
    "v_cvt_pk_bf16_f32 v70, v44, v45\n"
    "v_mfma_f32_16x16x32_bf16 v[0:3], v[116:119], a[132:135], v[0:3]\n"
    "ds_read_b64_tr_b16 v[194:195], v217 offset:34816\n"
    "v_cvt_pk_bf16_f32 v71, v46, v47\n"
    "v_mfma_f32_16x16x32_bf16 v[0:3], v[120:123], a[136:139], v[0:3]\n"
    "ds_read_b64_tr_b16 v[196:197], v217 offset:36864\n"
    "v_or3_b32 v228, v228, v70, v71\n"
    "v_mfma_f32_16x16x32_bf16 v[0:3], v[124:127], a[140:143], v[0:3]\n"
    "ds_read_b64_tr_b16 v[198:199], v217 offset:38912\n"
    "v_and_b32 v228, s60, v228\n"
    "v_mfma_f32_16x16x32_bf16 v[0:3], v[128:131], a[144:147], v[0:3]\n"
    "buffer_load_dwordx4 v220, s[48:51], s56 offen offset:0 lds\n"
    "v_mfma_f32_16x16x32_bf16 v[0:3], v[132:135], a[148:151], v[0:3]\n"
    "ds_read_b64_tr_b16 v[200:201], v217 offset:40960\n"
    "v_cmp_ne_u32 vcc, 0, v228\n"
    "v_mfma_f32_16x16x32_bf16 v[0:3], v[136:139], a[152:155], v[0:3]\n"
    "s_waitcnt lgkmcnt(5)\n"
    "ds_read_b64_tr_b16 v[202:203], v217 offset:43008\n"
    "v_mfma_f32_16x16x32_bf16 v[0:3], v[140:143], a[156:159], v[0:3]\n"
    "s_cbranch_vccnz fb_Lslow10\n"
    "fb_Lback10:\n"
    "ds_read_b64_tr_b16 v[204:205], v217 offset:45056\n"
    "ds_read_b64_tr_b16 v[206:207], v217 offset:47104\n"
    "v_mfma_f32_16x16x32_bf16 a[64:67], v[176:179], v[80:83], a[64:67]\n"
    "ds_read_b128 v[160:163], v212 offset:41216\n"
    "v_mfma_f32_16x16x32_bf16 a[64:67], v[180:183], v[84:87], a[64:67]\n"
    "ds_read_b128 v[164:167], v213 offset:41216\n"
    "v_mfma_f32_16x16x32_bf16 a[64:67], v[184:187], v[88:91], a[64:67]\n"
    "buffer_load_dwordx4 v221, s[48:51], s56 offen offset:1024 lds\n"
    "v_mfma_f32_16x16x32_bf16 a[64:67], v[188:191], v[92:95], a[64:67]\n"
    "ds_read_b128 v[168:171], v214 offset:41216\n"
    "ds_read_b128 v[172:175], v215 offset:41216\n"
    "v_mfma_f32_16x16x32_bf16 a[68:71], v[176:179], v[96:99], a[68:71]\n"
    "v_exp_f32 v40, v0\n"
    "v_mfma_f32_16x16x32_bf16 a[68:71], v[180:183], v[100:103], a[68:71]\n"
    "v_exp_f32 v41, v1\n"
    "v_mfma_f32_16x16x32_bf16 a[68:71], v[184:187], v[104:107], a[68:71]\n"
    "v_exp_f32 v42, v2\n"
    "v_mfma_f32_16x16x32_bf16 a[68:71], v[188:191], v[108:111], a[68:71]\n"
    "v_exp_f32 v43, v3\n"
    "v_mfma_f32_16x16x32_bf16 v[4:7], v[112:115], a[160:163], v[36:39]\n"
    "ds_read_b64_tr_b16 v[176:177], v218 offset:32768\n"
    "v_cvt_pk_bf16_f32 v56, v40, v41\n"
    "v_mfma_f32_16x16x32_bf16 v[4:7], v[116:119], a[164:167], v[4:7]\n"
    "ds_read_b64_tr_b16 v[178:179], v218 offset:34816\n"
    "v_cvt_pk_bf16_f32 v57, v42, v43\n"
    "v_mfma_f32_16x16x32_bf16 v[4:7], v[120:123], a[168:171], v[4:7]\n"
    "ds_read_b64_tr_b16 v[180:181], v218 offset:36864\n"
    "v_or_b32 v228, v56, v57\n"
    "v_mfma_f32_16x16x32_bf16 v[4:7], v[124:127], a[172:175], v[4:7]\n"
    "ds_read_b64_tr_b16 v[182:183], v218 offset:38912\n"
    "v_mfma_f32_16x16x32_bf16 v[4:7], v[128:131], a[176:179], v[4:7]\n"
    "buffer_load_dwordx4 v222, s[48:51], s56 offen offset:2048 lds\n"
    "v_mfma_f32_16x16x32_bf16 v[4:7], v[132:135], a[180:183], v[4:7]\n"
    "ds_read_b64_tr_b16 v[184:185], v218 offset:40960\n"
    "v_mfma_f32_16x16x32_bf16 v[4:7], v[136:139], a[184:187], v[4:7]\n"
    "s_waitcnt lgkmcnt(5)\n"
    "ds_read_b64_tr_b16 v[186:187], v218 offset:43008\n"
    "v_mfma_f32_16x16x32_bf16 v[4:7], v[140:143], a[188:191], v[4:7]\n"
    "ds_read_b64_tr_b16 v[188:189], v218 offset:45056\n"
    "v_mfma_f32_16x16x32_bf16 a[72:75], v[192:195], v[80:83], a[72:75]\n"
    "ds_read_b64_tr_b16 v[190:191], v218 offset:47104\n"
    "v_mfma_f32_16x16x32_bf16 a[72:75], v[196:199], v[84:87], a[72:75]\n"
    "ds_read_b128 v[112:115], v212 offset:49152\n"
    "v_mfma_f32_16x16x32_bf16 a[72:75], v[200:203], v[88:91], a[72:75]\n"
    "buffer_load_dwordx4 v223, s[48:51], s56 offen offset:3072 lds\n"
    "v_mfma_f32_16x16x32_bf16 a[72:75], v[204:207], v[92:95], a[72:75]\n"
    "ds_read_b128 v[116:119], v213 offset:49152\n"
    "v_mfma_f32_16x16x32_bf16 a[76:79], v[192:195], v[96:99], a[76:79]\n"
    "s_add_u32 s57, s57, 128\n"
    "v_exp_f32 v44, v4\n"
    "v_mfma_f32_16x16x32_bf16 a[76:79], v[196:199], v[100:103], a[76:79]\n"
    "v_exp_f32 v45, v5\n"
    "v_mfma_f32_16x16x32_bf16 a[76:79], v[200:203], v[104:107], a[76:79]\n"
    "s_add_u32 m0, s59, 81920\n"
    "v_exp_f32 v46, v6\n"
    "v_mfma_f32_16x16x32_bf16 a[76:79], v[204:207], v[108:111], a[76:79]\n"
    "s_add_u32 s56, s56, 0x4000\n"
    "v_exp_f32 v47, v7\n"
    "v_mfma_f32_16x16x32_bf16 a[196:199], v[208:211], v[96:99], a[196:199]\n"
    "ds_read_b128 v[120:123], v214 offset:49152\n"
    "v_mfma_f32_16x16x32_bf16 a[196:199], v[208:211], v[100:103], a[196:199]\n"
    "ds_read_b128 v[124:127], v215 offset:49152\n"
    "v_mfma_f32_16x16x32_bf16 a[196:199], v[208:211], v[104:107], a[196:199]\n"
    "ds_read_b64_tr_b16 v[192:193], v219 offset:32768\n"
    "v_mfma_f32_16x16x32_bf16 a[196:199], v[208:211], v[108:111], a[196:199]\n"
    "ds_read_b64_tr_b16 v[194:195], v219 offset:34816\n"
    "v_mfma_f32_16x16x32_bf16 v[8:11], v[144:147], a[128:131], v[32:35]\n"
    "ds_read_b64_tr_b16 v[196:197], v219 offset:36864\n"
    "v_cvt_pk_bf16_f32 v72, v44, v45\n"
    "v_mfma_f32_16x16x32_bf16 v[8:11], v[148:151], a[132:135], v[8:11]\n"
    "ds_read_b64_tr_b16 v[198:199], v219 offset:38912\n"
    "v_cvt_pk_bf16_f32 v73, v46, v47\n"
    "v_mfma_f32_16x16x32_bf16 v[8:11], v[152:155], a[136:139], v[8:11]\n"
    "ds_read_b64_tr_b16 v[200:201], v219 offset:40960\n"
    "v_or3_b32 v228, v228, v72, v73\n"
    "v_mfma_f32_16x16x32_bf16 v[8:11], v[156:159], a[140:143], v[8:11]\n"
    "ds_read_b64_tr_b16 v[202:203], v219 offset:43008\n"
    "v_mfma_f32_16x16x32_bf16 v[8:11], v[160:163], a[144:147], v[8:11]\n"
    "buffer_load_dwordx4 v224, s[88:91], s57 offen offset:0 lds\n"
    "v_mfma_f32_16x16x32_bf16 v[8:11], v[164:167], a[148:151], v[8:11]\n"
    "ds_read_b64_tr_b16 v[204:205], v219 offset:45056\n"
    "v_mfma_f32_16x16x32_bf16 v[8:11], v[168:171], a[152:155], v[8:11]\n"
    "s_waitcnt lgkmcnt(7)\n"
    "ds_read_b64_tr_b16 v[206:207], v219 offset:47104\n"
    "v_mfma_f32_16x16x32_bf16 v[8:11], v[172:175], a[156:159], v[8:11]\n"
    "ds_read_b128 v[128:131], v212 offset:49408\n"
    "v_mfma_f32_16x16x32_bf16 a[80:83], v[176:179], v[80:83], a[80:83]\n"
    "ds_read_b128 v[132:135], v213 offset:49408\n"
    "v_mfma_f32_16x16x32_bf16 a[80:83], v[180:183], v[84:87], a[80:83]\n"
    "ds_read_b128 v[136:139], v214 offset:49408\n"
    "v_mfma_f32_16x16x32_bf16 a[80:83], v[184:187], v[88:91], a[80:83]\n"
    "buffer_load_dwordx4 v225, s[88:91], s57 offen offset:1024 lds\n"
    "v_mfma_f32_16x16x32_bf16 a[80:83], v[188:191], v[92:95], a[80:83]\n"
    "ds_read_b128 v[140:143], v215 offset:49408\n"
    "v_mfma_f32_16x16x32_bf16 a[84:87], v[176:179], v[96:99], a[84:87]\n"
    "v_exp_f32 v40, v8\n"
    "v_mfma_f32_16x16x32_bf16 a[84:87], v[180:183], v[100:103], a[84:87]\n"
    "v_exp_f32 v41, v9\n"
    "v_mfma_f32_16x16x32_bf16 a[84:87], v[184:187], v[104:107], a[84:87]\n"
    "v_exp_f32 v42, v10\n"
    "v_mfma_f32_16x16x32_bf16 a[84:87], v[188:191], v[108:111], a[84:87]\n"
    "v_exp_f32 v43, v11\n"
    "v_mfma_f32_16x16x32_bf16 v[12:15], v[144:147], a[160:163], v[36:39]\n"
    "ds_read_b64_tr_b16 v[176:177], v216 offset:49152\n"
    "v_cvt_pk_bf16_f32 v58, v40, v41\n"
    "v_mfma_f32_16x16x32_bf16 v[12:15], v[148:151], a[164:167], v[12:15]\n"
    "ds_read_b64_tr_b16 v[178:179], v216 offset:51200\n"
    "v_cvt_pk_bf16_f32 v59, v42, v43\n"
    "v_mfma_f32_16x16x32_bf16 v[12:15], v[152:155], a[168:171], v[12:15]\n"
    "ds_read_b64_tr_b16 v[180:181], v216 offset:53248\n"
    "v_or3_b32 v228, v228, v58, v59\n"
    "v_mfma_f32_16x16x32_bf16 v[12:15], v[156:159], a[172:175], v[12:15]\n"
    "ds_read_b64_tr_b16 v[182:183], v216 offset:55296\n"
    "v_mfma_f32_16x16x32_bf16 v[12:15], v[160:163], a[176:179], v[12:15]\n"
    "buffer_load_dwordx4 v226, s[88:91], s57 offen offset:2048 lds\n"
    "v_mfma_f32_16x16x32_bf16 v[12:15], v[164:167], a[180:183], v[12:15]\n"
    "ds_read_b64_tr_b16 v[184:185], v216 offset:57344\n"
    "v_mfma_f32_16x16x32_bf16 v[12:15], v[168:171], a[184:187], v[12:15]\n"
    "s_waitcnt lgkmcnt(5)\n"
    "ds_read_b64_tr_b16 v[186:187], v216 offset:59392\n"
    "v_mfma_f32_16x16x32_bf16 v[12:15], v[172:175], a[188:191], v[12:15]\n"
    "ds_read_b64_tr_b16 v[188:189], v216 offset:61440\n"
    "ds_read_b64_tr_b16 v[190:191], v216 offset:63488\n"
    "v_mfma_f32_16x16x32_bf16 a[88:91], v[192:195], v[80:83], a[88:91]\n"
    "ds_read_b128 v[144:147], v212 offset:57344\n"
    "v_mfma_f32_16x16x32_bf16 a[88:91], v[196:199], v[84:87], a[88:91]\n"
    "ds_read_b128 v[148:151], v213 offset:57344\n"
    "v_mfma_f32_16x16x32_bf16 a[88:91], v[200:203], v[88:91], a[88:91]\n"
    "buffer_load_dwordx4 v227, s[88:91], s57 offen offset:3072 lds\n"
    "v_mfma_f32_16x16x32_bf16 a[88:91], v[204:207], v[92:95], a[88:91]\n"
    "ds_read_b128 v[152:155], v214 offset:57344\n"
    "ds_read_b128 v[156:159], v215 offset:57344\n"
    "v_mfma_f32_16x16x32_bf16 a[92:95], v[192:195], v[96:99], a[92:95]\n"
    "s_add_u32 m0, s59, 32768\n"
    "v_exp_f32 v44, v12\n"
    "v_mfma_f32_16x16x32_bf16 a[92:95], v[196:199], v[100:103], a[92:95]\n"
    "v_exp_f32 v45, v13\n"
    "v_mfma_f32_16x16x32_bf16 a[92:95], v[200:203], v[104:107], a[92:95]\n"
    "s_waitcnt vmcnt(8)\n"
    "v_exp_f32 v46, v14\n"
    "v_mfma_f32_16x16x32_bf16 a[92:95], v[204:207], v[108:111], a[92:95]\n"
    "s_barrier\n"
    "v_exp_f32 v47, v15\n"
    "v_mfma_f32_16x16x32_bf16 v[16:19], v[112:115], a[128:131], v[32:35]\n"
    "ds_read_b64_tr_b16 v[192:193], v217 offset:49152\n"
    "v_cvt_pk_bf16_f32 v74, v44, v45\n"
    "v_mfma_f32_16x16x32_bf16 v[16:19], v[116:119], a[132:135], v[16:19]\n"
    "ds_read_b64_tr_b16 v[194:195], v217 offset:51200\n"
    "v_cvt_pk_bf16_f32 v75, v46, v47\n"
    "v_mfma_f32_16x16x32_bf16 v[16:19], v[120:123], a[136:139], v[16:19]\n"
    "ds_read_b64_tr_b16 v[196:197], v217 offset:53248\n"
    "v_or3_b32 v228, v228, v74, v75\n"
    "v_mfma_f32_16x16x32_bf16 v[16:19], v[124:127], a[140:143], v[16:19]\n"
    "ds_read_b64_tr_b16 v[198:199], v217 offset:55296\n"
    "v_and_b32 v228, s60, v228\n"
    "v_mfma_f32_16x16x32_bf16 v[16:19], v[128:131], a[144:147], v[16:19]\n"
    "buffer_load_dwordx4 v220, s[48:51], s56 offen offset:0 lds\n"
    "v_mfma_f32_16x16x32_bf16 v[16:19], v[132:135], a[148:151], v[16:19]\n"
    "ds_read_b64_tr_b16 v[200:201], v217 offset:57344\n"
    "v_cmp_ne_u32 vcc, 0, v228\n"
    "v_mfma_f32_16x16x32_bf16 v[16:19], v[136:139], a[152:155], v[16:19]\n"
    "s_waitcnt lgkmcnt(5)\n"
    "ds_read_b64_tr_b16 v[202:203], v217 offset:59392\n"
    "v_mfma_f32_16x16x32_bf16 v[16:19], v[140:143], a[156:159], v[16:19]\n"
    "s_cbranch_vccnz fb_Lslow11\n"
    "fb_Lback11:\n"
    "ds_read_b64_tr_b16 v[204:205], v217 offset:61440\n"
    "ds_read_b64_tr_b16 v[206:207], v217 offset:63488\n"
    "v_mfma_f32_16x16x32_bf16 a[96:99], v[176:179], v[80:83], a[96:99]\n"
    "ds_read_b128 v[160:163], v212 offset:57600\n"
    "v_mfma_f32_16x16x32_bf16 a[96:99], v[180:183], v[84:87], a[96:99]\n"
    "ds_read_b128 v[164:167], v213 offset:57600\n"
    "v_mfma_f32_16x16x32_bf16 a[96:99], v[184:187], v[88:91], a[96:99]\n"
    "buffer_load_dwordx4 v221, s[48:51], s56 offen offset:1024 lds\n"
    "v_mfma_f32_16x16x32_bf16 a[96:99], v[188:191], v[92:95], a[96:99]\n"
    "ds_read_b128 v[168:171], v214 offset:57600\n"
    "ds_read_b128 v[172:175], v215 offset:57600\n"
    "v_mfma_f32_16x16x32_bf16 a[100:103], v[176:179], v[96:99], a[100:103]\n"
    "v_exp_f32 v40, v16\n"
    "v_mfma_f32_16x16x32_bf16 a[100:103], v[180:183], v[100:103], a[100:103]\n"
    "v_exp_f32 v41, v17\n"
    "v_mfma_f32_16x16x32_bf16 a[100:103], v[184:187], v[104:107], a[100:103]\n"
    "v_exp_f32 v42, v18\n"
    "v_mfma_f32_16x16x32_bf16 a[100:103], v[188:191], v[108:111], a[100:103]\n"
    "v_exp_f32 v43, v19\n"
    "v_mfma_f32_16x16x32_bf16 v[20:23], v[112:115], a[160:163], v[36:39]\n"
    "ds_read_b64_tr_b16 v[176:177], v218 offset:49152\n"
    "v_cvt_pk_bf16_f32 v60, v40, v41\n"
    "v_mfma_f32_16x16x32_bf16 v[20:23], v[116:119], a[164:167], v[20:23]\n"
    "ds_read_b64_tr_b16 v[178:179], v218 offset:51200\n"
    "v_cvt_pk_bf16_f32 v61, v42, v43\n"
    "v_mfma_f32_16x16x32_bf16 v[20:23], v[120:123], a[168:171], v[20:23]\n"
    "ds_read_b64_tr_b16 v[180:181], v218 offset:53248\n"
    "v_or_b32 v228, v60, v61\n"
    "v_mfma_f32_16x16x32_bf16 v[20:23], v[124:127], a[172:175], v[20:23]\n"
    "ds_read_b64_tr_b16 v[182:183], v218 offset:55296\n"
    "v_mfma_f32_16x16x32_bf16 v[20:23], v[128:131], a[176:179], v[20:23]\n"
    "buffer_load_dwordx4 v222, s[48:51], s56 offen offset:2048 lds\n"
    "v_mfma_f32_16x16x32_bf16 v[20:23], v[132:135], a[180:183], v[20:23]\n"
    "ds_read_b64_tr_b16 v[184:185], v218 offset:57344\n"
    "v_mfma_f32_16x16x32_bf16 v[20:23], v[136:139], a[184:187], v[20:23]\n"
    "s_waitcnt lgkmcnt(5)\n"
    "ds_read_b64_tr_b16 v[186:187], v218 offset:59392\n"
    "v_mfma_f32_16x16x32_bf16 v[20:23], v[140:143], a[188:191], v[20:23]\n"
    "ds_read_b64_tr_b16 v[188:189], v218 offset:61440\n"
    "ds_read_b64_tr_b16 v[190:191], v218 offset:63488\n"
    "v_mfma_f32_16x16x32_bf16 a[104:107], v[192:195], v[80:83], a[104:107]\n"
    "ds_read_b128 v[112:115], v212 offset:0\n"
    "v_mfma_f32_16x16x32_bf16 a[104:107], v[196:199], v[84:87], a[104:107]\n"
    "ds_read_b128 v[116:119], v213 offset:0\n"
    "v_mfma_f32_16x16x32_bf16 a[104:107], v[200:203], v[88:91], a[104:107]\n"
    "buffer_load_dwordx4 v223, s[48:51], s56 offen offset:3072 lds\n"
    "v_mfma_f32_16x16x32_bf16 a[104:107], v[204:207], v[92:95], a[104:107]\n"
    "ds_read_b128 v[120:123], v214 offset:0\n"
    "ds_read_b128 v[124:127], v215 offset:0\n"
    "v_mfma_f32_16x16x32_bf16 a[108:111], v[192:195], v[96:99], a[108:111]\n"
    "s_add_u32 s57, s57, 128\n"
    "v_exp_f32 v44, v20\n"
    "v_mfma_f32_16x16x32_bf16 a[108:111], v[196:199], v[100:103], a[108:111]\n"
    "v_exp_f32 v45, v21\n"
    "v_mfma_f32_16x16x32_bf16 a[108:111], v[200:203], v[104:107], a[108:111]\n"
    "s_add_u32 m0, s59, 98304\n"
    "v_exp_f32 v46, v22\n"
    "v_mfma_f32_16x16x32_bf16 a[108:111], v[204:207], v[108:111], a[108:111]\n"
    "s_add_u32 s56, s56, 0x4000\n"
    "v_exp_f32 v47, v23\n"
    "v_mfma_f32_16x16x32_bf16 v[24:27], v[144:147], a[128:131], v[32:35]\n"
    "ds_read_b64_tr_b16 v[192:193], v219 offset:49152\n"
    "v_cvt_pk_bf16_f32 v76, v44, v45\n"
    "v_mfma_f32_16x16x32_bf16 v[24:27], v[148:151], a[132:135], v[24:27]\n"
    "ds_read_b64_tr_b16 v[194:195], v219 offset:51200\n"
    "v_cvt_pk_bf16_f32 v77, v46, v47\n"
    "v_mfma_f32_16x16x32_bf16 v[24:27], v[152:155], a[136:139], v[24:27]\n"
    "ds_read_b64_tr_b16 v[196:197], v219 offset:53248\n"
    "v_or3_b32 v228, v228, v76, v77\n"
    "v_mfma_f32_16x16x32_bf16 v[24:27], v[156:159], a[140:143], v[24:27]\n"
    "ds_read_b64_tr_b16 v[198:199], v219 offset:55296\n"
    "v_mfma_f32_16x16x32_bf16 v[24:27], v[160:163], a[144:147], v[24:27]\n"
    "buffer_load_dwordx4 v224, s[88:91], s57 offen offset:0 lds\n"
    "v_mfma_f32_16x16x32_bf16 v[24:27], v[164:167], a[148:151], v[24:27]\n"
    "ds_read_b64_tr_b16 v[200:201], v219 offset:57344\n"
    "v_mfma_f32_16x16x32_bf16 v[24:27], v[168:171], a[152:155], v[24:27]\n"
    "s_waitcnt lgkmcnt(5)\n"
    "ds_read_b64_tr_b16 v[202:203], v219 offset:59392\n"
    "v_mfma_f32_16x16x32_bf16 v[24:27], v[172:175], a[156:159], v[24:27]\n"
    "ds_read_b64_tr_b16 v[204:205], v219 offset:61440\n"
    "ds_read_b64_tr_b16 v[206:207], v219 offset:63488\n"
    "v_mfma_f32_16x16x32_bf16 a[112:115], v[176:179], v[80:83], a[112:115]\n"
    "ds_read_b128 v[128:131], v212 offset:256\n"
    "v_mfma_f32_16x16x32_bf16 a[112:115], v[180:183], v[84:87], a[112:115]\n"
    "ds_read_b128 v[132:135], v213 offset:256\n"
    "v_mfma_f32_16x16x32_bf16 a[112:115], v[184:187], v[88:91], a[112:115]\n"
    "buffer_load_dwordx4 v225, s[88:91], s57 offen offset:1024 lds\n"
    "v_mfma_f32_16x16x32_bf16 a[112:115], v[188:191], v[92:95], a[112:115]\n"
    "ds_read_b128 v[136:139], v214 offset:256\n"
    "ds_read_b128 v[140:143], v215 offset:256\n"
    "v_mfma_f32_16x16x32_bf16 a[116:119], v[176:179], v[96:99], a[116:119]\n"
    "v_exp_f32 v40, v24\n"
    "v_mfma_f32_16x16x32_bf16 a[116:119], v[180:183], v[100:103], a[116:119]\n"
    "v_exp_f32 v41, v25\n"
    "v_mfma_f32_16x16x32_bf16 a[116:119], v[184:187], v[104:107], a[116:119]\n"
    "v_exp_f32 v42, v26\n"
    "v_mfma_f32_16x16x32_bf16 a[116:119], v[188:191], v[108:111], a[116:119]\n"
    "v_exp_f32 v43, v27\n"
    "v_mfma_f32_16x16x32_bf16 v[28:31], v[144:147], a[160:163], v[36:39]\n"
    "ds_read_b64_tr_b16 v[176:177], v216 offset:0\n"
    "v_cvt_pk_bf16_f32 v62, v40, v41\n"
    "v_mfma_f32_16x16x32_bf16 v[28:31], v[148:151], a[164:167], v[28:31]\n"
    "ds_read_b64_tr_b16 v[178:179], v216 offset:2048\n"
    "v_cvt_pk_bf16_f32 v63, v42, v43\n"
    "v_mfma_f32_16x16x32_bf16 v[28:31], v[152:155], a[168:171], v[28:31]\n"
    "ds_read_b64_tr_b16 v[180:181], v216 offset:4096\n"
    "v_or3_b32 v228, v228, v62, v63\n"
    "v_mfma_f32_16x16x32_bf16 v[28:31], v[156:159], a[172:175], v[28:31]\n"
    "ds_read_b64_tr_b16 v[182:183], v216 offset:6144\n"
    "v_mfma_f32_16x16x32_bf16 v[28:31], v[160:163], a[176:179], v[28:31]\n"
    "buffer_load_dwordx4 v226, s[88:91], s57 offen offset:2048 lds\n"
    "v_mfma_f32_16x16x32_bf16 v[28:31], v[164:167], a[180:183], v[28:31]\n"
    "s_sub_u32 s58, s58, 1\n"
    "ds_read_b64_tr_b16 v[184:185], v216 offset:8192\n"
    "v_mfma_f32_16x16x32_bf16 v[28:31], v[168:171], a[184:187], v[28:31]\n"
    "s_waitcnt lgkmcnt(5)\n"
    "ds_read_b64_tr_b16 v[186:187], v216 offset:10240\n"
    "v_mfma_f32_16x16x32_bf16 v[28:31], v[172:175], a[188:191], v[28:31]\n"
    "ds_read_b64_tr_b16 v[188:189], v216 offset:12288\n"
    "ds_read_b64_tr_b16 v[190:191], v216 offset:14336\n"
    "v_mfma_f32_16x16x32_bf16 a[120:123], v[192:195], v[80:83], a[120:123]\n"
    "ds_read_b128 v[144:147], v212 offset:8192\n"
    "v_mfma_f32_16x16x32_bf16 a[120:123], v[196:199], v[84:87], a[120:123]\n"
    "ds_read_b128 v[148:151], v213 offset:8192\n"
    "v_mfma_f32_16x16x32_bf16 a[120:123], v[200:203], v[88:91], a[120:123]\n"
    "buffer_load_dwordx4 v227, s[88:91], s57 offen offset:3072 lds\n"
    "v_mfma_f32_16x16x32_bf16 a[120:123], v[204:207], v[92:95], a[120:123]\n"
    "ds_read_b128 v[152:155], v214 offset:8192\n"
    "ds_read_b128 v[156:159], v215 offset:8192\n"
    "v_mfma_f32_16x16x32_bf16 a[124:127], v[192:195], v[96:99], a[124:127]\n"
    "s_add_u32 m0, s59, 49152\n"
    "v_exp_f32 v44, v28\n"
    "v_mfma_f32_16x16x32_bf16 a[124:127], v[196:199], v[100:103], a[124:127]\n"
    "s_cmp_lg_u32 s58, 0\n"
    "v_exp_f32 v45, v29\n"
    "v_mfma_f32_16x16x32_bf16 a[124:127], v[200:203], v[104:107], a[124:127]\n"
    "s_waitcnt vmcnt(8)\n"
    "v_exp_f32 v46, v30\n"
    "v_mfma_f32_16x16x32_bf16 a[124:127], v[204:207], v[108:111], a[124:127]\n"
    "s_barrier\n"
    "v_exp_f32 v47, v31\n"
    "s_cbranch_scc1 fb_Lloop\n"
    "s_mov_b64 s[44:45], %[c]\n"
    "s_and_b32 s45, s45, 0xffff\n"
    "s_mov_b32 s46, 896\n"
    "v_lshlrev_b32 v232, 1, v250\n"
    "buffer_load_ushort a228, v232, s[44:47], 0 offen offset:0\n"
    "buffer_load_ushort a229, v232, s[44:47], 0 offen offset:128\n"
    "buffer_load_ushort a230, v232, s[44:47], 0 offen offset:256\n"
    "buffer_load_ushort a231, v232, s[44:47], 0 offen offset:384\n"
    "buffer_load_ushort a232, v232, s[44:47], 0 offen offset:512\n"
    "buffer_load_ushort a233, v232, s[44:47], 0 offen offset:640\n"
    "buffer_load_ushort a234, v232, s[44:47], 0 offen offset:768\n"
    "s_lshl_b32 s61, %[wv], 10\n"
    "s_add_u32 s61, s61, 131072\n"
    "v_lshrrev_b32 v248, 3, v248\n"
    "v_add_u32 v248, s61, v248\n"
    "v_mfma_f32_16x16x32_bf16 v[0:3], v[112:115], a[128:131], v[32:35]\n"
    "ds_read_b64_tr_b16 v[192:193], v217 offset:0\n"
    "v_cvt_pk_bf16_f32 v78, v44, v45\n"
    "v_mfma_f32_16x16x32_bf16 v[0:3], v[116:119], a[132:135], v[0:3]\n"
    "ds_read_b64_tr_b16 v[194:195], v217 offset:2048\n"
    "v_cvt_pk_bf16_f32 v79, v46, v47\n"
    "v_mfma_f32_16x16x32_bf16 v[0:3], v[120:123], a[136:139], v[0:3]\n"
    "ds_read_b64_tr_b16 v[196:197], v217 offset:4096\n"
    "v_or3_b32 v228, v228, v78, v79\n"
    "v_mfma_f32_16x16x32_bf16 v[0:3], v[124:127], a[140:143], v[0:3]\n"
    "ds_read_b64_tr_b16 v[198:199], v217 offset:6144\n"
    "v_and_b32 v228, s60, v228\n"
    "v_mfma_f32_16x16x32_bf16 v[0:3], v[128:131], a[144:147], v[0:3]\n"
    "buffer_load_dwordx4 v220, s[48:51], s56 offen offset:0 lds\n"
    "v_mfma_f32_16x16x32_bf16 v[0:3], v[132:135], a[148:151], v[0:3]\n"
    "ds_read_b64_tr_b16 v[200:201], v217 offset:8192\n"
    "v_cmp_ne_u32 vcc, 0, v228\n"
    "v_mfma_f32_16x16x32_bf16 v[0:3], v[136:139], a[152:155], v[0:3]\n"
    "s_waitcnt lgkmcnt(5)\n"
    "ds_read_b64_tr_b16 v[202:203], v217 offset:10240\n"
    "v_mfma_f32_16x16x32_bf16 v[0:3], v[140:143], a[156:159], v[0:3]\n"
    "s_cbranch_vccnz fb_Lslow60\n"
    "fb_Lback60:\n"
    "ds_read_b64_tr_b16 v[204:205], v217 offset:12288\n"
    "ds_read_b64_tr_b16 v[206:207], v217 offset:14336\n"
    "v_mfma_f32_16x16x32_bf16 a[0:3], v[176:179], v[48:51], a[0:3]\n"
    "ds_read_b128 v[160:163], v212 offset:8448\n"
    "v_mfma_f32_16x16x32_bf16 a[0:3], v[180:183], v[52:55], a[0:3]\n"
    "ds_read_b128 v[164:167], v213 offset:8448\n"
    "v_mfma_f32_16x16x32_bf16 a[0:3], v[184:187], v[56:59], a[0:3]\n"
    "buffer_load_dwordx4 v221, s[48:51], s56 offen offset:1024 lds\n"
    "v_mfma_f32_16x16x32_bf16 a[0:3], v[188:191], v[60:63], a[0:3]\n"
    "ds_read_b128 v[168:171], v214 offset:8448\n"
    "ds_read_b128 v[172:175], v215 offset:8448\n"
    "v_mfma_f32_16x16x32_bf16 a[4:7], v[176:179], v[64:67], a[4:7]\n"
    "v_exp_f32 v40, v0\n"
    "v_mfma_f32_16x16x32_bf16 a[4:7], v[180:183], v[68:71], a[4:7]\n"
    "v_exp_f32 v41, v1\n"
    "v_mfma_f32_16x16x32_bf16 a[4:7], v[184:187], v[72:75], a[4:7]\n"
    "v_exp_f32 v42, v2\n"
    "v_mfma_f32_16x16x32_bf16 a[4:7], v[188:191], v[76:79], a[4:7]\n"
    "v_exp_f32 v43, v3\n"
    "v_mfma_f32_16x16x32_bf16 v[4:7], v[112:115], a[160:163], v[36:39]\n"
    "ds_read_b64_tr_b16 v[176:177], v218 offset:0\n"
    "v_cvt_pk_bf16_f32 v80, v40, v41\n"
    "v_mfma_f32_16x16x32_bf16 v[4:7], v[116:119], a[164:167], v[4:7]\n"
    "ds_read_b64_tr_b16 v[178:179], v218 offset:2048\n"
    "v_cvt_pk_bf16_f32 v81, v42, v43\n"
    "v_mfma_f32_16x16x32_bf16 v[4:7], v[120:123], a[168:171], v[4:7]\n"
    "ds_read_b64_tr_b16 v[180:181], v218 offset:4096\n"
    "v_or_b32 v228, v80, v81\n"
    "v_mfma_f32_16x16x32_bf16 v[4:7], v[124:127], a[172:175], v[4:7]\n"
    "ds_read_b64_tr_b16 v[182:183], v218 offset:6144\n"
    "v_mfma_f32_16x16x32_bf16 v[4:7], v[128:131], a[176:179], v[4:7]\n"
    "buffer_load_dwordx4 v222, s[48:51], s56 offen offset:2048 lds\n"
    "v_mfma_f32_16x16x32_bf16 v[4:7], v[132:135], a[180:183], v[4:7]\n"
    "ds_read_b64_tr_b16 v[184:185], v218 offset:8192\n"
    "v_mfma_f32_16x16x32_bf16 v[4:7], v[136:139], a[184:187], v[4:7]\n"
    "s_waitcnt lgkmcnt(5)\n"
    "ds_read_b64_tr_b16 v[186:187], v218 offset:10240\n"
    "v_mfma_f32_16x16x32_bf16 v[4:7], v[140:143], a[188:191], v[4:7]\n"
    "ds_read_b64_tr_b16 v[188:189], v218 offset:12288\n"
    "ds_read_b64_tr_b16 v[190:191], v218 offset:14336\n"
    "v_mfma_f32_16x16x32_bf16 a[8:11], v[192:195], v[48:51], a[8:11]\n"
    "ds_read_b128 v[112:115], v212 offset:16384\n"
    "v_mfma_f32_16x16x32_bf16 a[8:11], v[196:199], v[52:55], a[8:11]\n"
    "ds_read_b128 v[116:119], v213 offset:16384\n"
    "v_mfma_f32_16x16x32_bf16 a[8:11], v[200:203], v[56:59], a[8:11]\n"
    "buffer_load_dwordx4 v223, s[48:51], s56 offen offset:3072 lds\n"
    "v_mfma_f32_16x16x32_bf16 a[8:11], v[204:207], v[60:63], a[8:11]\n"
    "ds_read_b128 v[120:123], v214 offset:16384\n"
    "ds_read_b128 v[124:127], v215 offset:16384\n"
    "v_mfma_f32_16x16x32_bf16 a[12:15], v[192:195], v[64:67], a[12:15]\n"
    "s_add_u32 s57, s57, 128\n"
    "v_exp_f32 v44, v4\n"
    "v_mfma_f32_16x16x32_bf16 a[12:15], v[196:199], v[68:71], a[12:15]\n"
    "v_exp_f32 v45, v5\n"
    "v_mfma_f32_16x16x32_bf16 a[12:15], v[200:203], v[72:75], a[12:15]\n"
    "s_add_u32 m0, s59, 114688\n"
    "v_exp_f32 v46, v6\n"
    "v_mfma_f32_16x16x32_bf16 a[12:15], v[204:207], v[76:79], a[12:15]\n"
    "s_add_u32 s56, s56, 0x4000\n"
    "v_exp_f32 v47, v7\n"
    "v_mfma_f32_16x16x32_bf16 a[192:195], v[208:211], v[48:51], a[192:195]\n"
    "ds_read_b128 a[200:203], v248 offset:0\n"
    "ds_read_b128 a[204:207], v248 offset:4096\n"
    "v_mfma_f32_16x16x32_bf16 a[192:195], v[208:211], v[52:55], a[192:195]\n"
    "ds_read_b128 a[208:211], v248 offset:8192\n"
    "ds_read_b128 a[212:215], v248 offset:12288\n"
    "v_mfma_f32_16x16x32_bf16 a[192:195], v[208:211], v[56:59], a[192:195]\n"
    "ds_read_b128 a[216:219], v248 offset:16384\n"
    "ds_read_b128 a[220:223], v248 offset:20480\n"
    "v_mfma_f32_16x16x32_bf16 a[192:195], v[208:211], v[60:63], a[192:195]\n"
    "ds_read_b128 a[224:227], v248 offset:24576\n"
    "v_mfma_f32_16x16x32_bf16 v[8:11], v[144:147], a[128:131], v[32:35]\n"
    "ds_read_b64_tr_b16 v[192:193], v219 offset:0\n"
    "v_cvt_pk_bf16_f32 v96, v44, v45\n"
    "v_mfma_f32_16x16x32_bf16 v[8:11], v[148:151], a[132:135], v[8:11]\n"
    "ds_read_b64_tr_b16 v[194:195], v219 offset:2048\n"
    "v_cvt_pk_bf16_f32 v97, v46, v47\n"
    "v_mfma_f32_16x16x32_bf16 v[8:11], v[152:155], a[136:139], v[8:11]\n"
    "ds_read_b64_tr_b16 v[196:197], v219 offset:4096\n"
    "v_or3_b32 v228, v228, v96, v97\n"
    "v_mfma_f32_16x16x32_bf16 v[8:11], v[156:159], a[140:143], v[8:11]\n"
    "ds_read_b64_tr_b16 v[198:199], v219 offset:6144\n"
    "v_mfma_f32_16x16x32_bf16 v[8:11], v[160:163], a[144:147], v[8:11]\n"
    "buffer_load_dwordx4 v224, s[88:91], s57 offen offset:0 lds\n"
    "v_mfma_f32_16x16x32_bf16 v[8:11], v[164:167], a[148:151], v[8:11]\n"
    "ds_read_b64_tr_b16 v[200:201], v219 offset:8192\n"
    "v_mfma_f32_16x16x32_bf16 v[8:11], v[168:171], a[152:155], v[8:11]\n"
    "s_waitcnt lgkmcnt(5)\n"
    "ds_read_b64_tr_b16 v[202:203], v219 offset:10240\n"
    "v_mfma_f32_16x16x32_bf16 v[8:11], v[172:175], a[156:159], v[8:11]\n"
    "ds_read_b64_tr_b16 v[204:205], v219 offset:12288\n"
    "ds_read_b64_tr_b16 v[206:207], v219 offset:14336\n"
    "v_mfma_f32_16x16x32_bf16 a[16:19], v[176:179], v[48:51], a[16:19]\n"
    "ds_read_b128 v[128:131], v212 offset:16640\n"
    "v_mfma_f32_16x16x32_bf16 a[16:19], v[180:183], v[52:55], a[16:19]\n"
    "ds_read_b128 v[132:135], v213 offset:16640\n"
    "v_mfma_f32_16x16x32_bf16 a[16:19], v[184:187], v[56:59], a[16:19]\n"
    "buffer_load_dwordx4 v225, s[88:91], s57 offen offset:1024 lds\n"
    "v_mfma_f32_16x16x32_bf16 a[16:19], v[188:191], v[60:63], a[16:19]\n"
    "ds_read_b128 v[136:139], v214 offset:16640\n"
    "ds_read_b128 v[140:143], v215 offset:16640\n"
    "v_mfma_f32_16x16x32_bf16 a[20:23], v[176:179], v[64:67], a[20:23]\n"
    "s_and_b32 s56, s56, 0xfffff\n"
    "v_exp_f32 v40, v8\n"
    "v_mfma_f32_16x16x32_bf16 a[20:23], v[180:183], v[68:71], a[20:23]\n"
    "v_exp_f32 v41, v9\n"
    "v_mfma_f32_16x16x32_bf16 a[20:23], v[184:187], v[72:75], a[20:23]\n"
    "v_exp_f32 v42, v10\n"
    "v_mfma_f32_16x16x32_bf16 a[20:23], v[188:191], v[76:79], a[20:23]\n"
    "v_exp_f32 v43, v11\n"
    "v_mfma_f32_16x16x32_bf16 v[12:15], v[144:147], a[160:163], v[36:39]\n"
    "ds_read_b64_tr_b16 v[176:177], v216 offset:16384\n"
    "v_cvt_pk_bf16_f32 v82, v40, v41\n"
    "v_mfma_f32_16x16x32_bf16 v[12:15], v[148:151], a[164:167], v[12:15]\n"
    "ds_read_b64_tr_b16 v[178:179], v216 offset:18432\n"
    "v_cvt_pk_bf16_f32 v83, v42, v43\n"
    "v_mfma_f32_16x16x32_bf16 v[12:15], v[152:155], a[168:171], v[12:15]\n"
    "ds_read_b64_tr_b16 v[180:181], v216 offset:20480\n"
    "v_or3_b32 v228, v228, v82, v83\n"
    "v_mfma_f32_16x16x32_bf16 v[12:15], v[156:159], a[172:175], v[12:15]\n"
    "ds_read_b64_tr_b16 v[182:183], v216 offset:22528\n"
    "v_mfma_f32_16x16x32_bf16 v[12:15], v[160:163], a[176:179], v[12:15]\n"
    "buffer_load_dwordx4 v226, s[88:91], s57 offen offset:2048 lds\n"
    "v_mfma_f32_16x16x32_bf16 v[12:15], v[164:167], a[180:183], v[12:15]\n"
    "ds_read_b64_tr_b16 v[184:185], v216 offset:24576\n"
    "v_mfma_f32_16x16x32_bf16 v[12:15], v[168:171], a[184:187], v[12:15]\n"
    "s_waitcnt lgkmcnt(5)\n"
    "ds_read_b64_tr_b16 v[186:187], v216 offset:26624\n"
    "v_mfma_f32_16x16x32_bf16 v[12:15], v[172:175], a[188:191], v[12:15]\n"
    "ds_read_b64_tr_b16 v[188:189], v216 offset:28672\n"
    "ds_read_b64_tr_b16 v[190:191], v216 offset:30720\n"
    "v_mfma_f32_16x16x32_bf16 a[24:27], v[192:195], v[48:51], a[24:27]\n"
    "ds_read_b128 v[144:147], v212 offset:24576\n"
    "v_mfma_f32_16x16x32_bf16 a[24:27], v[196:199], v[52:55], a[24:27]\n"
    "ds_read_b128 v[148:151], v213 offset:24576\n"
    "v_mfma_f32_16x16x32_bf16 a[24:27], v[200:203], v[56:59], a[24:27]\n"
    "buffer_load_dwordx4 v227, s[88:91], s57 offen offset:3072 lds\n"
    "v_mfma_f32_16x16x32_bf16 a[24:27], v[204:207], v[60:63], a[24:27]\n"
    "ds_read_b128 v[152:155], v214 offset:24576\n"
    "ds_read_b128 v[156:159], v215 offset:24576\n"
    "v_mfma_f32_16x16x32_bf16 a[28:31], v[192:195], v[64:67], a[28:31]\n"
    "s_add_u32 m0, s59, 131072\n"
    "v_exp_f32 v44, v12\n"
    "v_mfma_f32_16x16x32_bf16 a[28:31], v[196:199], v[68:71], a[28:31]\n"
    "s_add_u32 s62, s84, 0\n"
    "v_exp_f32 v45, v13\n"
    "v_mfma_f32_16x16x32_bf16 a[28:31], v[200:203], v[72:75], a[28:31]\n"
    "s_waitcnt vmcnt(15)\n"
    "v_exp_f32 v46, v14\n"
    "v_mfma_f32_16x16x32_bf16 a[28:31], v[204:207], v[76:79], a[28:31]\n"
    "s_barrier\n"
    "v_exp_f32 v47, v15\n"
    "v_mfma_f32_16x16x32_bf16 v[16:19], v[112:115], a[128:131], v[32:35]\n"
    "ds_read_b64_tr_b16 v[192:193], v217 offset:16384\n"
    "v_cvt_pk_bf16_f32 v98, v44, v45\n"
    "v_mfma_f32_16x16x32_bf16 v[16:19], v[116:119], a[132:135], v[16:19]\n"
    "ds_read_b64_tr_b16 v[194:195], v217 offset:18432\n"
    "v_cvt_pk_bf16_f32 v99, v46, v47\n"
    "v_mfma_f32_16x16x32_bf16 v[16:19], v[120:123], a[136:139], v[16:19]\n"
    "ds_read_b64_tr_b16 v[196:197], v217 offset:20480\n"
    "v_or3_b32 v228, v228, v98, v99\n"
    "v_mfma_f32_16x16x32_bf16 v[16:19], v[124:127], a[140:143], v[16:19]\n"
    "ds_read_b64_tr_b16 v[198:199], v217 offset:22528\n"
    "v_and_b32 v228, s60, v228\n"
    "v_mfma_f32_16x16x32_bf16 v[16:19], v[128:131], a[144:147], v[16:19]\n"
    "buffer_load_dwordx4 v252, s[72:75], s62 offen offset:0 lds\n"
    "v_mfma_f32_16x16x32_bf16 v[16:19], v[132:135], a[148:151], v[16:19]\n"
    "ds_read_b64_tr_b16 v[200:201], v217 offset:24576\n"
    "v_cmp_ne_u32 vcc, 0, v228\n"
    "v_mfma_f32_16x16x32_bf16 v[16:19], v[136:139], a[152:155], v[16:19]\n"
    "s_waitcnt lgkmcnt(5)\n"
    "ds_read_b64_tr_b16 v[202:203], v217 offset:26624\n"
    "v_mfma_f32_16x16x32_bf16 v[16:19], v[140:143], a[156:159], v[16:19]\n"
    "s_cbranch_vccnz fb_Lslow61\n"
    "fb_Lback61:\n"
    "ds_read_b64_tr_b16 v[204:205], v217 offset:28672\n"
    "ds_read_b64_tr_b16 v[206:207], v217 offset:30720\n"
    "v_mfma_f32_16x16x32_bf16 a[32:35], v[176:179], v[48:51], a[32:35]\n"
    "ds_read_b128 v[160:163], v212 offset:24832\n"
    "v_mfma_f32_16x16x32_bf16 a[32:35], v[180:183], v[52:55], a[32:35]\n"
    "ds_read_b128 v[164:167], v213 offset:24832\n"
    "v_mfma_f32_16x16x32_bf16 a[32:35], v[184:187], v[56:59], a[32:35]\n"
    "buffer_load_dwordx4 v253, s[72:75], s62 offen offset:1024 lds\n"
    "v_mfma_f32_16x16x32_bf16 a[32:35], v[188:191], v[60:63], a[32:35]\n"
    "ds_read_b128 v[168:171], v214 offset:24832\n"
    "ds_read_b128 v[172:175], v215 offset:24832\n"
    "v_mfma_f32_16x16x32_bf16 a[36:39], v[176:179], v[64:67], a[36:39]\n"
    "v_exp_f32 v40, v16\n"
    "v_mfma_f32_16x16x32_bf16 a[36:39], v[180:183], v[68:71], a[36:39]\n"
    "v_exp_f32 v41, v17\n"
    "v_mfma_f32_16x16x32_bf16 a[36:39], v[184:187], v[72:75], a[36:39]\n"
    "v_exp_f32 v42, v18\n"
    "v_mfma_f32_16x16x32_bf16 a[36:39], v[188:191], v[76:79], a[36:39]\n"
    "v_exp_f32 v43, v19\n"
    "v_mfma_f32_16x16x32_bf16 v[20:23], v[112:115], a[160:163], v[36:39]\n"
    "ds_read_b64_tr_b16 v[176:177], v218 offset:16384\n"
    "v_cvt_pk_bf16_f32 v84, v40, v41\n"
    "v_mfma_f32_16x16x32_bf16 v[20:23], v[116:119], a[164:167], v[20:23]\n"
    "ds_read_b64_tr_b16 v[178:179], v218 offset:18432\n"
    "v_cvt_pk_bf16_f32 v85, v42, v43\n"
    "v_mfma_f32_16x16x32_bf16 v[20:23], v[120:123], a[168:171], v[20:23]\n"
    "ds_read_b64_tr_b16 v[180:181], v218 offset:20480\n"
    "v_or_b32 v228, v84, v85\n"
    "v_mfma_f32_16x16x32_bf16 v[20:23], v[124:127], a[172:175], v[20:23]\n"
    "ds_read_b64_tr_b16 v[182:183], v218 offset:22528\n"
    "v_mfma_f32_16x16x32_bf16 v[20:23], v[128:131], a[176:179], v[20:23]\n"
    "buffer_load_dwordx4 v254, s[72:75], s62 offen offset:2048 lds\n"
    "v_mfma_f32_16x16x32_bf16 v[20:23], v[132:135], a[180:183], v[20:23]\n"
    "ds_read_b64_tr_b16 v[184:185], v218 offset:24576\n"
    "v_mfma_f32_16x16x32_bf16 v[20:23], v[136:139], a[184:187], v[20:23]\n"
    "s_waitcnt lgkmcnt(5)\n"
    "ds_read_b64_tr_b16 v[186:187], v218 offset:26624\n"
    "v_mfma_f32_16x16x32_bf16 v[20:23], v[140:143], a[188:191], v[20:23]\n"
    "ds_read_b64_tr_b16 v[188:189], v218 offset:28672\n"
    "ds_read_b64_tr_b16 v[190:191], v218 offset:30720\n"
    "v_mfma_f32_16x16x32_bf16 a[40:43], v[192:195], v[48:51], a[40:43]\n"
    "ds_read_b128 v[112:115], v212 offset:32768\n"
    "v_mfma_f32_16x16x32_bf16 a[40:43], v[196:199], v[52:55], a[40:43]\n"
    "ds_read_b128 v[116:119], v213 offset:32768\n"
    "v_mfma_f32_16x16x32_bf16 a[40:43], v[200:203], v[56:59], a[40:43]\n"
    "buffer_load_dwordx4 v255, s[72:75], s62 offen offset:3072 lds\n"
    "v_mfma_f32_16x16x32_bf16 a[40:43], v[204:207], v[60:63], a[40:43]\n"
    "ds_read_b128 v[120:123], v214 offset:32768\n"
    "ds_read_b128 v[124:127], v215 offset:32768\n"
    "v_mfma_f32_16x16x32_bf16 a[44:47], v[192:195], v[64:67], a[44:47]\n"
    "s_add_u32 s57, s57, 65152\n"
    "v_exp_f32 v44, v20\n"
    "v_mfma_f32_16x16x32_bf16 a[44:47], v[196:199], v[68:71], a[44:47]\n"
    "s_and_b32 s57, s57, 0xfffff\n"
    "v_exp_f32 v45, v21\n"
    "v_mfma_f32_16x16x32_bf16 a[44:47], v[200:203], v[72:75], a[44:47]\n"
    "s_add_u32 m0, s59, 65536\n"
    "v_exp_f32 v46, v22\n"
    "v_mfma_f32_16x16x32_bf16 a[44:47], v[204:207], v[76:79], a[44:47]\n"
    "v_exp_f32 v47, v23\n"
    "v_mfma_f32_16x16x32_bf16 v[24:27], v[144:147], a[128:131], v[32:35]\n"
    "ds_read_b64_tr_b16 v[192:193], v219 offset:16384\n"
    "v_cvt_pk_bf16_f32 v100, v44, v45\n"
    "v_mfma_f32_16x16x32_bf16 v[24:27], v[148:151], a[132:135], v[24:27]\n"
    "ds_read_b64_tr_b16 v[194:195], v219 offset:18432\n"
    "v_cvt_pk_bf16_f32 v101, v46, v47\n"
    "v_mfma_f32_16x16x32_bf16 v[24:27], v[152:155], a[136:139], v[24:27]\n"
    "ds_read_b64_tr_b16 v[196:197], v219 offset:20480\n"
    "v_or3_b32 v228, v228, v100, v101\n"
    "v_mfma_f32_16x16x32_bf16 v[24:27], v[156:159], a[140:143], v[24:27]\n"
    "ds_read_b64_tr_b16 v[198:199], v219 offset:22528\n"
    "v_mfma_f32_16x16x32_bf16 v[24:27], v[160:163], a[144:147], v[24:27]\n"
    "buffer_load_dwordx4 v224, s[88:91], s57 offen offset:0 lds\n"
    "v_mfma_f32_16x16x32_bf16 v[24:27], v[164:167], a[148:151], v[24:27]\n"
    "ds_read_b64_tr_b16 v[200:201], v219 offset:24576\n"
    "v_mfma_f32_16x16x32_bf16 v[24:27], v[168:171], a[152:155], v[24:27]\n"
    "s_waitcnt lgkmcnt(5)\n"
    "ds_read_b64_tr_b16 v[202:203], v219 offset:26624\n"
    "v_mfma_f32_16x16x32_bf16 v[24:27], v[172:175], a[156:159], v[24:27]\n"
    "ds_read_b64_tr_b16 v[204:205], v219 offset:28672\n"
    "ds_read_b64_tr_b16 v[206:207], v219 offset:30720\n"
    "v_mfma_f32_16x16x32_bf16 a[48:51], v[176:179], v[48:51], a[48:51]\n"
    "ds_read_b128 v[128:131], v212 offset:33024\n"
    "v_mfma_f32_16x16x32_bf16 a[48:51], v[180:183], v[52:55], a[48:51]\n"
    "ds_read_b128 v[132:135], v213 offset:33024\n"
    "v_mfma_f32_16x16x32_bf16 a[48:51], v[184:187], v[56:59], a[48:51]\n"
    "buffer_load_dwordx4 v225, s[88:91], s57 offen offset:1024 lds\n"
    "v_mfma_f32_16x16x32_bf16 a[48:51], v[188:191], v[60:63], a[48:51]\n"
    "ds_read_b128 v[136:139], v214 offset:33024\n"
    "ds_read_b128 v[140:143], v215 offset:33024\n"
    "v_mfma_f32_16x16x32_bf16 a[52:55], v[176:179], v[64:67], a[52:55]\n"
    "v_exp_f32 v40, v24\n"
    "v_mfma_f32_16x16x32_bf16 a[52:55], v[180:183], v[68:71], a[52:55]\n"
    "v_exp_f32 v41, v25\n"
    "v_mfma_f32_16x16x32_bf16 a[52:55], v[184:187], v[72:75], a[52:55]\n"
    "v_exp_f32 v42, v26\n"
    "v_mfma_f32_16x16x32_bf16 a[52:55], v[188:191], v[76:79], a[52:55]\n"
    "v_exp_f32 v43, v27\n"
    "v_mfma_f32_16x16x32_bf16 v[28:31], v[144:147], a[160:163], v[36:39]\n"
    "ds_read_b64_tr_b16 v[176:177], v216 offset:32768\n"
    "v_cvt_pk_bf16_f32 v86, v40, v41\n"
    "v_mfma_f32_16x16x32_bf16 v[28:31], v[148:151], a[164:167], v[28:31]\n"
    "ds_read_b64_tr_b16 v[178:179], v216 offset:34816\n"
    "v_cvt_pk_bf16_f32 v87, v42, v43\n"
    "v_mfma_f32_16x16x32_bf16 v[28:31], v[152:155], a[168:171], v[28:31]\n"
    "ds_read_b64_tr_b16 v[180:181], v216 offset:36864\n"
    "v_or3_b32 v228, v228, v86, v87\n"
    "v_mfma_f32_16x16x32_bf16 v[28:31], v[156:159], a[172:175], v[28:31]\n"
    "ds_read_b64_tr_b16 v[182:183], v216 offset:38912\n"
    "v_mfma_f32_16x16x32_bf16 v[28:31], v[160:163], a[176:179], v[28:31]\n"
    "buffer_load_dwordx4 v226, s[88:91], s57 offen offset:2048 lds\n"
    "v_mfma_f32_16x16x32_bf16 v[28:31], v[164:167], a[180:183], v[28:31]\n"
    "ds_read_b64_tr_b16 v[184:185], v216 offset:40960\n"
    "v_mfma_f32_16x16x32_bf16 v[28:31], v[168:171], a[184:187], v[28:31]\n"
    "s_waitcnt lgkmcnt(5)\n"
    "ds_read_b64_tr_b16 v[186:187], v216 offset:43008\n"
    "v_mfma_f32_16x16x32_bf16 v[28:31], v[172:175], a[188:191], v[28:31]\n"
    "ds_read_b64_tr_b16 v[188:189], v216 offset:45056\n"
    "ds_read_b64_tr_b16 v[190:191], v216 offset:47104\n"
    "v_mfma_f32_16x16x32_bf16 a[56:59], v[192:195], v[48:51], a[56:59]\n"
    "ds_read_b128 v[144:147], v212 offset:40960\n"
    "v_mfma_f32_16x16x32_bf16 a[56:59], v[196:199], v[52:55], a[56:59]\n"
    "ds_read_b128 v[148:151], v213 offset:40960\n"
    "v_mfma_f32_16x16x32_bf16 a[56:59], v[200:203], v[56:59], a[56:59]\n"
    "buffer_load_dwordx4 v227, s[88:91], s57 offen offset:3072 lds\n"
    "v_mfma_f32_16x16x32_bf16 a[56:59], v[204:207], v[60:63], a[56:59]\n"
    "ds_read_b128 v[152:155], v214 offset:40960\n"
    "ds_read_b128 v[156:159], v215 offset:40960\n"
    "v_mfma_f32_16x16x32_bf16 a[60:63], v[192:195], v[64:67], a[60:63]\n"
    "s_add_u32 m0, s59, 147456\n"
    "v_exp_f32 v44, v28\n"
    "v_mfma_f32_16x16x32_bf16 a[60:63], v[196:199], v[68:71], a[60:63]\n"
    "s_add_u32 s62, s84, 229376\n"
    "v_exp_f32 v45, v29\n"
    "v_mfma_f32_16x16x32_bf16 a[60:63], v[200:203], v[72:75], a[60:63]\n"
    "s_waitcnt vmcnt(8)\n"
    "v_exp_f32 v46, v30\n"
    "v_mfma_f32_16x16x32_bf16 a[60:63], v[204:207], v[76:79], a[60:63]\n"
    "s_barrier\n"
    "v_exp_f32 v47, v31\n"
    "v_mfma_f32_16x16x32_bf16 v[0:3], v[112:115], a[128:131], v[32:35]\n"
    "ds_read_b64_tr_b16 v[192:193], v217 offset:32768\n"
    "v_cvt_pk_bf16_f32 v102, v44, v45\n"
    "v_mfma_f32_16x16x32_bf16 v[0:3], v[116:119], a[132:135], v[0:3]\n"
    "ds_read_b64_tr_b16 v[194:195], v217 offset:34816\n"
    "v_cvt_pk_bf16_f32 v103, v46, v47\n"
    "v_mfma_f32_16x16x32_bf16 v[0:3], v[120:123], a[136:139], v[0:3]\n"
    "ds_read_b64_tr_b16 v[196:197], v217 offset:36864\n"
    "v_or3_b32 v228, v228, v102, v103\n"
    "v_mfma_f32_16x16x32_bf16 v[0:3], v[124:127], a[140:143], v[0:3]\n"
    "ds_read_b64_tr_b16 v[198:199], v217 offset:38912\n"
    "v_and_b32 v228, s60, v228\n"
    "v_mfma_f32_16x16x32_bf16 v[0:3], v[128:131], a[144:147], v[0:3]\n"
    "buffer_load_dwordx4 v252, s[72:75], s62 offen offset:0 lds\n"
    "v_mfma_f32_16x16x32_bf16 v[0:3], v[132:135], a[148:151], v[0:3]\n"
    "ds_read_b64_tr_b16 v[200:201], v217 offset:40960\n"
    "v_cmp_ne_u32 vcc, 0, v228\n"
    "v_mfma_f32_16x16x32_bf16 v[0:3], v[136:139], a[152:155], v[0:3]\n"
    "s_waitcnt lgkmcnt(5)\n"
    "ds_read_b64_tr_b16 v[202:203], v217 offset:43008\n"
    "v_mfma_f32_16x16x32_bf16 v[0:3], v[140:143], a[156:159], v[0:3]\n"
    "s_cbranch_vccnz fb_Lslow62\n"
    "fb_Lback62:\n"
    "ds_read_b64_tr_b16 v[204:205], v217 offset:45056\n"
    "ds_read_b64_tr_b16 v[206:207], v217 offset:47104\n"
    "v_mfma_f32_16x16x32_bf16 a[64:67], v[176:179], v[48:51], a[64:67]\n"
    "ds_read_b128 v[160:163], v212 offset:41216\n"
    "v_mfma_f32_16x16x32_bf16 a[64:67], v[180:183], v[52:55], a[64:67]\n"
    "ds_read_b128 v[164:167], v213 offset:41216\n"
    "v_mfma_f32_16x16x32_bf16 a[64:67], v[184:187], v[56:59], a[64:67]\n"
    "buffer_load_dwordx4 v253, s[72:75], s62 offen offset:1024 lds\n"
    "v_mfma_f32_16x16x32_bf16 a[64:67], v[188:191], v[60:63], a[64:67]\n"
    "ds_read_b128 v[168:171], v214 offset:41216\n"
    "ds_read_b128 v[172:175], v215 offset:41216\n"
    "v_mfma_f32_16x16x32_bf16 a[68:71], v[176:179], v[64:67], a[68:71]\n"
    "v_exp_f32 v40, v0\n"
    "v_mfma_f32_16x16x32_bf16 a[68:71], v[180:183], v[68:71], a[68:71]\n"
    "v_exp_f32 v41, v1\n"
    "v_mfma_f32_16x16x32_bf16 a[68:71], v[184:187], v[72:75], a[68:71]\n"
    "v_exp_f32 v42, v2\n"
    "v_mfma_f32_16x16x32_bf16 a[68:71], v[188:191], v[76:79], a[68:71]\n"
    "v_exp_f32 v43, v3\n"
    "v_mfma_f32_16x16x32_bf16 v[4:7], v[112:115], a[160:163], v[36:39]\n"
    "ds_read_b64_tr_b16 v[176:177], v218 offset:32768\n"
    "v_cvt_pk_bf16_f32 v88, v40, v41\n"
    "v_mfma_f32_16x16x32_bf16 v[4:7], v[116:119], a[164:167], v[4:7]\n"
    "ds_read_b64_tr_b16 v[178:179], v218 offset:34816\n"
    "v_cvt_pk_bf16_f32 v89, v42, v43\n"
    "v_mfma_f32_16x16x32_bf16 v[4:7], v[120:123], a[168:171], v[4:7]\n"
    "ds_read_b64_tr_b16 v[180:181], v218 offset:36864\n"
    "v_or_b32 v228, v88, v89\n"
    "v_mfma_f32_16x16x32_bf16 v[4:7], v[124:127], a[172:175], v[4:7]\n"
    "ds_read_b64_tr_b16 v[182:183], v218 offset:38912\n"
    "v_mfma_f32_16x16x32_bf16 v[4:7], v[128:131], a[176:179], v[4:7]\n"
    "buffer_load_dwordx4 v254, s[72:75], s62 offen offset:2048 lds\n"
    "v_mfma_f32_16x16x32_bf16 v[4:7], v[132:135], a[180:183], v[4:7]\n"
    "ds_read_b64_tr_b16 v[184:185], v218 offset:40960\n"
    "v_mfma_f32_16x16x32_bf16 v[4:7], v[136:139], a[184:187], v[4:7]\n"
    "s_waitcnt lgkmcnt(5)\n"
    "ds_read_b64_tr_b16 v[186:187], v218 offset:43008\n"
    "v_mfma_f32_16x16x32_bf16 v[4:7], v[140:143], a[188:191], v[4:7]\n"
    "ds_read_b64_tr_b16 v[188:189], v218 offset:45056\n"
    "v_mfma_f32_16x16x32_bf16 a[72:75], v[192:195], v[48:51], a[72:75]\n"
    "ds_read_b64_tr_b16 v[190:191], v218 offset:47104\n"
    "v_mfma_f32_16x16x32_bf16 a[72:75], v[196:199], v[52:55], a[72:75]\n"
    "ds_read_b128 v[112:115], v212 offset:49152\n"
    "v_mfma_f32_16x16x32_bf16 a[72:75], v[200:203], v[56:59], a[72:75]\n"
    "buffer_load_dwordx4 v255, s[72:75], s62 offen offset:3072 lds\n"
    "v_mfma_f32_16x16x32_bf16 a[72:75], v[204:207], v[60:63], a[72:75]\n"
    "ds_read_b128 v[116:119], v213 offset:49152\n"
    "v_mfma_f32_16x16x32_bf16 a[76:79], v[192:195], v[64:67], a[76:79]\n"
    "s_add_u32 s57, s57, 128\n"
    "v_exp_f32 v44, v4\n"
    "v_mfma_f32_16x16x32_bf16 a[76:79], v[196:199], v[68:71], a[76:79]\n"
    "v_exp_f32 v45, v5\n"
    "v_mfma_f32_16x16x32_bf16 a[76:79], v[200:203], v[72:75], a[76:79]\n"
    "s_add_u32 m0, s59, 81920\n"
    "v_exp_f32 v46, v6\n"
    "v_mfma_f32_16x16x32_bf16 a[76:79], v[204:207], v[76:79], a[76:79]\n"
    "v_exp_f32 v47, v7\n"
    "v_mfma_f32_16x16x32_bf16 a[196:199], v[208:211], v[64:67], a[196:199]\n"
    "ds_read_b128 v[120:123], v214 offset:49152\n"
    "v_mfma_f32_16x16x32_bf16 a[196:199], v[208:211], v[68:71], a[196:199]\n"
    "ds_read_b128 v[124:127], v215 offset:49152\n"
    "v_mfma_f32_16x16x32_bf16 a[196:199], v[208:211], v[72:75], a[196:199]\n"
    "ds_read_b64_tr_b16 v[192:193], v219 offset:32768\n"
    "v_mfma_f32_16x16x32_bf16 a[196:199], v[208:211], v[76:79], a[196:199]\n"
    "ds_read_b64_tr_b16 v[194:195], v219 offset:34816\n"
    "v_mfma_f32_16x16x32_bf16 v[8:11], v[144:147], a[128:131], v[32:35]\n"
    "ds_read_b64_tr_b16 v[196:197], v219 offset:36864\n"
    "v_cvt_pk_bf16_f32 v104, v44, v45\n"
    "v_mfma_f32_16x16x32_bf16 v[8:11], v[148:151], a[132:135], v[8:11]\n"
    "ds_read_b64_tr_b16 v[198:199], v219 offset:38912\n"
    "v_cvt_pk_bf16_f32 v105, v46, v47\n"
    "v_mfma_f32_16x16x32_bf16 v[8:11], v[152:155], a[136:139], v[8:11]\n"
    "ds_read_b64_tr_b16 v[200:201], v219 offset:40960\n"
    "v_or3_b32 v228, v228, v104, v105\n"
    "v_mfma_f32_16x16x32_bf16 v[8:11], v[156:159], a[140:143], v[8:11]\n"
    "ds_read_b64_tr_b16 v[202:203], v219 offset:43008\n"
    "v_mfma_f32_16x16x32_bf16 v[8:11], v[160:163], a[144:147], v[8:11]\n"
    "buffer_load_dwordx4 v224, s[88:91], s57 offen offset:0 lds\n"
    "v_mfma_f32_16x16x32_bf16 v[8:11], v[164:167], a[148:151], v[8:11]\n"
    "ds_read_b64_tr_b16 v[204:205], v219 offset:45056\n"
    "v_mfma_f32_16x16x32_bf16 v[8:11], v[168:171], a[152:155], v[8:11]\n"
    "s_waitcnt lgkmcnt(7)\n"
    "ds_read_b64_tr_b16 v[206:207], v219 offset:47104\n"
    "v_mfma_f32_16x16x32_bf16 v[8:11], v[172:175], a[156:159], v[8:11]\n"
    "ds_read_b128 v[128:131], v212 offset:49408\n"
    "v_mfma_f32_16x16x32_bf16 a[80:83], v[176:179], v[48:51], a[80:83]\n"
    "ds_read_b128 v[132:135], v213 offset:49408\n"
    "v_mfma_f32_16x16x32_bf16 a[80:83], v[180:183], v[52:55], a[80:83]\n"
    "ds_read_b128 v[136:139], v214 offset:49408\n"
    "v_mfma_f32_16x16x32_bf16 a[80:83], v[184:187], v[56:59], a[80:83]\n"
    "buffer_load_dwordx4 v225, s[88:91], s57 offen offset:1024 lds\n"
    "v_mfma_f32_16x16x32_bf16 a[80:83], v[188:191], v[60:63], a[80:83]\n"
    "ds_read_b128 v[140:143], v215 offset:49408\n"
    "v_mfma_f32_16x16x32_bf16 a[84:87], v[176:179], v[64:67], a[84:87]\n"
    "v_exp_f32 v40, v8\n"
    "v_mfma_f32_16x16x32_bf16 a[84:87], v[180:183], v[68:71], a[84:87]\n"
    "v_exp_f32 v41, v9\n"
    "v_mfma_f32_16x16x32_bf16 a[84:87], v[184:187], v[72:75], a[84:87]\n"
    "v_exp_f32 v42, v10\n"
    "v_mfma_f32_16x16x32_bf16 a[84:87], v[188:191], v[76:79], a[84:87]\n"
    "v_exp_f32 v43, v11\n"
    "v_mfma_f32_16x16x32_bf16 v[12:15], v[144:147], a[160:163], v[36:39]\n"
    "ds_read_b64_tr_b16 v[176:177], v216 offset:49152\n"
    "v_cvt_pk_bf16_f32 v90, v40, v41\n"
    "v_mfma_f32_16x16x32_bf16 v[12:15], v[148:151], a[164:167], v[12:15]\n"
    "ds_read_b64_tr_b16 v[178:179], v216 offset:51200\n"
    "v_cvt_pk_bf16_f32 v91, v42, v43\n"
    "v_mfma_f32_16x16x32_bf16 v[12:15], v[152:155], a[168:171], v[12:15]\n"
    "ds_read_b64_tr_b16 v[180:181], v216 offset:53248\n"
    "v_or3_b32 v228, v228, v90, v91\n"
    "v_mfma_f32_16x16x32_bf16 v[12:15], v[156:159], a[172:175], v[12:15]\n"
    "ds_read_b64_tr_b16 v[182:183], v216 offset:55296\n"
    "v_mfma_f32_16x16x32_bf16 v[12:15], v[160:163], a[176:179], v[12:15]\n"
    "buffer_load_dwordx4 v226, s[88:91], s57 offen offset:2048 lds\n"
    "v_mfma_f32_16x16x32_bf16 v[12:15], v[164:167], a[180:183], v[12:15]\n"
    "ds_read_b64_tr_b16 v[184:185], v216 offset:57344\n"
    "v_mfma_f32_16x16x32_bf16 v[12:15], v[168:171], a[184:187], v[12:15]\n"
    "s_waitcnt lgkmcnt(5)\n"
    "ds_read_b64_tr_b16 v[186:187], v216 offset:59392\n"
    "v_mfma_f32_16x16x32_bf16 v[12:15], v[172:175], a[188:191], v[12:15]\n"
    "ds_read_b64_tr_b16 v[188:189], v216 offset:61440\n"
    "ds_read_b64_tr_b16 v[190:191], v216 offset:63488\n"
    "v_mfma_f32_16x16x32_bf16 a[88:91], v[192:195], v[48:51], a[88:91]\n"
    "ds_read_b128 v[144:147], v212 offset:57344\n"
    "v_mfma_f32_16x16x32_bf16 a[88:91], v[196:199], v[52:55], a[88:91]\n"
    "ds_read_b128 v[148:151], v213 offset:57344\n"
    "v_mfma_f32_16x16x32_bf16 a[88:91], v[200:203], v[56:59], a[88:91]\n"
    "buffer_load_dwordx4 v227, s[88:91], s57 offen offset:3072 lds\n"
    "v_mfma_f32_16x16x32_bf16 a[88:91], v[204:207], v[60:63], a[88:91]\n"
    "ds_read_b128 v[152:155], v214 offset:57344\n"
    "ds_read_b128 v[156:159], v215 offset:57344\n"
    "v_mfma_f32_16x16x32_bf16 a[92:95], v[192:195], v[64:67], a[92:95]\n"
    "s_add_u32 m0, s59, 0\n"
    "v_exp_f32 v44, v12\n"
    "v_mfma_f32_16x16x32_bf16 a[92:95], v[196:199], v[68:71], a[92:95]\n"
    "s_add_u32 s62, s84, 128\n"
    "v_exp_f32 v45, v13\n"
    "v_mfma_f32_16x16x32_bf16 a[92:95], v[200:203], v[72:75], a[92:95]\n"
    "s_waitcnt vmcnt(8)\n"
    "v_exp_f32 v46, v14\n"
    "v_mfma_f32_16x16x32_bf16 a[92:95], v[204:207], v[76:79], a[92:95]\n"
    "s_barrier\n"
    "v_exp_f32 v47, v15\n"
    "v_mfma_f32_16x16x32_bf16 v[16:19], v[112:115], a[128:131], v[32:35]\n"
    "ds_read_b64_tr_b16 v[192:193], v217 offset:49152\n"
    "v_cvt_pk_bf16_f32 v106, v44, v45\n"
    "v_mfma_f32_16x16x32_bf16 v[16:19], v[116:119], a[132:135], v[16:19]\n"
    "ds_read_b64_tr_b16 v[194:195], v217 offset:51200\n"
    "v_cvt_pk_bf16_f32 v107, v46, v47\n"
    "v_mfma_f32_16x16x32_bf16 v[16:19], v[120:123], a[136:139], v[16:19]\n"
    "ds_read_b64_tr_b16 v[196:197], v217 offset:53248\n"
    "v_or3_b32 v228, v228, v106, v107\n"
    "v_mfma_f32_16x16x32_bf16 v[16:19], v[124:127], a[140:143], v[16:19]\n"
    "ds_read_b64_tr_b16 v[198:199], v217 offset:55296\n"
    "v_and_b32 v228, s60, v228\n"
    "v_mfma_f32_16x16x32_bf16 v[16:19], v[128:131], a[144:147], v[16:19]\n"
    "buffer_load_dwordx4 v252, s[72:75], s62 offen offset:0 lds\n"
    "v_mfma_f32_16x16x32_bf16 v[16:19], v[132:135], a[148:151], v[16:19]\n"
    "ds_read_b64_tr_b16 v[200:201], v217 offset:57344\n"
    "v_cmp_ne_u32 vcc, 0, v228\n"
    "v_mfma_f32_16x16x32_bf16 v[16:19], v[136:139], a[152:155], v[16:19]\n"
    "s_waitcnt lgkmcnt(5)\n"
    "ds_read_b64_tr_b16 v[202:203], v217 offset:59392\n"
    "v_mfma_f32_16x16x32_bf16 v[16:19], v[140:143], a[156:159], v[16:19]\n"
    "s_cbranch_vccnz fb_Lslow63\n"
    "fb_Lback63:\n"
    "ds_read_b64_tr_b16 v[204:205], v217 offset:61440\n"
    "ds_read_b64_tr_b16 v[206:207], v217 offset:63488\n"
    "v_mfma_f32_16x16x32_bf16 a[96:99], v[176:179], v[48:51], a[96:99]\n"
    "ds_read_b128 v[160:163], v212 offset:57600\n"
    "v_mfma_f32_16x16x32_bf16 a[96:99], v[180:183], v[52:55], a[96:99]\n"
    "ds_read_b128 v[164:167], v213 offset:57600\n"
    "v_mfma_f32_16x16x32_bf16 a[96:99], v[184:187], v[56:59], a[96:99]\n"
    "buffer_load_dwordx4 v253, s[72:75], s62 offen offset:1024 lds\n"
    "v_mfma_f32_16x16x32_bf16 a[96:99], v[188:191], v[60:63], a[96:99]\n"
    "ds_read_b128 v[168:171], v214 offset:57600\n"
    "ds_read_b128 v[172:175], v215 offset:57600\n"
    "v_mfma_f32_16x16x32_bf16 a[100:103], v[176:179], v[64:67], a[100:103]\n"
    "v_exp_f32 v40, v16\n"
    "v_mfma_f32_16x16x32_bf16 a[100:103], v[180:183], v[68:71], a[100:103]\n"
    "v_exp_f32 v41, v17\n"
    "v_mfma_f32_16x16x32_bf16 a[100:103], v[184:187], v[72:75], a[100:103]\n"
    "v_exp_f32 v42, v18\n"
    "v_mfma_f32_16x16x32_bf16 a[100:103], v[188:191], v[76:79], a[100:103]\n"
    "v_exp_f32 v43, v19\n"
    "v_mfma_f32_16x16x32_bf16 v[20:23], v[112:115], a[160:163], v[36:39]\n"
    "ds_read_b64_tr_b16 v[176:177], v218 offset:49152\n"
    "v_cvt_pk_bf16_f32 v92, v40, v41\n"
    "v_mfma_f32_16x16x32_bf16 v[20:23], v[116:119], a[164:167], v[20:23]\n"
    "ds_read_b64_tr_b16 v[178:179], v218 offset:51200\n"
    "v_cvt_pk_bf16_f32 v93, v42, v43\n"
    "v_mfma_f32_16x16x32_bf16 v[20:23], v[120:123], a[168:171], v[20:23]\n"
    "ds_read_b64_tr_b16 v[180:181], v218 offset:53248\n"
    "v_or_b32 v228, v92, v93\n"
    "v_mfma_f32_16x16x32_bf16 v[20:23], v[124:127], a[172:175], v[20:23]\n"
    "ds_read_b64_tr_b16 v[182:183], v218 offset:55296\n"
    "v_mfma_f32_16x16x32_bf16 v[20:23], v[128:131], a[176:179], v[20:23]\n"
    "buffer_load_dwordx4 v254, s[72:75], s62 offen offset:2048 lds\n"
    "v_mfma_f32_16x16x32_bf16 v[20:23], v[132:135], a[180:183], v[20:23]\n"
    "ds_read_b64_tr_b16 v[184:185], v218 offset:57344\n"
    "v_mfma_f32_16x16x32_bf16 v[20:23], v[136:139], a[184:187], v[20:23]\n"
    "s_waitcnt lgkmcnt(5)\n"
    "ds_read_b64_tr_b16 v[186:187], v218 offset:59392\n"
    "v_mfma_f32_16x16x32_bf16 v[20:23], v[140:143], a[188:191], v[20:23]\n"
    "ds_read_b64_tr_b16 v[188:189], v218 offset:61440\n"
    "ds_read_b64_tr_b16 v[190:191], v218 offset:63488\n"
    "v_mfma_f32_16x16x32_bf16 a[104:107], v[192:195], v[48:51], a[104:107]\n"
    "v_mfma_f32_16x16x32_bf16 a[104:107], v[196:199], v[52:55], a[104:107]\n"
    "v_mfma_f32_16x16x32_bf16 a[104:107], v[200:203], v[56:59], a[104:107]\n"
    "buffer_load_dwordx4 v255, s[72:75], s62 offen offset:3072 lds\n"
    "v_mfma_f32_16x16x32_bf16 a[104:107], v[204:207], v[60:63], a[104:107]\n"
    "v_mfma_f32_16x16x32_bf16 a[108:111], v[192:195], v[64:67], a[108:111]\n"
    "s_add_u32 s57, s57, 128\n"
    "v_exp_f32 v44, v20\n"
    "v_mfma_f32_16x16x32_bf16 a[108:111], v[196:199], v[68:71], a[108:111]\n"
    "v_exp_f32 v45, v21\n"
    "v_mfma_f32_16x16x32_bf16 a[108:111], v[200:203], v[72:75], a[108:111]\n"
    "s_add_u32 m0, s59, 98304\n"
    "v_exp_f32 v46, v22\n"
    "v_mfma_f32_16x16x32_bf16 a[108:111], v[204:207], v[76:79], a[108:111]\n"
    "v_exp_f32 v47, v23\n"
    "v_mfma_f32_16x16x32_bf16 v[24:27], v[144:147], a[128:131], v[32:35]\n"
    "ds_read_b64_tr_b16 v[192:193], v219 offset:49152\n"
    "v_cvt_pk_bf16_f32 v108, v44, v45\n"
    "v_mfma_f32_16x16x32_bf16 v[24:27], v[148:151], a[132:135], v[24:27]\n"
    "ds_read_b64_tr_b16 v[194:195], v219 offset:51200\n"
    "v_cvt_pk_bf16_f32 v109, v46, v47\n"
    "v_mfma_f32_16x16x32_bf16 v[24:27], v[152:155], a[136:139], v[24:27]\n"
    "ds_read_b64_tr_b16 v[196:197], v219 offset:53248\n"
    "v_or3_b32 v228, v228, v108, v109\n"
    "v_mfma_f32_16x16x32_bf16 v[24:27], v[156:159], a[140:143], v[24:27]\n"
    "ds_read_b64_tr_b16 v[198:199], v219 offset:55296\n"
    "v_mfma_f32_16x16x32_bf16 v[24:27], v[160:163], a[144:147], v[24:27]\n"
    "buffer_load_dwordx4 v224, s[88:91], s57 offen offset:0 lds\n"
    "v_mfma_f32_16x16x32_bf16 v[24:27], v[164:167], a[148:151], v[24:27]\n"
    "ds_read_b64_tr_b16 v[200:201], v219 offset:57344\n"
    "v_mfma_f32_16x16x32_bf16 v[24:27], v[168:171], a[152:155], v[24:27]\n"
    "s_waitcnt lgkmcnt(5)\n"
    "ds_read_b64_tr_b16 v[202:203], v219 offset:59392\n"
    "v_mfma_f32_16x16x32_bf16 v[24:27], v[172:175], a[156:159], v[24:27]\n"
    "ds_read_b64_tr_b16 v[204:205], v219 offset:61440\n"
    "ds_read_b64_tr_b16 v[206:207], v219 offset:63488\n"
    "v_mfma_f32_16x16x32_bf16 a[112:115], v[176:179], v[48:51], a[112:115]\n"
    "v_mfma_f32_16x16x32_bf16 a[112:115], v[180:183], v[52:55], a[112:115]\n"
    "v_mfma_f32_16x16x32_bf16 a[112:115], v[184:187], v[56:59], a[112:115]\n"
    "buffer_load_dwordx4 v225, s[88:91], s57 offen offset:1024 lds\n"
    "v_mfma_f32_16x16x32_bf16 a[112:115], v[188:191], v[60:63], a[112:115]\n"
    "v_mfma_f32_16x16x32_bf16 a[116:119], v[176:179], v[64:67], a[116:119]\n"
    "v_exp_f32 v40, v24\n"
    "v_mfma_f32_16x16x32_bf16 a[116:119], v[180:183], v[68:71], a[116:119]\n"
    "v_exp_f32 v41, v25\n"
    "v_mfma_f32_16x16x32_bf16 a[116:119], v[184:187], v[72:75], a[116:119]\n"
    "v_exp_f32 v42, v26\n"
    "v_mfma_f32_16x16x32_bf16 a[116:119], v[188:191], v[76:79], a[116:119]\n"
    "v_exp_f32 v43, v27\n"
    "v_mfma_f32_16x16x32_bf16 v[28:31], v[144:147], a[160:163], v[36:39]\n"
    "ds_read_b64_tr_b16 v[176:177], v216 offset:0\n"
    "v_cvt_pk_bf16_f32 v94, v40, v41\n"
    "v_mfma_f32_16x16x32_bf16 v[28:31], v[148:151], a[164:167], v[28:31]\n"
    "ds_read_b64_tr_b16 v[178:179], v216 offset:2048\n"
    "v_cvt_pk_bf16_f32 v95, v42, v43\n"
    "v_mfma_f32_16x16x32_bf16 v[28:31], v[152:155], a[168:171], v[28:31]\n"
    "ds_read_b64_tr_b16 v[180:181], v216 offset:4096\n"
    "v_or3_b32 v228, v228, v94, v95\n"
    "v_mfma_f32_16x16x32_bf16 v[28:31], v[156:159], a[172:175], v[28:31]\n"
    "ds_read_b64_tr_b16 v[182:183], v216 offset:6144\n"
    "v_mfma_f32_16x16x32_bf16 v[28:31], v[160:163], a[176:179], v[28:31]\n"
    "buffer_load_dwordx4 v226, s[88:91], s57 offen offset:2048 lds\n"
    "v_mfma_f32_16x16x32_bf16 v[28:31], v[164:167], a[180:183], v[28:31]\n"
    "ds_read_b64_tr_b16 v[184:185], v216 offset:8192\n"
    "v_mfma_f32_16x16x32_bf16 v[28:31], v[168:171], a[184:187], v[28:31]\n"
    "s_waitcnt lgkmcnt(5)\n"
    "ds_read_b64_tr_b16 v[186:187], v216 offset:10240\n"
    "v_mfma_f32_16x16x32_bf16 v[28:31], v[172:175], a[188:191], v[28:31]\n"
    "ds_read_b64_tr_b16 v[188:189], v216 offset:12288\n"
    "ds_read_b64_tr_b16 v[190:191], v216 offset:14336\n"
    "v_mfma_f32_16x16x32_bf16 a[120:123], v[192:195], v[48:51], a[120:123]\n"
    "v_mfma_f32_16x16x32_bf16 a[120:123], v[196:199], v[52:55], a[120:123]\n"
    "v_mfma_f32_16x16x32_bf16 a[120:123], v[200:203], v[56:59], a[120:123]\n"
    "buffer_load_dwordx4 v227, s[88:91], s57 offen offset:3072 lds\n"
    "v_mfma_f32_16x16x32_bf16 a[120:123], v[204:207], v[60:63], a[120:123]\n"
    "v_mfma_f32_16x16x32_bf16 a[124:127], v[192:195], v[64:67], a[124:127]\n"
    "v_exp_f32 v44, v28\n"
    "v_mfma_f32_16x16x32_bf16 a[124:127], v[196:199], v[68:71], a[124:127]\n"
    "v_exp_f32 v45, v29\n"
    "v_mfma_f32_16x16x32_bf16 a[124:127], v[200:203], v[72:75], a[124:127]\n"
    "s_waitcnt vmcnt(8)\n"
    "v_exp_f32 v46, v30\n"
    "v_mfma_f32_16x16x32_bf16 a[124:127], v[204:207], v[76:79], a[124:127]\n"
    "s_barrier\n"
    "v_exp_f32 v47, v31\n"
    "s_nop 0\n"
    "v_cvt_pk_bf16_f32 v110, v44, v45\n"
    "v_cvt_pk_bf16_f32 v111, v46, v47\n"
    "v_or3_b32 v228, v228, v110, v111\n"
    "v_and_b32 v228, s60, v228\n"
    "v_cmp_ne_u32 vcc, 0, v228\n"
    "s_cbranch_vccnz fb_Lslow64\n"
    "fb_Lback64:\n"
    "s_nop 0\n"
    "v_mfma_f32_16x16x32_bf16 a[192:195], v[208:211], v[80:83], a[192:195]\n"
    "s_add_u32 s57, s57, 128\n"
    "ds_read_b64_tr_b16 v[192:193], v217 offset:0\n"
    "v_lshlrev_b32 v237, 4, v250\n"
    "v_mfma_f32_16x16x32_bf16 a[192:195], v[208:211], v[84:87], a[192:195]\n"
    "s_add_u32 m0, s59, 114688\n"
    "ds_read_b64_tr_b16 v[194:195], v217 offset:2048\n"
    "v_add_u32 v237, 65536, v237\n"
    "v_mfma_f32_16x16x32_bf16 a[192:195], v[208:211], v[88:91], a[192:195]\n"
    "ds_read_b64_tr_b16 v[196:197], v217 offset:4096\n"
    "v_add_u32 v233, s59, v237\n"
    "v_mfma_f32_16x16x32_bf16 a[192:195], v[208:211], v[92:95], a[192:195]\n"
    "buffer_load_dwordx4 v224, s[88:91], s57 offen offset:0 lds\n"
    "buffer_load_dwordx4 v225, s[88:91], s57 offen offset:1024 lds\n"
    "ds_read_b64_tr_b16 v[198:199], v217 offset:6144\n"
    "v_mfma_f32_16x16x32_bf16 a[196:199], v[208:211], v[96:99], a[196:199]\n"
    "s_lshl_b32 s61, %[wv], 5\n"
    "ds_read_b64_tr_b16 v[200:201], v217 offset:8192\n"
    "v_xor_b32 v235, s61, v216\n"
    "v_mfma_f32_16x16x32_bf16 a[196:199], v[208:211], v[100:103], a[196:199]\n"
    "ds_read_b64_tr_b16 v[202:203], v217 offset:10240\n"
    "v_subrev_u32 v234, 65536, v235\n"
    "v_mfma_f32_16x16x32_bf16 a[196:199], v[208:211], v[104:107], a[196:199]\n"
    "ds_read_b64_tr_b16 v[204:205], v217 offset:12288\n"
    "v_add_u32 v236, 65536, v235\n"
    "v_mfma_f32_16x16x32_bf16 a[196:199], v[208:211], v[108:111], a[196:199]\n"
    "s_waitcnt lgkmcnt(7)\n"
    "ds_read_b64_tr_b16 v[206:207], v217 offset:14336\n"
    "buffer_load_dwordx4 v226, s[88:91], s57 offen offset:2048 lds\n"
    "buffer_load_dwordx4 v227, s[88:91], s57 offen offset:3072 lds\n"
    "v_bfe_u32 v242, v250, 1, 3\n"
    "v_mul_u32_u24 v242, 2176, v242\n"
    "v_and_b32 v243, 1, v250\n"
    "v_mfma_f32_16x16x32_bf16 a[0:3], v[176:179], v[80:83], a[0:3]\n"
    "s_add_u32 m0, s59, 16384\n"
    "ds_read_b64_tr_b16 v[0:1], v218 offset:0\n"
    "v_lshl_add_u32 v242, v243, 6, v242\n"
    "v_mfma_f32_16x16x32_bf16 a[0:3], v[180:183], v[84:87], a[0:3]\n"
    "s_add_u32 s62, s84, 229504\n"
    "ds_read_b64_tr_b16 v[2:3], v218 offset:2048\n"
    "v_lshrrev_b32 v243, 4, v250\n"
    "v_mfma_f32_16x16x32_bf16 a[0:3], v[184:187], v[88:91], a[0:3]\n"
    "s_lshl_b32 s61, %[wv], 7\n"
    "ds_read_b64_tr_b16 v[4:5], v218 offset:4096\n"
    "v_lshl_add_u32 v240, v243, 4, v242\n"
    "v_mfma_f32_16x16x32_bf16 a[0:3], v[188:191], v[92:95], a[0:3]\n"
    "buffer_load_dwordx4 v252, s[72:75], s62 offen offset:0 lds\n"
    "buffer_load_dwordx4 v253, s[72:75], s62 offen offset:1024 lds\n"
    "ds_read_b64_tr_b16 v[6:7], v218 offset:6144\n"
    "v_add_u32 v240, s61, v240\n"
    "v_mfma_f32_16x16x32_bf16 a[4:7], v[176:179], v[96:99], a[4:7]\n"
    "s_mul_i32 s85, s67, 69632\n"
    "s_add_u32 s96, s85, 0\n"
    "ds_read_b64_tr_b16 v[8:9], v218 offset:8192\n"
    "v_mfma_f32_16x16x32_bf16 a[4:7], v[180:183], v[100:103], a[4:7]\n"
    "s_add_u32 s97, s85, 17408\n"
    "s_add_u32 s98, s85, 34816\n"
    "ds_read_b64_tr_b16 v[10:11], v218 offset:10240\n"
    "v_mfma_f32_16x16x32_bf16 a[4:7], v[184:187], v[104:107], a[4:7]\n"
    "s_add_u32 s99, s85, 52224\n"
    "ds_read_b64_tr_b16 v[12:13], v218 offset:12288\n"
    "v_accvgpr_read_b32 v48, a200\n"
    "v_mfma_f32_16x16x32_bf16 a[4:7], v[188:191], v[108:111], a[4:7]\n"
    "s_waitcnt lgkmcnt(7)\n"
    "ds_read_b64_tr_b16 v[14:15], v218 offset:14336\n"
    "buffer_load_dwordx4 v254, s[72:75], s62 offen offset:2048 lds\n"
    "v_accvgpr_read_b32 v49, a201\n"
    "v_accvgpr_read_b32 v50, a202\n"
    "v_accvgpr_read_b32 v51, a203\n"
    "v_accvgpr_read_b32 v52, a204\n"
    "v_accvgpr_read_b32 v53, a205\n"
    "v_mfma_f32_16x16x32_bf16 a[8:11], v[192:195], v[80:83], a[8:11]\n"
    "ds_read_b64_tr_b16 v[16:17], v219 offset:0\n"
    "v_accvgpr_read_b32 v54, a206\n"
    "v_mfma_f32_16x16x32_bf16 a[8:11], v[196:199], v[84:87], a[8:11]\n"
    "ds_read_b64_tr_b16 v[18:19], v219 offset:2048\n"
    "v_accvgpr_read_b32 v55, a207\n"
    "v_mfma_f32_16x16x32_bf16 a[8:11], v[200:203], v[88:91], a[8:11]\n"
    "ds_read_b64_tr_b16 v[20:21], v219 offset:4096\n"
    "v_accvgpr_read_b32 v56, a208\n"
    "v_mfma_f32_16x16x32_bf16 a[8:11], v[204:207], v[92:95], a[8:11]\n"
    "buffer_load_dwordx4 v255, s[72:75], s62 offen offset:3072 lds\n"
    "ds_read_b64_tr_b16 v[22:23], v219 offset:6144\n"
    "v_accvgpr_read_b32 v57, a209\n"
    "v_accvgpr_read_b32 v58, a210\n"
    "v_accvgpr_read_b32 v59, a211\n"
    "v_accvgpr_read_b32 v60, a212\n"
    "v_mfma_f32_16x16x32_bf16 a[12:15], v[192:195], v[96:99], a[12:15]\n"
    "s_add_u32 m0, s59, 32768\n"
    "ds_read_b64_tr_b16 v[24:25], v219 offset:8192\n"
    "v_accvgpr_read_b32 v61, a213\n"
    "v_mfma_f32_16x16x32_bf16 a[12:15], v[196:199], v[100:103], a[12:15]\n"
    "s_add_u32 s62, s84, 114688\n"
    "ds_read_b64_tr_b16 v[26:27], v219 offset:10240\n"
    "v_accvgpr_read_b32 v62, a214\n"
    "v_mfma_f32_16x16x32_bf16 a[12:15], v[200:203], v[104:107], a[12:15]\n"
    "ds_read_b64_tr_b16 v[28:29], v219 offset:12288\n"
    "v_accvgpr_read_b32 v63, a215\n"
    "v_mfma_f32_16x16x32_bf16 a[12:15], v[204:207], v[108:111], a[12:15]\n"
    "s_waitcnt lgkmcnt(7)\n"
    "ds_read_b64_tr_b16 v[30:31], v219 offset:14336\n"
    "buffer_load_dwordx4 v252, s[72:75], s62 offen offset:0 lds\n"
    "v_accvgpr_read_b32 v64, a216\n"
    "v_accvgpr_read_b32 v65, a217\n"
    "v_accvgpr_read_b32 v66, a218\n"
    "v_accvgpr_read_b32 v67, a219\n"
    "v_mfma_f32_16x16x32_bf16 a[16:19], v[0:3], v[80:83], a[16:19]\n"
    "ds_read_b64_tr_b16 v[176:177], v216 offset:16384\n"
    "v_accvgpr_read_b32 v68, a220\n"
    "v_mfma_f32_16x16x32_bf16 a[16:19], v[4:7], v[84:87], a[16:19]\n"
    "ds_read_b64_tr_b16 v[178:179], v216 offset:18432\n"
    "v_accvgpr_read_b32 v69, a221\n"
    "v_mfma_f32_16x16x32_bf16 a[16:19], v[8:11], v[88:91], a[16:19]\n"
    "ds_read_b64_tr_b16 v[180:181], v216 offset:20480\n"
    "v_accvgpr_read_b32 v70, a222\n"
    "v_mfma_f32_16x16x32_bf16 a[16:19], v[12:15], v[92:95], a[16:19]\n"
    "buffer_load_dwordx4 v253, s[72:75], s62 offen offset:1024 lds\n"
    "ds_read_b64_tr_b16 v[182:183], v216 offset:22528\n"
    "v_accvgpr_read_b32 v71, a223\n"
    "v_accvgpr_read_b32 v72, a224\n"
    "v_accvgpr_read_b32 v73, a225\n"
    "v_accvgpr_read_b32 v74, a226\n"
    "v_mfma_f32_16x16x32_bf16 a[20:23], v[0:3], v[96:99], a[20:23]\n"
    "ds_read_b64_tr_b16 v[184:185], v216 offset:24576\n"
    "v_accvgpr_read_b32 v75, a227\n"
    "v_mfma_f32_16x16x32_bf16 a[20:23], v[4:7], v[100:103], a[20:23]\n"
    "ds_read_b64_tr_b16 v[186:187], v216 offset:26624\n"
    "v_accvgpr_read_b32 v212, a228\n"
    "v_mfma_f32_16x16x32_bf16 a[20:23], v[8:11], v[104:107], a[20:23]\n"
    "ds_read_b64_tr_b16 v[188:189], v216 offset:28672\n"
    "v_accvgpr_read_b32 v213, a229\n"
    "v_mfma_f32_16x16x32_bf16 a[20:23], v[12:15], v[108:111], a[20:23]\n"
    "s_waitcnt lgkmcnt(7)\n"
    "ds_read_b64_tr_b16 v[190:191], v216 offset:30720\n"
    "buffer_load_dwordx4 v254, s[72:75], s62 offen offset:2048 lds\n"
    "v_accvgpr_read_b32 v214, a230\n"
    "v_accvgpr_read_b32 v215, a231\n"
    "v_accvgpr_read_b32 v220, a232\n"
    "v_accvgpr_read_b32 v221, a233\n"
    "v_mfma_f32_16x16x32_bf16 a[24:27], v[16:19], v[80:83], a[24:27]\n"
    "ds_read_b64_tr_b16 v[192:193], v217 offset:16384\n"
    "v_accvgpr_read_b32 v222, a234\n"
    "v_mfma_f32_16x16x32_bf16 a[24:27], v[20:23], v[84:87], a[24:27]\n"
    "ds_read_b64_tr_b16 v[194:195], v217 offset:18432\n"
    "v_accvgpr_read_b32 v230, a192\n"
    "v_mfma_f32_16x16x32_bf16 a[24:27], v[24:27], v[88:91], a[24:27]\n"
    "ds_read_b64_tr_b16 v[196:197], v217 offset:20480\n"
    "v_accvgpr_read_b32 v231, a196\n"
    "v_mfma_f32_16x16x32_bf16 a[24:27], v[28:31], v[92:95], a[24:27]\n"
    "buffer_load_dwordx4 v255, s[72:75], s62 offen offset:3072 lds\n"
    "ds_read_b64_tr_b16 v[198:199], v217 offset:22528\n"
    "v_rcp_f32 v230, v230\n"
    "v_rcp_f32 v231, v231\n"
    "v_mfma_f32_16x16x32_bf16 a[28:31], v[16:19], v[96:99], a[28:31]\n"
    "ds_read_b64_tr_b16 v[200:201], v217 offset:24576\n"
    "v_mfma_f32_16x16x32_bf16 a[28:31], v[20:23], v[100:103], a[28:31]\n"
    "ds_read_b64_tr_b16 v[202:203], v217 offset:26624\n"
    "v_mfma_f32_16x16x32_bf16 a[28:31], v[24:27], v[104:107], a[28:31]\n"
    "s_waitcnt vmcnt(12)\n"
    "ds_read_b64_tr_b16 v[204:205], v217 offset:28672\n"
    "v_mfma_f32_16x16x32_bf16 a[28:31], v[28:31], v[108:111], a[28:31]\n"
    "ds_read_b64_tr_b16 v[206:207], v217 offset:30720\n"
    "s_barrier\n"
    "s_waitcnt lgkmcnt(8)\n"
    "v_mfma_f32_16x16x32_bf16 a[32:35], v[176:179], v[80:83], a[32:35]\n"
    "s_add_u32 m0, s59, 49152\n"
    "ds_read_b64_tr_b16 v[0:1], v218 offset:16384\n"
    "v_accvgpr_read_b32 v112, a0\n"
    "v_accvgpr_read_b32 v113, a1\n"
    "v_accvgpr_read_b32 v114, a2\n"
    "v_accvgpr_read_b32 v115, a3\n"
    "v_accvgpr_read_b32 v116, a8\n"
    "v_accvgpr_read_b32 v117, a9\n"
    "v_mfma_f32_16x16x32_bf16 a[32:35], v[180:183], v[84:87], a[32:35]\n"
    "s_add_u32 s62, s84, 344064\n"
    "ds_read_b64_tr_b16 v[2:3], v218 offset:18432\n"
    "v_accvgpr_read_b32 v118, a10\n"
    "v_accvgpr_read_b32 v119, a11\n"
    "v_mul_f32 v112, v112, v230\n"
    "v_mul_f32 v113, v113, v230\n"
    "v_mul_f32 v114, v114, v230\n"
    "v_mul_f32 v115, v115, v230\n"
    "v_mfma_f32_16x16x32_bf16 a[32:35], v[184:187], v[88:91], a[32:35]\n"
    "ds_read_b64_tr_b16 v[4:5], v218 offset:20480\n"
    "v_mul_f32 v116, v116, v230\n"
    "v_mfma_f32_16x16x32_bf16 a[32:35], v[188:191], v[92:95], a[32:35]\n"
    "ds_read_b64_tr_b16 v[144:145], v236 offset:0\n"
    "ds_read_b64_tr_b16 v[146:147], v236 offset:2048\n"
    "ds_read_b64_tr_b16 v[148:149], v236 offset:4096\n"
    "buffer_load_dwordx4 v252, s[72:75], s62 offen offset:0 lds\n"
    "ds_read_b64_tr_b16 v[6:7], v218 offset:22528\n"
    "v_mul_f32 v117, v117, v230\n"
    "v_mul_f32 v118, v118, v230\n"
    "v_mul_f32 v119, v119, v230\n"
    "v_cvt_pk_bf16_f32 v120, v112, v113\n"
    "v_cvt_pk_bf16_f32 v121, v114, v115\n"
    "v_cvt_pk_bf16_f32 v122, v116, v117\n"
    "v_mfma_f32_16x16x32_bf16 a[36:39], v[176:179], v[96:99], a[36:39]\n"
    "ds_read_b64_tr_b16 v[8:9], v218 offset:24576\n"
    "v_cvt_pk_bf16_f32 v123, v118, v119\n"
    "v_mfma_f32_16x16x32_bf16 a[36:39], v[180:183], v[100:103], a[36:39]\n"
    "ds_read_b64_tr_b16 v[10:11], v218 offset:26624\n"
    "ds_write_b128 v233, v[120:123] offset:0\n"
    "v_accvgpr_read_b32 v128, a4\n"
    "v_accvgpr_read_b32 v129, a5\n"
    "v_accvgpr_read_b32 v130, a6\n"
    "v_accvgpr_read_b32 v131, a7\n"
    "v_accvgpr_read_b32 v132, a12\n"
    "v_mfma_f32_16x16x32_bf16 a[36:39], v[184:187], v[104:107], a[36:39]\n"
    "ds_read_b64_tr_b16 v[12:13], v218 offset:28672\n"
    "v_accvgpr_read_b32 v133, a13\n"
    "v_accvgpr_read_b32 v134, a14\n"
    "v_accvgpr_read_b32 v135, a15\n"
    "v_mul_f32 v128, v128, v231\n"
    "v_mul_f32 v129, v129, v231\n"
    "v_mul_f32 v130, v130, v231\n"
    "v_mfma_f32_16x16x32_bf16 a[36:39], v[188:191], v[108:111], a[36:39]\n"
    "s_waitcnt lgkmcnt(11)\n"
    "ds_read_b64_tr_b16 v[14:15], v218 offset:30720\n"
    "buffer_load_dwordx4 v253, s[72:75], s62 offen offset:1024 lds\n"
    "ds_read_b64_tr_b16 v[150:151], v236 offset:6144\n"
    "ds_read_b64_tr_b16 v[152:153], v236 offset:8192\n"
    "ds_read_b64_tr_b16 v[154:155], v236 offset:10240\n"
    "v_mul_f32 v131, v131, v231\n"
    "v_mul_f32 v132, v132, v231\n"
    "v_mul_f32 v133, v133, v231\n"
    "v_mul_f32 v134, v134, v231\n"
    "v_mul_f32 v135, v135, v231\n"
    "v_cvt_pk_bf16_f32 v136, v128, v129\n"
    "v_mfma_f32_16x16x32_bf16 a[40:43], v[192:195], v[80:83], a[40:43]\n"
    "ds_read_b64_tr_b16 v[16:17], v219 offset:16384\n"
    "v_cvt_pk_bf16_f32 v137, v130, v131\n"
    "v_mfma_f32_16x16x32_bf16 a[40:43], v[196:199], v[84:87], a[40:43]\n"
    "ds_read_b64_tr_b16 v[18:19], v219 offset:18432\n"
    "v_cvt_pk_bf16_f32 v138, v132, v133\n"
    "v_mfma_f32_16x16x32_bf16 a[40:43], v[200:203], v[88:91], a[40:43]\n"
    "ds_read_b64_tr_b16 v[20:21], v219 offset:20480\n"
    "v_cvt_pk_bf16_f32 v139, v134, v135\n"
    "v_mfma_f32_16x16x32_bf16 a[40:43], v[204:207], v[92:95], a[40:43]\n"
    "ds_read_b64_tr_b16 v[156:157], v236 offset:12288\n"
    "ds_read_b64_tr_b16 v[158:159], v236 offset:14336\n"
    "buffer_load_dwordx4 v254, s[72:75], s62 offen offset:2048 lds\n"
    "ds_read_b64_tr_b16 v[22:23], v219 offset:22528\n"
    "ds_write_b128 v233, v[136:139] offset:2048\n"
    "v_accvgpr_read_b32 v112, a16\n"
    "v_accvgpr_read_b32 v113, a17\n"
    "v_accvgpr_read_b32 v114, a18\n"
    "v_accvgpr_read_b32 v115, a19\n"
    "v_accvgpr_read_b32 v116, a24\n"
    "v_mfma_f32_16x16x32_bf16 a[44:47], v[192:195], v[96:99], a[44:47]\n"
    "ds_read_b64_tr_b16 v[24:25], v219 offset:24576\n"
    "v_accvgpr_read_b32 v117, a25\n"
    "v_mfma_f32_16x16x32_bf16 a[44:47], v[196:199], v[100:103], a[44:47]\n"
    "ds_read_b64_tr_b16 v[26:27], v219 offset:26624\n"
    "v_accvgpr_read_b32 v118, a26\n"
    "v_mfma_f32_16x16x32_bf16 a[44:47], v[200:203], v[104:107], a[44:47]\n"
    "ds_read_b64_tr_b16 v[28:29], v219 offset:28672\n"
    "v_accvgpr_read_b32 v119, a27\n"
    "v_mfma_f32_16x16x32_bf16 a[44:47], v[204:207], v[108:111], a[44:47]\n"
    "s_waitcnt lgkmcnt(13)\n"
    "ds_read_b64_tr_b16 v[30:31], v219 offset:30720\n"
    "buffer_load_dwordx4 v255, s[72:75], s62 offen offset:3072 lds\n"
    "ds_read_b64_tr_b16 v[160:161], v236 offset:16384\n"
    "ds_read_b64_tr_b16 v[162:163], v236 offset:18432\n"
    "v_mul_f32 v112, v112, v230\n"
    "v_mul_f32 v113, v113, v230\n"
    "v_mul_f32 v114, v114, v230\n"
    "v_mul_f32 v115, v115, v230\n"
    "v_mul_f32 v116, v116, v230\n"
    "v_mul_f32 v117, v117, v230\n"
    "v_mfma_f32_16x16x32_bf16 a[48:51], v[0:3], v[80:83], a[48:51]\n"
    "ds_read_b64_tr_b16 v[176:177], v216 offset:32768\n"
    "v_mul_f32 v118, v118, v230\n"
    "v_mfma_f32_16x16x32_bf16 a[48:51], v[4:7], v[84:87], a[48:51]\n"
    "ds_read_b64_tr_b16 v[178:179], v216 offset:34816\n"
    "v_mul_f32 v119, v119, v230\n"
    "v_mfma_f32_16x16x32_bf16 a[48:51], v[8:11], v[88:91], a[48:51]\n"
    "ds_read_b64_tr_b16 v[180:181], v216 offset:36864\n"
    "v_cvt_pk_bf16_f32 v120, v112, v113\n"
    "v_mfma_f32_16x16x32_bf16 a[48:51], v[12:15], v[92:95], a[48:51]\n"
    "ds_read_b64_tr_b16 v[164:165], v236 offset:20480\n"
    "ds_read_b64_tr_b16 v[166:167], v236 offset:22528\n"
    "ds_read_b64_tr_b16 v[182:183], v216 offset:38912\n"
    "v_cvt_pk_bf16_f32 v121, v114, v115\n"
    "v_cvt_pk_bf16_f32 v122, v116, v117\n"
    "v_cvt_pk_bf16_f32 v123, v118, v119\n"
    "ds_write_b128 v233, v[120:123] offset:1024\n"
    "v_accvgpr_read_b32 v128, a20\n"
    "v_accvgpr_read_b32 v129, a21\n"
    "v_mfma_f32_16x16x32_bf16 a[52:55], v[0:3], v[96:99], a[52:55]\n"
    "ds_read_b64_tr_b16 v[184:185], v216 offset:40960\n"
    "v_accvgpr_read_b32 v130, a22\n"
    "v_mfma_f32_16x16x32_bf16 a[52:55], v[4:7], v[100:103], a[52:55]\n"
    "ds_read_b64_tr_b16 v[186:187], v216 offset:43008\n"
    "v_accvgpr_read_b32 v131, a23\n"
    "v_mfma_f32_16x16x32_bf16 a[52:55], v[8:11], v[104:107], a[52:55]\n"
    "ds_read_b64_tr_b16 v[188:189], v216 offset:45056\n"
    "v_accvgpr_read_b32 v132, a28\n"
    "v_mfma_f32_16x16x32_bf16 a[52:55], v[12:15], v[108:111], a[52:55]\n"
    "ds_read_b64_tr_b16 v[168:169], v236 offset:24576\n"
    "ds_read_b64_tr_b16 v[170:171], v236 offset:26624\n"
    "s_waitcnt lgkmcnt(14)\n"
    "ds_read_b64_tr_b16 v[190:191], v216 offset:47104\n"
    "v_accvgpr_read_b32 v133, a29\n"
    "v_accvgpr_read_b32 v134, a30\n"
    "v_accvgpr_read_b32 v135, a31\n"
    "v_mul_f32 v128, v128, v231\n"
    "v_mul_f32 v129, v129, v231\n"
    "v_mul_f32 v130, v130, v231\n"
    "v_mfma_f32_16x16x32_bf16 a[56:59], v[16:19], v[80:83], a[56:59]\n"
    "ds_read_b64_tr_b16 v[192:193], v217 offset:32768\n"
    "v_mul_f32 v131, v131, v231\n"
    "v_mfma_f32_16x16x32_bf16 a[56:59], v[20:23], v[84:87], a[56:59]\n"
    "ds_read_b64_tr_b16 v[194:195], v217 offset:34816\n"
    "v_mul_f32 v132, v132, v231\n"
    "v_mfma_f32_16x16x32_bf16 a[56:59], v[24:27], v[88:91], a[56:59]\n"
    "ds_read_b64_tr_b16 v[196:197], v217 offset:36864\n"
    "v_mul_f32 v133, v133, v231\n"
    "v_mfma_f32_16x16x32_bf16 a[56:59], v[28:31], v[92:95], a[56:59]\n"
    "ds_read_b64_tr_b16 v[172:173], v236 offset:28672\n"
    "ds_read_b64_tr_b16 v[174:175], v236 offset:30720\n"
    "ds_read_b64_tr_b16 v[198:199], v217 offset:38912\n"
    "v_mul_f32 v134, v134, v231\n"
    "v_mul_f32 v135, v135, v231\n"
    "v_cvt_pk_bf16_f32 v136, v128, v129\n"
    "v_cvt_pk_bf16_f32 v137, v130, v131\n"
    "v_cvt_pk_bf16_f32 v138, v132, v133\n"
    "v_cvt_pk_bf16_f32 v139, v134, v135\n"
    "v_mfma_f32_16x16x32_bf16 a[60:63], v[16:19], v[96:99], a[60:63]\n"
    "ds_read_b64_tr_b16 v[200:201], v217 offset:40960\n"
    "ds_write_b128 v233, v[136:139] offset:3072\n"
    "v_mfma_f32_16x16x32_bf16 a[60:63], v[20:23], v[100:103], a[60:63]\n"
    "ds_read_b64_tr_b16 v[202:203], v217 offset:43008\n"
    "v_mfma_f32_16x16x32_bf16 a[60:63], v[24:27], v[104:107], a[60:63]\n"
    "s_waitcnt vmcnt(12)\n"
    "ds_read_b64_tr_b16 v[204:205], v217 offset:45056\n"
    "v_mfma_f32_16x16x32_bf16 a[60:63], v[28:31], v[108:111], a[60:63]\n"
    "s_waitcnt lgkmcnt(2)\n"
    "ds_read_b64_tr_b16 v[206:207], v217 offset:47104\n"
    "s_barrier\n"
    "v_mfma_f32_16x16x32_bf16 a[64:67], v[176:179], v[80:83], a[64:67]\n"
    "s_add_u32 m0, s59, 131072\n"
    "ds_read_b64_tr_b16 v[0:1], v218 offset:32768\n"
    "v_accvgpr_read_b32 v112, a32\n"
    "v_accvgpr_read_b32 v113, a33\n"
    "v_accvgpr_read_b32 v114, a34\n"
    "v_accvgpr_read_b32 v115, a35\n"
    "v_accvgpr_read_b32 v116, a40\n"
    "v_mfma_f32_16x16x32_bf16 a[64:67], v[180:183], v[84:87], a[64:67]\n"
    "s_add_u32 s62, s84, 114816\n"
    "ds_read_b64_tr_b16 v[2:3], v218 offset:34816\n"
    "v_accvgpr_read_b32 v117, a41\n"
    "v_accvgpr_read_b32 v118, a42\n"
    "v_accvgpr_read_b32 v119, a43\n"
    "v_mul_f32 v112, v112, v230\n"
    "v_mul_f32 v113, v113, v230\n"
    "v_mfma_f32_16x16x32_bf16 a[64:67], v[184:187], v[88:91], a[64:67]\n"
    "ds_read_b64_tr_b16 v[4:5], v218 offset:36864\n"
    "v_mul_f32 v114, v114, v230\n"
    "v_mfma_f32_16x16x32_bf16 a[64:67], v[188:191], v[92:95], a[64:67]\n"
    "buffer_load_dwordx4 v252, s[72:75], s62 offen offset:0 lds\n"
    "ds_read_b64_tr_b16 v[6:7], v218 offset:38912\n"
    "v_mul_f32 v115, v115, v230\n"
    "v_mul_f32 v116, v116, v230\n"
    "v_mul_f32 v117, v117, v230\n"
    "v_mul_f32 v118, v118, v230\n"
    "v_mul_f32 v119, v119, v230\n"
    "v_cvt_pk_bf16_f32 v120, v112, v113\n"
    "v_mfma_f32_16x16x32_bf16 a[68:71], v[176:179], v[96:99], a[68:71]\n"
    "ds_read_b64_tr_b16 v[8:9], v218 offset:40960\n"
    "v_cvt_pk_bf16_f32 v121, v114, v115\n"
    "v_mfma_f32_16x16x32_bf16 a[68:71], v[180:183], v[100:103], a[68:71]\n"
    "ds_read_b64_tr_b16 v[10:11], v218 offset:43008\n"
    "v_cvt_pk_bf16_f32 v122, v116, v117\n"
    "v_mfma_f32_16x16x32_bf16 a[68:71], v[184:187], v[104:107], a[68:71]\n"
    "ds_read_b64_tr_b16 v[12:13], v218 offset:45056\n"
    "v_cvt_pk_bf16_f32 v123, v118, v119\n"
    "v_mfma_f32_16x16x32_bf16 a[68:71], v[188:191], v[108:111], a[68:71]\n"
    "s_waitcnt lgkmcnt(7)\n"
    "ds_read_b64_tr_b16 v[14:15], v218 offset:47104\n"
    "buffer_load_dwordx4 v253, s[72:75], s62 offen offset:1024 lds\n"
    "ds_write_b128 v233, v[120:123] offset:16384\n"
    "v_accvgpr_read_b32 v128, a36\n"
    "v_accvgpr_read_b32 v129, a37\n"
    "v_accvgpr_read_b32 v130, a38\n"
    "v_mfma_f32_16x16x32_bf16 a[72:75], v[192:195], v[80:83], a[72:75]\n"
    "ds_read_b64_tr_b16 v[16:17], v219 offset:32768\n"
    "v_accvgpr_read_b32 v131, a39\n"
    "v_accvgpr_read_b32 v132, a44\n"
    "v_accvgpr_read_b32 v133, a45\n"
    "v_accvgpr_read_b32 v134, a46\n"
    "v_accvgpr_read_b32 v135, a47\n"
    "v_mul_f32 v128, v128, v231\n"
    "v_mfma_f32_16x16x32_bf16 a[72:75], v[196:199], v[84:87], a[72:75]\n"
    "ds_read_b64_tr_b16 v[18:19], v219 offset:34816\n"
    "v_mul_f32 v129, v129, v231\n"
    "v_mfma_f32_16x16x32_bf16 a[72:75], v[200:203], v[88:91], a[72:75]\n"
    "ds_read_b64_tr_b16 v[20:21], v219 offset:36864\n"
    "v_mul_f32 v130, v130, v231\n"
    "v_mfma_f32_16x16x32_bf16 a[72:75], v[204:207], v[92:95], a[72:75]\n"
    "buffer_load_dwordx4 v254, s[72:75], s62 offen offset:2048 lds\n"
    "ds_read_b64_tr_b16 v[22:23], v219 offset:38912\n"
    "v_mul_f32 v131, v131, v231\n"
    "v_mul_f32 v132, v132, v231\n"
    "v_mul_f32 v133, v133, v231\n"
    "v_mul_f32 v134, v134, v231\n"
    "v_mul_f32 v135, v135, v231\n"
    "v_cvt_pk_bf16_f32 v136, v128, v129\n"
    "v_mfma_f32_16x16x32_bf16 a[76:79], v[192:195], v[96:99], a[76:79]\n"
    "ds_read_b64_tr_b16 v[24:25], v219 offset:40960\n"
    "v_cvt_pk_bf16_f32 v137, v130, v131\n"
    "v_mfma_f32_16x16x32_bf16 a[76:79], v[196:199], v[100:103], a[76:79]\n"
    "ds_read_b64_tr_b16 v[26:27], v219 offset:43008\n"
    "v_cvt_pk_bf16_f32 v138, v132, v133\n"
    "v_mfma_f32_16x16x32_bf16 a[76:79], v[200:203], v[104:107], a[76:79]\n"
    "ds_read_b64_tr_b16 v[28:29], v219 offset:45056\n"
    "v_cvt_pk_bf16_f32 v139, v134, v135\n"
    "v_mfma_f32_16x16x32_bf16 a[76:79], v[204:207], v[108:111], a[76:79]\n"
    "s_waitcnt lgkmcnt(8)\n"
    "ds_read_b64_tr_b16 v[30:31], v219 offset:47104\n"
    "buffer_load_dwordx4 v255, s[72:75], s62 offen offset:3072 lds\n"
    "ds_write_b128 v233, v[136:139] offset:18432\n"
    "v_accvgpr_read_b32 v112, a48\n"
    "v_accvgpr_read_b32 v113, a49\n"
    "v_accvgpr_read_b32 v114, a50\n"
    "v_accvgpr_read_b32 v115, a51\n"
    "v_accvgpr_read_b32 v116, a56\n"
    "v_mfma_f32_16x16x32_bf16 a[80:83], v[0:3], v[80:83], a[80:83]\n"
    "ds_read_b64_tr_b16 v[176:177], v216 offset:49152\n"
    "v_accvgpr_read_b32 v117, a57\n"
    "v_accvgpr_read_b32 v118, a58\n"
    "v_accvgpr_read_b32 v119, a59\n"
    "v_mul_f32 v112, v112, v230\n"
    "v_mul_f32 v113, v113, v230\n"
    "v_mul_f32 v114, v114, v230\n"
    "v_mfma_f32_16x16x32_bf16 a[80:83], v[4:7], v[84:87], a[80:83]\n"
    "ds_read_b64_tr_b16 v[178:179], v216 offset:51200\n"
    "v_mul_f32 v115, v115, v230\n"
    "v_mfma_f32_16x16x32_bf16 a[80:83], v[8:11], v[88:91], a[80:83]\n"
    "ds_read_b64_tr_b16 v[180:181], v216 offset:53248\n"
    "v_mul_f32 v116, v116, v230\n"
    "v_mfma_f32_16x16x32_bf16 a[80:83], v[12:15], v[92:95], a[80:83]\n"
    "ds_read_b64_tr_b16 v[182:183], v216 offset:55296\n"
    "v_mul_f32 v117, v117, v230\n"
    "v_mul_f32 v118, v118, v230\n"
    "v_mul_f32 v119, v119, v230\n"
    "v_cvt_pk_bf16_f32 v120, v112, v113\n"
    "v_cvt_pk_bf16_f32 v121, v114, v115\n"
    "v_cvt_pk_bf16_f32 v122, v116, v117\n"
    "v_mfma_f32_16x16x32_bf16 a[84:87], v[0:3], v[96:99], a[84:87]\n"
    "ds_read_b64_tr_b16 v[184:185], v216 offset:57344\n"
    "v_cvt_pk_bf16_f32 v123, v118, v119\n"
    "v_mfma_f32_16x16x32_bf16 a[84:87], v[4:7], v[100:103], a[84:87]\n"
    "ds_read_b64_tr_b16 v[186:187], v216 offset:59392\n"
    "ds_write_b128 v233, v[120:123] offset:17408\n"
    "v_accvgpr_read_b32 v128, a52\n"
    "v_accvgpr_read_b32 v129, a53\n"
    "v_accvgpr_read_b32 v130, a54\n"
    "v_accvgpr_read_b32 v131, a55\n"
    "v_mfma_f32_16x16x32_bf16 a[84:87], v[8:11], v[104:107], a[84:87]\n"
    "ds_read_b64_tr_b16 v[188:189], v216 offset:61440\n"
    "v_accvgpr_read_b32 v132, a60\n"
    "v_mfma_f32_16x16x32_bf16 a[84:87], v[12:15], v[108:111], a[84:87]\n"
    "s_waitcnt lgkmcnt(9)\n"
    "ds_read_b64_tr_b16 v[190:191], v216 offset:63488\n"
    "v_accvgpr_read_b32 v133, a61\n"
    "v_accvgpr_read_b32 v134, a62\n"
    "v_accvgpr_read_b32 v135, a63\n"
    "v_mul_f32 v128, v128, v231\n"
    "v_mul_f32 v129, v129, v231\n"
    "v_mul_f32 v130, v130, v231\n"
    "v_mfma_f32_16x16x32_bf16 a[88:91], v[16:19], v[80:83], a[88:91]\n"
    "ds_read_b64_tr_b16 v[192:193], v217 offset:49152\n"
    "v_mul_f32 v131, v131, v231\n"
    "v_mfma_f32_16x16x32_bf16 a[88:91], v[20:23], v[84:87], a[88:91]\n"
    "ds_read_b64_tr_b16 v[194:195], v217 offset:51200\n"
    "v_mul_f32 v132, v132, v231\n"
    "v_mfma_f32_16x16x32_bf16 a[88:91], v[24:27], v[88:91], a[88:91]\n"
    "ds_read_b64_tr_b16 v[196:197], v217 offset:53248\n"
    "v_mul_f32 v133, v133, v231\n"
    "v_mfma_f32_16x16x32_bf16 a[88:91], v[28:31], v[92:95], a[88:91]\n"
    "ds_read_b64_tr_b16 v[198:199], v217 offset:55296\n"
    "v_mul_f32 v134, v134, v231\n"
    "v_mul_f32 v135, v135, v231\n"
    "v_cvt_pk_bf16_f32 v136, v128, v129\n"
    "v_cvt_pk_bf16_f32 v137, v130, v131\n"
    "v_cvt_pk_bf16_f32 v138, v132, v133\n"
    "v_cvt_pk_bf16_f32 v139, v134, v135\n"
    "v_mfma_f32_16x16x32_bf16 a[92:95], v[16:19], v[96:99], a[92:95]\n"
    "ds_read_b64_tr_b16 v[200:201], v217 offset:57344\n"
    "ds_write_b128 v233, v[136:139] offset:19456\n"
    "v_mfma_f32_16x16x32_bf16 a[92:95], v[20:23], v[100:103], a[92:95]\n"
    "ds_read_b64_tr_b16 v[202:203], v217 offset:59392\n"
    "v_mfma_f32_16x16x32_bf16 a[92:95], v[24:27], v[104:107], a[92:95]\n"
    "ds_read_b64_tr_b16 v[204:205], v217 offset:61440\n"
    "v_mfma_f32_16x16x32_bf16 a[92:95], v[28:31], v[108:111], a[92:95]\n"
    "s_waitcnt lgkmcnt(2)\n"
    "ds_read_b64_tr_b16 v[206:207], v217 offset:63488\n"
    "s_barrier\n"
    "v_mfma_f32_16x16x32_bf16 a[96:99], v[176:179], v[80:83], a[96:99]\n"
    "s_add_u32 m0, s59, 147456\n"
    "ds_read_b64_tr_b16 v[0:1], v218 offset:49152\n"
    "v_accvgpr_read_b32 v112, a64\n"
    "v_mfma_f32_16x16x32_bf16 a[96:99], v[180:183], v[84:87], a[96:99]\n"
    "s_add_u32 s62, s84, 344192\n"
    "ds_read_b64_tr_b16 v[2:3], v218 offset:51200\n"
    "v_accvgpr_read_b32 v113, a65\n"
    "v_mfma_f32_16x16x32_bf16 a[96:99], v[184:187], v[88:91], a[96:99]\n"
    "ds_read_b64_tr_b16 v[4:5], v218 offset:53248\n"
    "v_accvgpr_read_b32 v114, a66\n"
    "v_mfma_f32_16x16x32_bf16 a[96:99], v[188:191], v[92:95], a[96:99]\n"
    "ds_read_b128 a[128:131], v237 offset:0\n"
    "ds_read_b128 a[144:147], v237 offset:1024\n"
    "ds_read_b64_tr_b16 v[6:7], v218 offset:55296\n"
    "v_accvgpr_read_b32 v115, a67\n"
    "v_accvgpr_read_b32 v116, a72\n"
    "v_accvgpr_read_b32 v117, a73\n"
    "v_mfma_f32_16x16x32_bf16 a[100:103], v[176:179], v[96:99], a[100:103]\n"
    "ds_read_b64_tr_b16 v[8:9], v218 offset:57344\n"
    "v_accvgpr_read_b32 v118, a74\n"
    "v_mfma_f32_16x16x32_bf16 a[100:103], v[180:183], v[100:103], a[100:103]\n"
    "ds_read_b64_tr_b16 v[10:11], v218 offset:59392\n"
    "v_accvgpr_read_b32 v119, a75\n"
    "v_mfma_f32_16x16x32_bf16 a[100:103], v[184:187], v[104:107], a[100:103]\n"
    "ds_read_b64_tr_b16 v[12:13], v218 offset:61440\n"
    "v_mul_f32 v112, v112, v230\n"
    "v_mfma_f32_16x16x32_bf16 a[100:103], v[188:191], v[108:111], a[100:103]\n"
    "ds_read_b128 a[160:163], v237 offset:16384\n"
    "s_waitcnt lgkmcnt(10)\n"
    "ds_read_b64_tr_b16 v[14:15], v218 offset:63488\n"
    "v_mul_f32 v113, v113, v230\n"
    "v_mul_f32 v114, v114, v230\n"
    "v_mul_f32 v115, v115, v230\n"
    "v_mfma_f32_16x16x32_bf16 a[104:107], v[192:195], v[80:83], a[104:107]\n"
    "ds_read_b64_tr_b16 v[16:17], v219 offset:49152\n"
    "v_mul_f32 v116, v116, v230\n"
    "v_mfma_f32_16x16x32_bf16 a[104:107], v[196:199], v[84:87], a[104:107]\n"
    "ds_read_b64_tr_b16 v[18:19], v219 offset:51200\n"
    "v_mul_f32 v117, v117, v230\n"
    "v_mfma_f32_16x16x32_bf16 a[104:107], v[200:203], v[88:91], a[104:107]\n"
    "ds_read_b64_tr_b16 v[20:21], v219 offset:53248\n"
    "v_mul_f32 v118, v118, v230\n"
    "v_mfma_f32_16x16x32_bf16 a[104:107], v[204:207], v[92:95], a[104:107]\n"
    "ds_read_b128 a[176:179], v237 offset:17408\n"
    "ds_read_b64_tr_b16 v[22:23], v219 offset:55296\n"
    "v_mul_f32 v119, v119, v230\n"
    "v_cvt_pk_bf16_f32 v120, v112, v113\n"
    "v_mfma_f32_16x16x32_bf16 a[108:111], v[192:195], v[96:99], a[108:111]\n"
    "ds_read_b64_tr_b16 v[24:25], v219 offset:57344\n"
    "v_cvt_pk_bf16_f32 v121, v114, v115\n"
    "v_mfma_f32_16x16x32_bf16 a[108:111], v[196:199], v[100:103], a[108:111]\n"
    "ds_read_b64_tr_b16 v[26:27], v219 offset:59392\n"
    "v_cvt_pk_bf16_f32 v122, v116, v117\n"
    "v_mfma_f32_16x16x32_bf16 a[108:111], v[200:203], v[104:107], a[108:111]\n"
    "ds_read_b64_tr_b16 v[28:29], v219 offset:61440\n"
    "v_cvt_pk_bf16_f32 v123, v118, v119\n"
    "v_mfma_f32_16x16x32_bf16 a[108:111], v[204:207], v[108:111], a[108:111]\n"
    "ds_read_b128 a[192:195], v237 offset:8192\n"
    "s_waitcnt lgkmcnt(9)\n"
    "ds_read_b64_tr_b16 v[30:31], v219 offset:63488\n"
    "ds_write_b128 v233, v[120:123] offset:32768\n"
    "v_mfma_f32_16x16x32_bf16 a[112:115], v[0:3], v[80:83], a[112:115]\n"
    "ds_read_b128 a[208:211], v237 offset:9216\n"
    "buffer_load_dwordx4 v252, s[72:75], s62 offen offset:0 lds\n"
    "ds_read_b128 a[224:227], v237 offset:24576\n"
    "v_accvgpr_read_b32 v128, a68\n"
    "v_accvgpr_read_b32 v129, a69\n"
    "v_accvgpr_read_b32 v130, a70\n"
    "v_mfma_f32_16x16x32_bf16 a[112:115], v[4:7], v[84:87], a[112:115]\n"
    "ds_read_b128 a[240:243], v237 offset:25600\n"
    "buffer_load_dwordx4 v253, s[72:75], s62 offen offset:1024 lds\n"
    "ds_read_b128 a[132:135], v237 offset:2048\n"
    "v_accvgpr_read_b32 v131, a71\n"
    "v_accvgpr_read_b32 v132, a76\n"
    "v_accvgpr_read_b32 v133, a77\n"
    "v_mfma_f32_16x16x32_bf16 a[112:115], v[8:11], v[88:91], a[112:115]\n"
    "ds_read_b128 a[148:151], v237 offset:3072\n"
    "buffer_load_dwordx4 v254, s[72:75], s62 offen offset:2048 lds\n"
    "ds_read_b128 a[164:167], v237 offset:18432\n"
    "v_accvgpr_read_b32 v134, a78\n"
    "v_accvgpr_read_b32 v135, a79\n"
    "v_mul_f32 v128, v128, v231\n"
    "v_mfma_f32_16x16x32_bf16 a[112:115], v[12:15], v[92:95], a[112:115]\n"
    "ds_read_b128 a[180:183], v237 offset:19456\n"
    "buffer_load_dwordx4 v255, s[72:75], s62 offen offset:3072 lds\n"
    "ds_read_b128 a[196:199], v237 offset:10240\n"
    "v_mul_f32 v129, v129, v231\n"
    "v_mul_f32 v130, v130, v231\n"
    "v_mul_f32 v131, v131, v231\n"
    "v_mfma_f32_16x16x32_bf16 a[116:119], v[0:3], v[96:99], a[116:119]\n"
    "ds_read_b128 a[212:215], v237 offset:11264\n"
    "v_mul_f32 v132, v132, v231\n"
    "v_mfma_f32_16x16x32_bf16 a[116:119], v[4:7], v[100:103], a[116:119]\n"
    "ds_read_b128 a[228:231], v237 offset:26624\n"
    "v_mul_f32 v133, v133, v231\n"
    "v_mfma_f32_16x16x32_bf16 a[116:119], v[8:11], v[104:107], a[116:119]\n"
    "ds_read_b128 a[244:247], v237 offset:27648\n"
    "v_mul_f32 v134, v134, v231\n"
    "v_mfma_f32_16x16x32_bf16 a[116:119], v[12:15], v[108:111], a[116:119]\n"
    "ds_read_b128 a[136:139], v237 offset:4096\n"
    "s_waitcnt lgkmcnt(13)\n"
    "ds_read_b128 a[152:155], v237 offset:5120\n"
    "v_mul_f32 v135, v135, v231\n"
    "v_cvt_pk_bf16_f32 v136, v128, v129\n"
    "v_mfma_f32_16x16x32_bf16 a[120:123], v[16:19], v[80:83], a[120:123]\n"
    "ds_read_b128 a[168:171], v237 offset:20480\n"
    "v_cvt_pk_bf16_f32 v137, v130, v131\n"
    "v_mfma_f32_16x16x32_bf16 a[120:123], v[20:23], v[84:87], a[120:123]\n"
    "ds_read_b128 a[184:187], v237 offset:21504\n"
    "v_cvt_pk_bf16_f32 v138, v132, v133\n"
    "v_mfma_f32_16x16x32_bf16 a[120:123], v[24:27], v[88:91], a[120:123]\n"
    "ds_read_b128 a[200:203], v237 offset:12288\n"
    "v_cvt_pk_bf16_f32 v139, v134, v135\n"
    "v_mfma_f32_16x16x32_bf16 a[120:123], v[28:31], v[92:95], a[120:123]\n"
    "ds_read_b128 a[216:219], v237 offset:13312\n"
    "ds_read_b128 a[232:235], v237 offset:28672\n"
    "ds_write_b128 v233, v[136:139] offset:34816\n"
    "v_mfma_f32_16x16x32_bf16 a[124:127], v[16:19], v[96:99], a[124:127]\n"
    "ds_read_b128 a[248:251], v237 offset:29696\n"
    "v_mfma_f32_16x16x32_bf16 a[124:127], v[20:23], v[100:103], a[124:127]\n"
    "v_mfma_f32_16x16x32_bf16 a[124:127], v[24:27], v[104:107], a[124:127]\n"
    "v_mfma_f32_16x16x32_bf16 a[124:127], v[28:31], v[108:111], a[124:127]\n"
    "s_waitcnt lgkmcnt(0)\n"
    "s_barrier\n"
    "v_mfma_f32_16x16x32_bf16 v[0:3], v[144:147], a[128:131], 0\n"
    "ds_read_b128 a[140:143], v237 offset:6144\n"
    "v_accvgpr_read_b32 v112, a80\n"
    "v_accvgpr_read_b32 v113, a81\n"
    "v_accvgpr_read_b32 v114, a82\n"
    "v_accvgpr_read_b32 v115, a83\n"
    "v_accvgpr_read_b32 v116, a88\n"
    "v_accvgpr_read_b32 v117, a89\n"
    "v_accvgpr_read_b32 v118, a90\n"
    "v_accvgpr_read_b32 v119, a91\n"
    "v_mfma_f32_16x16x32_bf16 v[0:3], v[148:151], a[144:147], v[0:3]\n"
    "ds_read_b128 a[156:159], v237 offset:7168\n"
    "v_mul_f32 v112, v112, v230\n"
    "v_mul_f32 v113, v113, v230\n"
    "v_mul_f32 v114, v114, v230\n"
    "v_mul_f32 v115, v115, v230\n"
    "v_mul_f32 v116, v116, v230\n"
    "v_mul_f32 v117, v117, v230\n"
    "v_mul_f32 v118, v118, v230\n"
    "v_mul_f32 v119, v119, v230\n"
    "v_mfma_f32_16x16x32_bf16 v[0:3], v[152:155], a[160:163], v[0:3]\n"
    "ds_read_b128 a[172:175], v237 offset:22528\n"
    "v_cvt_pk_bf16_f32 v120, v112, v113\n"
    "v_mfma_f32_16x16x32_bf16 v[0:3], v[156:159], a[176:179], v[0:3]\n"
    "ds_read_b128 a[188:191], v237 offset:23552\n"
    "v_cvt_pk_bf16_f32 v121, v114, v115\n"
    "v_mfma_f32_16x16x32_bf16 v[0:3], v[160:163], a[192:195], v[0:3]\n"
    "ds_read_b128 a[204:207], v237 offset:14336\n"
    "v_cvt_pk_bf16_f32 v122, v116, v117\n"
    "v_mfma_f32_16x16x32_bf16 v[0:3], v[164:167], a[208:211], v[0:3]\n"
    "ds_read_b128 a[220:223], v237 offset:15360\n"
    "v_cvt_pk_bf16_f32 v123, v118, v119\n"
    "v_mfma_f32_16x16x32_bf16 v[0:3], v[168:171], a[224:227], v[0:3]\n"
    "ds_read_b128 a[236:239], v237 offset:30720\n"
    "ds_write_b128 v233, v[120:123] offset:33792\n"
    "v_accvgpr_read_b32 v128, a84\n"
    "v_accvgpr_read_b32 v129, a85\n"
    "v_accvgpr_read_b32 v130, a86\n"
    "v_accvgpr_read_b32 v131, a87\n"
    "v_mfma_f32_16x16x32_bf16 v[0:3], v[172:175], a[240:243], v[0:3]\n"
    "ds_read_b128 a[252:255], v237 offset:31744\n"
    "ds_read_b64_tr_b16 v[176:177], v234 offset:0\n"
    "ds_read_b64_tr_b16 v[178:179], v234 offset:2048\n"
    "ds_read_b64_tr_b16 v[180:181], v234 offset:4096\n"
    "ds_read_b64_tr_b16 v[182:183], v234 offset:6144\n"
    "v_accvgpr_read_b32 v132, a92\n"
    "v_accvgpr_read_b32 v133, a93\n"
    "v_accvgpr_read_b32 v134, a94\n"
    "v_accvgpr_read_b32 v135, a95\n"
    "v_mul_f32 v128, v128, v231\n"
    "v_mul_f32 v129, v129, v231\n"
    "v_mul_f32 v130, v130, v231\n"
    "v_mul_f32 v131, v131, v231\n"
    "v_mfma_f32_16x16x32_bf16 v[4:7], v[144:147], a[132:135], 0\n"
    "ds_read_b64_tr_b16 v[184:185], v234 offset:8192\n"
    "v_mul_f32 v132, v132, v231\n"
    "v_mul_f32 v133, v133, v231\n"
    "v_mul_f32 v134, v134, v231\n"
    "v_mul_f32 v135, v135, v231\n"
    "v_cvt_pk_bf16_f32 v136, v128, v129\n"
    "v_cvt_pk_bf16_f32 v137, v130, v131\n"
    "v_cvt_pk_bf16_f32 v138, v132, v133\n"
    "v_cvt_pk_bf16_f32 v139, v134, v135\n"
    "v_mfma_f32_16x16x32_bf16 v[4:7], v[148:151], a[148:151], v[4:7]\n"
    "ds_read_b64_tr_b16 v[186:187], v234 offset:10240\n"
    "ds_write_b128 v233, v[136:139] offset:35840\n"
    "v_accvgpr_read_b32 v112, a96\n"
    "v_accvgpr_read_b32 v113, a97\n"
    "v_accvgpr_read_b32 v114, a98\n"
    "v_accvgpr_read_b32 v115, a99\n"
    "v_mfma_f32_16x16x32_bf16 v[4:7], v[152:155], a[164:167], v[4:7]\n"
    "ds_read_b64_tr_b16 v[188:189], v234 offset:12288\n"
    "v_accvgpr_read_b32 v116, a104\n"
    "v_accvgpr_read_b32 v117, a105\n"
    "v_accvgpr_read_b32 v118, a106\n"
    "v_accvgpr_read_b32 v119, a107\n"
    "v_mul_f32 v112, v112, v230\n"
    "v_mul_f32 v113, v113, v230\n"
    "v_mul_f32 v114, v114, v230\n"
    "v_mul_f32 v115, v115, v230\n"
    "v_mfma_f32_16x16x32_bf16 v[4:7], v[156:159], a[180:183], v[4:7]\n"
    "ds_read_b64_tr_b16 v[190:191], v234 offset:14336\n"
    "v_mul_f32 v116, v116, v230\n"
    "v_mul_f32 v117, v117, v230\n"
    "v_mul_f32 v118, v118, v230\n"
    "v_mul_f32 v119, v119, v230\n"
    "v_cvt_pk_bf16_f32 v120, v112, v113\n"
    "v_cvt_pk_bf16_f32 v121, v114, v115\n"
    "v_cvt_pk_bf16_f32 v122, v116, v117\n"
    "v_cvt_pk_bf16_f32 v123, v118, v119\n"
    "v_mfma_f32_16x16x32_bf16 v[4:7], v[160:163], a[196:199], v[4:7]\n"
    "ds_write_b128 v233, v[120:123] offset:49152\n"
    "v_mfma_f32_16x16x32_bf16 v[4:7], v[164:167], a[212:215], v[4:7]\n"
    "v_mfma_f32_16x16x32_bf16 v[4:7], v[168:171], a[228:231], v[4:7]\n"
    "s_waitcnt vmcnt(16)\n"
    "v_mfma_f32_16x16x32_bf16 v[4:7], v[172:175], a[244:247], v[4:7]\n"
    "s_waitcnt lgkmcnt(0)\n"
    "s_barrier\n"
    "v_mfma_f32_16x16x32_bf16 v[8:11], v[144:147], a[136:139], 0\n"
    "s_add_u32 m0, s59, 0\n"
    "v_accvgpr_read_b32 v128, a100\n"
    "v_accvgpr_read_b32 v129, a101\n"
    "v_accvgpr_read_b32 v130, a102\n"
    "v_accvgpr_read_b32 v131, a103\n"
    "v_accvgpr_read_b32 v132, a108\n"
    "v_accvgpr_read_b32 v133, a109\n"
    "v_accvgpr_read_b32 v134, a110\n"
    "s_add_u32 s62, s84, 256\n"
    "v_mfma_f32_16x16x32_bf16 v[8:11], v[148:151], a[152:155], v[8:11]\n"
    "buffer_load_dwordx4 v252, s[72:75], s62 offen offset:0 lds\n"
    "v_accvgpr_read_b32 v135, a111\n"
    "v_mul_f32 v128, v128, v231\n"
    "v_mul_f32 v129, v129, v231\n"
    "v_mul_f32 v130, v130, v231\n"
    "v_mul_f32 v131, v131, v231\n"
    "v_mfma_f32_16x16x32_bf16 v[8:11], v[152:155], a[168:171], v[8:11]\n"
    "ds_read_b64_tr_b16 v[192:193], v234 offset:16384\n"
    "v_mul_f32 v132, v132, v231\n"
    "v_mul_f32 v133, v133, v231\n"
    "v_mul_f32 v134, v134, v231\n"
    "v_mul_f32 v135, v135, v231\n"
    "v_cvt_pk_bf16_f32 v136, v128, v129\n"
    "v_cvt_pk_bf16_f32 v137, v130, v131\n"
    "v_mfma_f32_16x16x32_bf16 v[8:11], v[156:159], a[184:187], v[8:11]\n"
    "ds_read_b64_tr_b16 v[194:195], v234 offset:18432\n"
    "v_cvt_pk_bf16_f32 v138, v132, v133\n"
    "v_mfma_f32_16x16x32_bf16 v[8:11], v[160:163], a[200:203], v[8:11]\n"
    "ds_read_b64_tr_b16 v[196:197], v234 offset:20480\n"
    "v_cvt_pk_bf16_f32 v139, v134, v135\n"
    "v_mfma_f32_16x16x32_bf16 v[8:11], v[164:167], a[216:219], v[8:11]\n"
    "ds_write_b128 v233, v[136:139] offset:51200\n"
    "buffer_load_dwordx4 v253, s[72:75], s62 offen offset:1024 lds\n"
    "v_accvgpr_read_b32 v112, a112\n"
    "v_accvgpr_read_b32 v113, a113\n"
    "v_accvgpr_read_b32 v114, a114\n"
    "v_mfma_f32_16x16x32_bf16 v[8:11], v[168:171], a[232:235], v[8:11]\n"
    "ds_read_b64_tr_b16 v[198:199], v234 offset:22528\n"
    "v_accvgpr_read_b32 v115, a115\n"
    "v_accvgpr_read_b32 v116, a120\n"
    "v_accvgpr_read_b32 v117, a121\n"
    "v_accvgpr_read_b32 v118, a122\n"
    "v_accvgpr_read_b32 v119, a123\n"
    "v_mul_f32 v112, v112, v230\n"
    "v_mul_f32 v113, v113, v230\n"
    "v_mul_f32 v114, v114, v230\n"
    "v_mfma_f32_16x16x32_bf16 v[8:11], v[172:175], a[248:251], v[8:11]\n"
    "ds_read_b64_tr_b16 v[200:201], v234 offset:24576\n"
    "v_mul_f32 v115, v115, v230\n"
    "v_mul_f32 v116, v116, v230\n"
    "v_mul_f32 v117, v117, v230\n"
    "v_mul_f32 v118, v118, v230\n"
    "v_mul_f32 v119, v119, v230\n"
    "v_cvt_pk_bf16_f32 v120, v112, v113\n"
    "v_cvt_pk_bf16_f32 v121, v114, v115\n"
    "v_cvt_pk_bf16_f32 v122, v116, v117\n"
    "v_mfma_f32_16x16x32_bf16 v[12:15], v[144:147], a[140:143], 0\n"
    "ds_read_b64_tr_b16 v[202:203], v234 offset:26624\n"
    "v_cvt_pk_bf16_f32 v123, v118, v119\n"
    "v_mfma_f32_16x16x32_bf16 v[12:15], v[148:151], a[156:159], v[12:15]\n"
    "ds_write_b128 v233, v[120:123] offset:50176\n"
    "buffer_load_dwordx4 v254, s[72:75], s62 offen offset:2048 lds\n"
    "v_accvgpr_read_b32 v128, a116\n"
    "v_accvgpr_read_b32 v129, a117\n"
    "v_accvgpr_read_b32 v130, a118\n"
    "v_accvgpr_read_b32 v131, a119\n"
    "v_mfma_f32_16x16x32_bf16 v[12:15], v[152:155], a[172:175], v[12:15]\n"
    "ds_read_b64_tr_b16 v[204:205], v234 offset:28672\n"
    "v_accvgpr_read_b32 v132, a124\n"
    "v_accvgpr_read_b32 v133, a125\n"
    "v_accvgpr_read_b32 v134, a126\n"
    "v_accvgpr_read_b32 v135, a127\n"
    "v_mul_f32 v128, v128, v231\n"
    "v_mul_f32 v129, v129, v231\n"
    "v_mul_f32 v130, v130, v231\n"
    "v_mul_f32 v131, v131, v231\n"
    "v_mfma_f32_16x16x32_bf16 v[12:15], v[156:159], a[188:191], v[12:15]\n"
    "ds_read_b64_tr_b16 v[206:207], v234 offset:30720\n"
    "v_mul_f32 v132, v132, v231\n"
    "v_mul_f32 v133, v133, v231\n"
    "v_mul_f32 v134, v134, v231\n"
    "v_mul_f32 v135, v135, v231\n"
    "v_cvt_pk_bf16_f32 v136, v128, v129\n"
    "v_cvt_pk_bf16_f32 v137, v130, v131\n"
    "v_cvt_pk_bf16_f32 v138, v132, v133\n"
    "v_cvt_pk_bf16_f32 v139, v134, v135\n"
    "v_mfma_f32_16x16x32_bf16 v[12:15], v[160:163], a[204:207], v[12:15]\n"
    "ds_write_b128 v233, v[136:139] offset:52224\n"
    "v_mfma_f32_16x16x32_bf16 v[12:15], v[164:167], a[220:223], v[12:15]\n"
    "buffer_load_dwordx4 v255, s[72:75], s62 offen offset:3072 lds\n"
    "v_mfma_f32_16x16x32_bf16 v[12:15], v[168:171], a[236:239], v[12:15]\n"
    "s_waitcnt vmcnt(16)\n"
    "v_mfma_f32_16x16x32_bf16 v[12:15], v[172:175], a[252:255], v[12:15]\n"
    "s_waitcnt lgkmcnt(0)\n"
    "s_barrier\n"
    "v_mfma_f32_16x16x32_bf16 v[16:19], v[176:179], a[128:131], 0\n"
    "s_add_u32 m0, s59, 16384\n"
    "ds_read_b128 a[0:3], v237 offset:32768\n"
    "s_add_u32 s62, s84, 114944\n"
    "ds_read_b128 a[16:19], v237 offset:33792\n"
    "v_lshlrev_b32 v212, 16, v212\n"
    "v_mfma_f32_16x16x32_bf16 v[16:19], v[180:183], a[144:147], v[16:19]\n"
    "ds_read_b128 a[32:35], v237 offset:49152\n"
    "buffer_load_dwordx4 v252, s[72:75], s62 offen offset:0 lds\n"
    "ds_read_b128 a[48:51], v237 offset:50176\n"
    "v_lshlrev_b32 v213, 16, v213\n"
    "v_mfma_f32_16x16x32_bf16 v[16:19], v[184:187], a[160:163], v[16:19]\n"
    "ds_read_b128 a[64:67], v237 offset:40960\n"
    "v_lshlrev_b32 v214, 16, v214\n"
    "v_mfma_f32_16x16x32_bf16 v[16:19], v[188:191], a[176:179], v[16:19]\n"
    "ds_read_b128 a[80:83], v237 offset:41984\n"
    "v_lshlrev_b32 v215, 16, v215\n"
    "v_mfma_f32_16x16x32_bf16 v[16:19], v[192:195], a[192:195], v[16:19]\n"
    "ds_read_b128 a[96:99], v237 offset:57344\n"
    "v_lshlrev_b32 v220, 16, v220\n"
    "v_mfma_f32_16x16x32_bf16 v[16:19], v[196:199], a[208:211], v[16:19]\n"
    "ds_read_b128 a[112:115], v237 offset:58368\n"
    "buffer_load_dwordx4 v253, s[72:75], s62 offen offset:1024 lds\n"
    "ds_read_b128 a[4:7], v237 offset:34816\n"
    "v_lshlrev_b32 v221, 16, v221\n"
    "v_mfma_f32_16x16x32_bf16 v[16:19], v[200:203], a[224:227], v[16:19]\n"
    "ds_read_b128 a[20:23], v237 offset:35840\n"
    "v_lshlrev_b32 v222, 16, v222\n"
    "v_mfma_f32_16x16x32_bf16 v[16:19], v[204:207], a[240:243], v[16:19]\n"
    "ds_read_b64_tr_b16 v[144:145], v234 offset:32768\n"
    "ds_read_b64_tr_b16 v[146:147], v234 offset:34816\n"
    "ds_read_b64_tr_b16 v[148:149], v234 offset:36864\n"
    "v_mul_f32 v96, 0xbfb8aa3b, v212\n"
    "v_mfma_f32_16x16x32_bf16 v[20:23], v[176:179], a[132:135], 0\n"
    "ds_read_b64_tr_b16 v[150:151], v234 offset:38912\n"
    "v_mul_f32 v97, 0xbfb8aa3b, v213\n"
    "v_mfma_f32_16x16x32_bf16 v[20:23], v[180:183], a[148:151], v[20:23]\n"
    "ds_read_b64_tr_b16 v[152:153], v234 offset:40960\n"
    "buffer_load_dwordx4 v254, s[72:75], s62 offen offset:2048 lds\n"
    "ds_read_b64_tr_b16 v[154:155], v234 offset:43008\n"
    "v_mfma_f32_16x16x32_bf16 v[20:23], v[184:187], a[164:167], v[20:23]\n"
    "ds_read_b64_tr_b16 v[156:157], v234 offset:45056\n"
    "v_mul_f32 v98, 0xbfb8aa3b, v214\n"
    "v_mfma_f32_16x16x32_bf16 v[20:23], v[188:191], a[180:183], v[20:23]\n"
    "ds_read_b64_tr_b16 v[158:159], v234 offset:47104\n"
    "v_mul_f32 v99, 0xbfb8aa3b, v215\n"
    "v_mfma_f32_16x16x32_bf16 v[20:23], v[192:195], a[196:199], v[20:23]\n"
    "v_mul_f32 v100, 0xbfb8aa3b, v220\n"
    "v_mul_f32 v101, 0xbfb8aa3b, v221\n"
    "v_mfma_f32_16x16x32_bf16 v[20:23], v[196:199], a[212:215], v[20:23]\n"
    "buffer_load_dwordx4 v255, s[72:75], s62 offen offset:3072 lds\n"
    "v_mfma_f32_16x16x32_bf16 v[20:23], v[200:203], a[228:231], v[20:23]\n"
    "v_mul_f32 v102, 0xbfb8aa3b, v222\n"
    "s_waitcnt vmcnt(16)\n"
    "v_mfma_f32_16x16x32_bf16 v[20:23], v[204:207], a[244:247], v[20:23]\n"
    "s_waitcnt lgkmcnt(0)\n"
    "s_barrier\n"
    "v_mfma_f32_16x16x32_bf16 v[24:27], v[176:179], a[136:139], 0\n"
    "s_add_u32 m0, s59, 32768\n"
    "ds_read_b128 a[36:39], v237 offset:51200\n"
    "s_add_u32 s62, s84, 229632\n"
    "ds_read_b128 a[52:55], v237 offset:52224\n"
    "v_exp_f32 v96, v96\n"
    "v_exp_f32 v97, v97\n"
    "v_exp_f32 v98, v98\n"
    "v_mfma_f32_16x16x32_bf16 v[24:27], v[180:183], a[152:155], v[24:27]\n"
    "ds_read_b128 a[68:71], v237 offset:43008\n"
    "buffer_load_dwordx4 v252, s[72:75], s62 offen offset:0 lds\n"
    "ds_read_b128 a[84:87], v237 offset:44032\n"
    "v_exp_f32 v99, v99\n"
    "v_exp_f32 v100, v100\n"
    "v_mfma_f32_16x16x32_bf16 v[24:27], v[184:187], a[168:171], v[24:27]\n"
    "ds_read_b128 a[100:103], v237 offset:59392\n"
    "v_mfma_f32_16x16x32_bf16 v[24:27], v[188:191], a[184:187], v[24:27]\n"
    "ds_read_b128 a[116:119], v237 offset:60416\n"
    "v_mfma_f32_16x16x32_bf16 v[24:27], v[192:195], a[200:203], v[24:27]\n"
    "ds_read_b128 a[8:11], v237 offset:36864\n"
    "v_mfma_f32_16x16x32_bf16 v[24:27], v[196:199], a[216:219], v[24:27]\n"
    "ds_read_b128 a[24:27], v237 offset:37888\n"
    "buffer_load_dwordx4 v253, s[72:75], s62 offen offset:1024 lds\n"
    "ds_read_b128 a[40:43], v237 offset:53248\n"
    "v_exp_f32 v101, v101\n"
    "v_exp_f32 v102, v102\n"
    "v_mfma_f32_16x16x32_bf16 v[24:27], v[200:203], a[232:235], v[24:27]\n"
    "ds_read_b128 a[56:59], v237 offset:54272\n"
    "v_add_f32 v96, 1.0, v96\n"
    "v_mfma_f32_16x16x32_bf16 v[24:27], v[204:207], a[248:251], v[24:27]\n"
    "ds_read_b64_tr_b16 v[160:161], v234 offset:49152\n"
    "ds_read_b64_tr_b16 v[162:163], v234 offset:51200\n"
    "ds_read_b64_tr_b16 v[164:165], v234 offset:53248\n"
    "v_mfma_f32_16x16x32_bf16 v[28:31], v[176:179], a[140:143], 0\n"
    "ds_read_b64_tr_b16 v[166:167], v234 offset:55296\n"
    "v_add_f32 v97, 1.0, v97\n"
    "v_mfma_f32_16x16x32_bf16 v[28:31], v[180:183], a[156:159], v[28:31]\n"
    "ds_read_b64_tr_b16 v[168:169], v234 offset:57344\n"
    "buffer_load_dwordx4 v254, s[72:75], s62 offen offset:2048 lds\n"
    "ds_read_b64_tr_b16 v[170:171], v234 offset:59392\n"
    "v_mfma_f32_16x16x32_bf16 v[28:31], v[184:187], a[172:175], v[28:31]\n"
    "ds_read_b64_tr_b16 v[172:173], v234 offset:61440\n"
    "v_add_f32 v98, 1.0, v98\n"
    "v_mfma_f32_16x16x32_bf16 v[28:31], v[188:191], a[188:191], v[28:31]\n"
    "ds_read_b64_tr_b16 v[174:175], v234 offset:63488\n"
    "v_add_f32 v99, 1.0, v99\n"
    "v_mfma_f32_16x16x32_bf16 v[28:31], v[192:195], a[204:207], v[28:31]\n"
    "v_add_f32 v100, 1.0, v100\n"
    "v_add_f32 v101, 1.0, v101\n"
    "v_mfma_f32_16x16x32_bf16 v[28:31], v[196:199], a[220:223], v[28:31]\n"
    "buffer_load_dwordx4 v255, s[72:75], s62 offen offset:3072 lds\n"
    "v_mfma_f32_16x16x32_bf16 v[28:31], v[200:203], a[236:239], v[28:31]\n"
    "v_add_f32 v102, 1.0, v102\n"
    "s_waitcnt vmcnt(16)\n"
    "v_mfma_f32_16x16x32_bf16 v[28:31], v[204:207], a[252:255], v[28:31]\n"
    "s_waitcnt lgkmcnt(0)\n"
    "s_barrier\n"
    "v_mfma_f32_16x16x32_bf16 v[0:3], v[144:147], a[0:3], v[0:3]\n"
    "s_add_u32 m0, s59, 49152\n"
    "ds_read_b128 a[72:75], v237 offset:45056\n"
    "s_add_u32 s62, s84, 344320\n"
    "ds_read_b128 a[88:91], v237 offset:46080\n"
    "ds_read_b128 a[104:107], v237 offset:61440\n"
    "v_rcp_f32 v96, v96\n"
    "v_rcp_f32 v97, v97\n"
    "v_rcp_f32 v98, v98\n"
    "v_mfma_f32_16x16x32_bf16 v[0:3], v[148:151], a[16:19], v[0:3]\n"
    "ds_read_b128 a[120:123], v237 offset:62464\n"
    "buffer_load_dwordx4 v252, s[72:75], s62 offen offset:0 lds\n"
    "ds_read_b128 a[12:15], v237 offset:38912\n"
    "v_rcp_f32 v99, v99\n"
    "v_rcp_f32 v100, v100\n"
    "v_mfma_f32_16x16x32_bf16 v[0:3], v[152:155], a[32:35], v[0:3]\n"
    "ds_read_b128 a[28:31], v237 offset:39936\n"
    "v_mfma_f32_16x16x32_bf16 v[0:3], v[156:159], a[48:51], v[0:3]\n"
    "ds_read_b128 a[44:47], v237 offset:55296\n"
    "v_mfma_f32_16x16x32_bf16 v[0:3], v[160:163], a[64:67], v[0:3]\n"
    "ds_read_b128 a[60:63], v237 offset:56320\n"
    "v_mfma_f32_16x16x32_bf16 v[0:3], v[164:167], a[80:83], v[0:3]\n"
    "ds_read_b128 a[76:79], v237 offset:47104\n"
    "buffer_load_dwordx4 v253, s[72:75], s62 offen offset:1024 lds\n"
    "ds_read_b128 a[92:95], v237 offset:48128\n"
    "v_rcp_f32 v101, v101\n"
    "v_rcp_f32 v102, v102\n"
    "v_mfma_f32_16x16x32_bf16 v[0:3], v[168:171], a[96:99], v[0:3]\n"
    "ds_read_b128 a[108:111], v237 offset:63488\n"
    "v_mul_f32 v212, v212, v96\n"
    "v_mfma_f32_16x16x32_bf16 v[0:3], v[172:175], a[112:115], v[0:3]\n"
    "ds_read_b128 a[124:127], v237 offset:64512\n"
    "ds_read_b64_tr_b16 v[176:177], v236 offset:0\n"
    "ds_read_b64_tr_b16 v[178:179], v236 offset:2048\n"
    "ds_read_b64_tr_b16 v[180:181], v236 offset:4096\n"
    "v_mfma_f32_16x16x32_bf16 v[4:7], v[144:147], a[4:7], v[4:7]\n"
    "ds_read_b64_tr_b16 v[182:183], v236 offset:6144\n"
    "v_mul_f32 v213, v213, v97\n"
    "v_mfma_f32_16x16x32_bf16 v[4:7], v[148:151], a[20:23], v[4:7]\n"
    "ds_read_b64_tr_b16 v[184:185], v236 offset:8192\n"
    "buffer_load_dwordx4 v254, s[72:75], s62 offen offset:2048 lds\n"
    "ds_read_b64_tr_b16 v[186:187], v236 offset:10240\n"
    "v_mfma_f32_16x16x32_bf16 v[4:7], v[152:155], a[36:39], v[4:7]\n"
    "ds_read_b64_tr_b16 v[188:189], v236 offset:12288\n"
    "v_mul_f32 v214, v214, v98\n"
    "v_mfma_f32_16x16x32_bf16 v[4:7], v[156:159], a[52:55], v[4:7]\n"
    "ds_read_b64_tr_b16 v[190:191], v236 offset:14336\n"
    "v_mul_f32 v215, v215, v99\n"
    "v_mfma_f32_16x16x32_bf16 v[4:7], v[160:163], a[68:71], v[4:7]\n"
    "v_mul_f32 v220, v220, v100\n"
    "v_mul_f32 v221, v221, v101\n"
    "v_mfma_f32_16x16x32_bf16 v[4:7], v[164:167], a[84:87], v[4:7]\n"
    "buffer_load_dwordx4 v255, s[72:75], s62 offen offset:3072 lds\n"
    "v_mfma_f32_16x16x32_bf16 v[4:7], v[168:171], a[100:103], v[4:7]\n"
    "v_mul_f32 v222, v222, v102\n"
    "s_waitcnt vmcnt(16)\n"
    "v_mfma_f32_16x16x32_bf16 v[4:7], v[172:175], a[116:119], v[4:7]\n"
    "s_waitcnt lgkmcnt(0)\n"
    "s_barrier\n"
    "v_mfma_f32_16x16x32_bf16 v[8:11], v[144:147], a[8:11], v[8:11]\n"
    "s_add_u32 m0, s59, 131072\n"
    "v_cvt_pk_bf16_f32 v212, 0, v212\n"
    "s_add_u32 s62, s84, 384\n"
    "v_mfma_f32_16x16x32_bf16 v[8:11], v[148:151], a[24:27], v[8:11]\n"
    "buffer_load_dwordx4 v252, s[72:75], s62 offen offset:0 lds\n"
    "v_mfma_f32_16x16x32_bf16 v[8:11], v[152:155], a[40:43], v[8:11]\n"
    "ds_read_b64_tr_b16 v[192:193], v236 offset:16384\n"
    "v_cvt_pk_bf16_f32 v213, 0, v213\n"
    "v_mfma_f32_16x16x32_bf16 v[8:11], v[156:159], a[56:59], v[8:11]\n"
    "ds_read_b64_tr_b16 v[194:195], v236 offset:18432\n"
    "v_cvt_pk_bf16_f32 v214, 0, v214\n"
    "v_mfma_f32_16x16x32_bf16 v[8:11], v[160:163], a[72:75], v[8:11]\n"
    "ds_read_b64_tr_b16 v[196:197], v236 offset:20480\n"
    "v_cvt_pk_bf16_f32 v215, 0, v215\n"
    "v_mfma_f32_16x16x32_bf16 v[8:11], v[164:167], a[88:91], v[8:11]\n"
    "buffer_load_dwordx4 v253, s[72:75], s62 offen offset:1024 lds\n"
    "v_mfma_f32_16x16x32_bf16 v[8:11], v[168:171], a[104:107], v[8:11]\n"
    "ds_read_b64_tr_b16 v[198:199], v236 offset:22528\n"
    "v_cvt_pk_bf16_f32 v220, 0, v220\n"
    "v_mfma_f32_16x16x32_bf16 v[8:11], v[172:175], a[120:123], v[8:11]\n"
    "ds_read_b64_tr_b16 v[200:201], v236 offset:24576\n"
    "v_cvt_pk_bf16_f32 v221, 0, v221\n"
    "v_cvt_pk_bf16_f32 v222, 0, v222\n"
    "v_lshlrev_b32 v96, 16, v48\n"
    "v_mfma_f32_16x16x32_bf16 v[12:15], v[144:147], a[12:15], v[12:15]\n"
    "ds_read_b64_tr_b16 v[202:203], v236 offset:26624\n"
    "v_and_b32 v97, 0xffff0000, v48\n"
    "v_mfma_f32_16x16x32_bf16 v[12:15], v[148:151], a[28:31], v[12:15]\n"
    "buffer_load_dwordx4 v254, s[72:75], s62 offen offset:2048 lds\n"
    "v_mfma_f32_16x16x32_bf16 v[12:15], v[152:155], a[44:47], v[12:15]\n"
    "ds_read_b64_tr_b16 v[204:205], v236 offset:28672\n"
    "v_lshlrev_b32 v98, 16, v49\n"
    "v_mfma_f32_16x16x32_bf16 v[12:15], v[156:159], a[60:63], v[12:15]\n"
    "ds_read_b64_tr_b16 v[206:207], v236 offset:30720\n"
    "v_and_b32 v99, 0xffff0000, v49\n"
    "v_mfma_f32_16x16x32_bf16 v[12:15], v[160:163], a[76:79], v[12:15]\n"
    "v_lshlrev_b32 v100, 16, v50\n"
    "v_and_b32 v101, 0xffff0000, v50\n"
    "v_mfma_f32_16x16x32_bf16 v[12:15], v[164:167], a[92:95], v[12:15]\n"
    "buffer_load_dwordx4 v255, s[72:75], s62 offen offset:3072 lds\n"
    "v_mfma_f32_16x16x32_bf16 v[12:15], v[168:171], a[108:111], v[12:15]\n"
    "v_lshlrev_b32 v102, 16, v51\n"
    "s_waitcnt vmcnt(16)\n"
    "v_mfma_f32_16x16x32_bf16 v[12:15], v[172:175], a[124:127], v[12:15]\n"
    "s_waitcnt lgkmcnt(0)\n"
    "s_barrier\n"
    "v_mfma_f32_16x16x32_bf16 v[16:19], v[176:179], a[0:3], v[16:19]\n"
    "s_add_u32 m0, s59, 147456\n"
    "v_and_b32 v103, 0xffff0000, v51\n"
    "v_mul_f32 v208, v96, v212\n"
    "v_mul_f32 v209, v97, v212\n"
    "v_mul_f32 v210, v98, v212\n"
    "s_add_u32 s62, s84, 115072\n"
    "v_mfma_f32_16x16x32_bf16 v[16:19], v[180:183], a[16:19], v[16:19]\n"
    "buffer_load_dwordx4 v252, s[72:75], s62 offen offset:0 lds\n"
    "v_mul_f32 v211, v99, v212\n"
    "v_mul_f32 v216, v100, v212\n"
    "v_mul_f32 v217, v101, v212\n"
    "v_mfma_f32_16x16x32_bf16 v[16:19], v[184:187], a[32:35], v[16:19]\n"
    "ds_read_b64_tr_b16 v[144:145], v234 offset:0\n"
    "v_cvt_pk_bf16_f32 v112, v0, v1\n"
    "v_mfma_f32_16x16x32_bf16 v[16:19], v[188:191], a[48:51], v[16:19]\n"
    "ds_read_b64_tr_b16 v[146:147], v234 offset:2048\n"
    "v_cvt_pk_bf16_f32 v113, v2, v3\n"
    "v_mfma_f32_16x16x32_bf16 v[16:19], v[192:195], a[64:67], v[16:19]\n"
    "ds_read_b64_tr_b16 v[148:149], v234 offset:4096\n"
    "v_cvt_pk_bf16_f32 v116, v4, v5\n"
    "v_mfma_f32_16x16x32_bf16 v[16:19], v[196:199], a[80:83], v[16:19]\n"
    "buffer_load_dwordx4 v253, s[72:75], s62 offen offset:1024 lds\n"
    "v_mfma_f32_16x16x32_bf16 v[16:19], v[200:203], a[96:99], v[16:19]\n"
    "ds_read_b64_tr_b16 v[150:151], v234 offset:6144\n"
    "v_cvt_pk_bf16_f32 v117, v6, v7\n"
    "v_mfma_f32_16x16x32_bf16 v[16:19], v[204:207], a[112:115], v[16:19]\n"
    "ds_read_b64_tr_b16 v[152:153], v234 offset:8192\n"
    "v_mul_f32 v218, v102, v212\n"
    "v_mul_f32 v219, v103, v212\n"
    "v_lshlrev_b32 v96, 16, v52\n"
    "v_and_b32 v97, 0xffff0000, v52\n"
    "v_cvt_pk_bf16_f32 v120, v8, v9\n"
    "v_mfma_f32_16x16x32_bf16 v[20:23], v[176:179], a[4:7], v[20:23]\n"
    "ds_read_b64_tr_b16 v[154:155], v234 offset:10240\n"
    "v_cvt_pk_bf16_f32 v121, v10, v11\n"
    "v_mfma_f32_16x16x32_bf16 v[20:23], v[180:183], a[20:23], v[20:23]\n"
    "buffer_load_dwordx4 v254, s[72:75], s62 offen offset:2048 lds\n"
    "v_mfma_f32_16x16x32_bf16 v[20:23], v[184:187], a[36:39], v[20:23]\n"
    "ds_read_b64_tr_b16 v[156:157], v234 offset:12288\n"
    "v_cvt_pk_bf16_f32 v124, v12, v13\n"
    "v_mfma_f32_16x16x32_bf16 v[20:23], v[188:191], a[52:55], v[20:23]\n"
    "ds_read_b64_tr_b16 v[158:159], v234 offset:14336\n"
    "v_cvt_pk_bf16_f32 v125, v14, v15\n"
    "v_mfma_f32_16x16x32_bf16 v[20:23], v[192:195], a[68:71], v[20:23]\n"
    "v_lshlrev_b32 v98, 16, v53\n"
    "v_and_b32 v99, 0xffff0000, v53\n"
    "v_mfma_f32_16x16x32_bf16 v[20:23], v[196:199], a[84:87], v[20:23]\n"
    "buffer_load_dwordx4 v255, s[72:75], s62 offen offset:3072 lds\n"
    "v_mfma_f32_16x16x32_bf16 v[20:23], v[200:203], a[100:103], v[20:23]\n"
    "v_lshlrev_b32 v100, 16, v54\n"
    "s_waitcnt vmcnt(16)\n"
    "v_mfma_f32_16x16x32_bf16 v[20:23], v[204:207], a[116:119], v[20:23]\n"
    "s_waitcnt lgkmcnt(0)\n"
    "s_barrier\n"
    "v_mfma_f32_16x16x32_bf16 v[24:27], v[176:179], a[8:11], v[24:27]\n"
    "s_add_u32 m0, s59, 0\n"
    "v_and_b32 v101, 0xffff0000, v54\n"
    "s_add_u32 s62, s84, 229760\n"
    "v_mfma_f32_16x16x32_bf16 v[24:27], v[180:183], a[24:27], v[24:27]\n"
    "buffer_load_dwordx4 v252, s[72:75], s62 offen offset:0 lds\n"
    "v_mfma_f32_16x16x32_bf16 v[24:27], v[184:187], a[40:43], v[24:27]\n"
    "ds_read_b64_tr_b16 v[160:161], v234 offset:16384\n"
    "v_lshlrev_b32 v102, 16, v55\n"
    "v_mfma_f32_16x16x32_bf16 v[24:27], v[188:191], a[56:59], v[24:27]\n"
    "ds_read_b64_tr_b16 v[162:163], v234 offset:18432\n"
    "v_and_b32 v103, 0xffff0000, v55\n"
    "v_mfma_f32_16x16x32_bf16 v[24:27], v[192:195], a[72:75], v[24:27]\n"
    "ds_read_b64_tr_b16 v[164:165], v234 offset:20480\n"
    "v_fmac_f32 v208, v96, v213\n"
    "v_mfma_f32_16x16x32_bf16 v[24:27], v[196:199], a[88:91], v[24:27]\n"
    "buffer_load_dwordx4 v253, s[72:75], s62 offen offset:1024 lds\n"
    "v_mfma_f32_16x16x32_bf16 v[24:27], v[200:203], a[104:107], v[24:27]\n"
    "ds_read_b64_tr_b16 v[166:167], v234 offset:22528\n"
    "v_fmac_f32 v209, v97, v213\n"
    "v_mfma_f32_16x16x32_bf16 v[24:27], v[204:207], a[120:123], v[24:27]\n"
    "ds_read_b64_tr_b16 v[168:169], v234 offset:24576\n"
    "v_fmac_f32 v210, v98, v213\n"
    "v_fmac_f32 v211, v99, v213\n"
    "v_fmac_f32 v216, v100, v213\n"
    "v_mfma_f32_16x16x32_bf16 v[28:31], v[176:179], a[12:15], v[28:31]\n"
    "ds_read_b64_tr_b16 v[170:171], v234 offset:26624\n"
    "v_fmac_f32 v217, v101, v213\n"
    "v_mfma_f32_16x16x32_bf16 v[28:31], v[180:183], a[28:31], v[28:31]\n"
    "buffer_load_dwordx4 v254, s[72:75], s62 offen offset:2048 lds\n"
    "v_mfma_f32_16x16x32_bf16 v[28:31], v[184:187], a[44:47], v[28:31]\n"
    "ds_read_b64_tr_b16 v[172:173], v234 offset:28672\n"
    "v_fmac_f32 v218, v102, v213\n"
    "v_mfma_f32_16x16x32_bf16 v[28:31], v[188:191], a[60:63], v[28:31]\n"
    "ds_read_b64_tr_b16 v[174:175], v234 offset:30720\n"
    "v_fmac_f32 v219, v103, v213\n"
    "v_mfma_f32_16x16x32_bf16 v[28:31], v[192:195], a[76:79], v[28:31]\n"
    "v_lshlrev_b32 v96, 16, v56\n"
    "v_and_b32 v97, 0xffff0000, v56\n"
    "v_mfma_f32_16x16x32_bf16 v[28:31], v[196:199], a[92:95], v[28:31]\n"
    "buffer_load_dwordx4 v255, s[72:75], s62 offen offset:3072 lds\n"
    "v_mfma_f32_16x16x32_bf16 v[28:31], v[200:203], a[108:111], v[28:31]\n"
    "v_lshlrev_b32 v98, 16, v57\n"
    "s_waitcnt vmcnt(16)\n"
    "v_mfma_f32_16x16x32_bf16 v[28:31], v[204:207], a[124:127], v[28:31]\n"
    "s_waitcnt lgkmcnt(0)\n"
    "s_barrier\n"
    "v_mfma_f32_16x16x32_bf16 v[32:35], v[144:147], a[128:131], 0\n"
    "s_add_u32 m0, s59, 16384\n"
    "v_and_b32 v99, 0xffff0000, v57\n"
    "v_lshlrev_b32 v100, 16, v58\n"
    "v_and_b32 v101, 0xffff0000, v58\n"
    "v_lshlrev_b32 v102, 16, v59\n"
    "s_add_u32 s62, s84, 344448\n"
    "v_mfma_f32_16x16x32_bf16 v[32:35], v[148:151], a[144:147], v[32:35]\n"
    "buffer_load_dwordx4 v252, s[72:75], s62 offen offset:0 lds\n"
    "v_and_b32 v103, 0xffff0000, v59\n"
    "v_fmac_f32 v208, v96, v214\n"
    "v_fmac_f32 v209, v97, v214\n"
    "v_mfma_f32_16x16x32_bf16 v[32:35], v[152:155], a[160:163], v[32:35]\n"
    "ds_read_b64_tr_b16 v[176:177], v234 offset:32768\n"
    "v_cvt_pk_bf16_f32 v114, v16, v17\n"
    "v_mfma_f32_16x16x32_bf16 v[32:35], v[156:159], a[176:179], v[32:35]\n"
    "ds_read_b64_tr_b16 v[178:179], v234 offset:34816\n"
    "v_cvt_pk_bf16_f32 v115, v18, v19\n"
    "v_mfma_f32_16x16x32_bf16 v[32:35], v[160:163], a[0:3], v[32:35]\n"
    "ds_read_b64_tr_b16 v[180:181], v234 offset:36864\n"
    "v_cvt_pk_bf16_f32 v118, v20, v21\n"
    "v_mfma_f32_16x16x32_bf16 v[32:35], v[164:167], a[16:19], v[32:35]\n"
    "buffer_load_dwordx4 v253, s[72:75], s62 offen offset:1024 lds\n"
    "v_mfma_f32_16x16x32_bf16 v[32:35], v[168:171], a[32:35], v[32:35]\n"
    "ds_read_b64_tr_b16 v[182:183], v234 offset:38912\n"
    "v_cvt_pk_bf16_f32 v119, v22, v23\n"
    "v_mfma_f32_16x16x32_bf16 v[32:35], v[172:175], a[48:51], v[32:35]\n"
    "ds_read_b64_tr_b16 v[184:185], v234 offset:40960\n"
    "v_fmac_f32 v210, v98, v214\n"
    "v_fmac_f32 v211, v99, v214\n"
    "v_fmac_f32 v216, v100, v214\n"
    "v_fmac_f32 v217, v101, v214\n"
    "v_cvt_pk_bf16_f32 v122, v24, v25\n"
    "v_mfma_f32_16x16x32_bf16 v[36:39], v[144:147], a[132:135], 0\n"
    "ds_read_b64_tr_b16 v[186:187], v234 offset:43008\n"
    "v_cvt_pk_bf16_f32 v123, v26, v27\n"
    "v_mfma_f32_16x16x32_bf16 v[36:39], v[148:151], a[148:151], v[36:39]\n"
    "buffer_load_dwordx4 v254, s[72:75], s62 offen offset:2048 lds\n"
    "v_mfma_f32_16x16x32_bf16 v[36:39], v[152:155], a[164:167], v[36:39]\n"
    "ds_read_b64_tr_b16 v[188:189], v234 offset:45056\n"
    "v_cvt_pk_bf16_f32 v126, v28, v29\n"
    "v_mfma_f32_16x16x32_bf16 v[36:39], v[156:159], a[180:183], v[36:39]\n"
    "ds_read_b64_tr_b16 v[190:191], v234 offset:47104\n"
    "v_cvt_pk_bf16_f32 v127, v30, v31\n"
    "v_mfma_f32_16x16x32_bf16 v[36:39], v[160:163], a[4:7], v[36:39]\n"
    "v_fmac_f32 v218, v102, v214\n"
    "v_fmac_f32 v219, v103, v214\n"
    "v_mfma_f32_16x16x32_bf16 v[36:39], v[164:167], a[20:23], v[36:39]\n"
    "buffer_load_dwordx4 v255, s[72:75], s62 offen offset:3072 lds\n"
    "v_mfma_f32_16x16x32_bf16 v[36:39], v[168:171], a[36:39], v[36:39]\n"
    "v_lshlrev_b32 v96, 16, v60\n"
    "s_waitcnt vmcnt(16)\n"
    "v_mfma_f32_16x16x32_bf16 v[36:39], v[172:175], a[52:55], v[36:39]\n"
    "s_waitcnt lgkmcnt(0)\n"
    "s_barrier\n"
    "v_mfma_f32_16x16x32_bf16 v[40:43], v[144:147], a[136:139], 0\n"
    "s_add_u32 m0, s59, 32768\n"
    "v_and_b32 v97, 0xffff0000, v60\n"
    "v_lshlrev_b32 v98, 16, v61\n"
    "s_add_u32 s62, s84, 512\n"
    "v_mfma_f32_16x16x32_bf16 v[40:43], v[148:151], a[152:155], v[40:43]\n"
    "buffer_load_dwordx4 v252, s[72:75], s62 offen offset:0 lds\n"
    "v_mfma_f32_16x16x32_bf16 v[40:43], v[152:155], a[168:171], v[40:43]\n"
    "ds_read_b64_tr_b16 v[192:193], v234 offset:49152\n"
    "v_and_b32 v99, 0xffff0000, v61\n"
    "v_mfma_f32_16x16x32_bf16 v[40:43], v[156:159], a[184:187], v[40:43]\n"
    "ds_read_b64_tr_b16 v[194:195], v234 offset:51200\n"
    "v_lshlrev_b32 v100, 16, v62\n"
    "v_mfma_f32_16x16x32_bf16 v[40:43], v[160:163], a[8:11], v[40:43]\n"
    "ds_read_b64_tr_b16 v[196:197], v234 offset:53248\n"
    "v_and_b32 v101, 0xffff0000, v62\n"
    "v_mfma_f32_16x16x32_bf16 v[40:43], v[164:167], a[24:27], v[40:43]\n"
    "buffer_load_dwordx4 v253, s[72:75], s62 offen offset:1024 lds\n"
    "v_mfma_f32_16x16x32_bf16 v[40:43], v[168:171], a[40:43], v[40:43]\n"
    "ds_read_b64_tr_b16 v[198:199], v234 offset:55296\n"
    "v_lshlrev_b32 v102, 16, v63\n"
    "v_mfma_f32_16x16x32_bf16 v[40:43], v[172:175], a[56:59], v[40:43]\n"
    "ds_read_b64_tr_b16 v[200:201], v234 offset:57344\n"
    "v_and_b32 v103, 0xffff0000, v63\n"
    "v_fmac_f32 v208, v96, v215\n"
    "v_fmac_f32 v209, v97, v215\n"
    "v_fmac_f32 v210, v98, v215\n"
    "v_mfma_f32_16x16x32_bf16 v[44:47], v[144:147], a[140:143], 0\n"
    "ds_read_b64_tr_b16 v[202:203], v234 offset:59392\n"
    "v_fmac_f32 v211, v99, v215\n"
    "v_mfma_f32_16x16x32_bf16 v[44:47], v[148:151], a[156:159], v[44:47]\n"
    "buffer_load_dwordx4 v254, s[72:75], s62 offen offset:2048 lds\n"
    "v_mfma_f32_16x16x32_bf16 v[44:47], v[152:155], a[172:175], v[44:47]\n"
    "ds_read_b64_tr_b16 v[204:205], v234 offset:61440\n"
    "v_fmac_f32 v216, v100, v215\n"
    "v_mfma_f32_16x16x32_bf16 v[44:47], v[156:159], a[188:191], v[44:47]\n"
    "ds_read_b64_tr_b16 v[206:207], v234 offset:63488\n"
    "v_fmac_f32 v217, v101, v215\n"
    "v_mfma_f32_16x16x32_bf16 v[44:47], v[160:163], a[12:15], v[44:47]\n"
    "buffer_store_dwordx4 v[112:115], v240, s[76:79], s96 offen offset:0 sc1\n"
    "v_mfma_f32_16x16x32_bf16 v[44:47], v[164:167], a[28:31], v[44:47]\n"
    "buffer_load_dwordx4 v255, s[72:75], s62 offen offset:3072 lds\n"
    "v_mfma_f32_16x16x32_bf16 v[44:47], v[168:171], a[44:47], v[44:47]\n"
    "v_fmac_f32 v218, v102, v215\n"
    "s_waitcnt vmcnt(17)\n"
    "v_mfma_f32_16x16x32_bf16 v[44:47], v[172:175], a[60:63], v[44:47]\n"
    "s_waitcnt lgkmcnt(0)\n"
    "s_barrier\n"
    "v_mfma_f32_16x16x32_bf16 v[32:35], v[176:179], a[192:195], v[32:35]\n"
    "s_add_u32 m0, s59, 49152\n"
    "v_fmac_f32 v219, v103, v215\n"
    "v_lshlrev_b32 v96, 16, v64\n"
    "s_add_u32 s62, s84, 115200\n"
    "v_mfma_f32_16x16x32_bf16 v[32:35], v[180:183], a[208:211], v[32:35]\n"
    "buffer_load_dwordx4 v252, s[72:75], s62 offen offset:0 lds\n"
    "v_mfma_f32_16x16x32_bf16 v[32:35], v[184:187], a[224:227], v[32:35]\n"
    "ds_read_b64_tr_b16 v[144:145], v236 offset:0\n"
    "v_and_b32 v97, 0xffff0000, v64\n"
    "v_mfma_f32_16x16x32_bf16 v[32:35], v[188:191], a[240:243], v[32:35]\n"
    "ds_read_b64_tr_b16 v[146:147], v236 offset:2048\n"
    "v_lshlrev_b32 v98, 16, v65\n"
    "v_mfma_f32_16x16x32_bf16 v[32:35], v[192:195], a[64:67], v[32:35]\n"
    "ds_read_b64_tr_b16 v[148:149], v236 offset:4096\n"
    "v_and_b32 v99, 0xffff0000, v65\n"
    "v_mfma_f32_16x16x32_bf16 v[32:35], v[196:199], a[80:83], v[32:35]\n"
    "buffer_load_dwordx4 v253, s[72:75], s62 offen offset:1024 lds\n"
    "v_mfma_f32_16x16x32_bf16 v[32:35], v[200:203], a[96:99], v[32:35]\n"
    "ds_read_b64_tr_b16 v[150:151], v236 offset:6144\n"
    "v_lshlrev_b32 v100, 16, v66\n"
    "v_mfma_f32_16x16x32_bf16 v[32:35], v[204:207], a[112:115], v[32:35]\n"
    "ds_read_b64_tr_b16 v[152:153], v236 offset:8192\n"
    "v_and_b32 v101, 0xffff0000, v66\n"
    "v_lshlrev_b32 v102, 16, v67\n"
    "v_and_b32 v103, 0xffff0000, v67\n"
    "v_fmac_f32 v208, v96, v220\n"
    "v_mfma_f32_16x16x32_bf16 v[36:39], v[176:179], a[196:199], v[36:39]\n"
    "ds_read_b64_tr_b16 v[154:155], v236 offset:10240\n"
    "v_fmac_f32 v209, v97, v220\n"
    "v_mfma_f32_16x16x32_bf16 v[36:39], v[180:183], a[212:215], v[36:39]\n"
    "buffer_load_dwordx4 v254, s[72:75], s62 offen offset:2048 lds\n"
    "v_mfma_f32_16x16x32_bf16 v[36:39], v[184:187], a[228:231], v[36:39]\n"
    "ds_read_b64_tr_b16 v[156:157], v236 offset:12288\n"
    "v_fmac_f32 v210, v98, v220\n"
    "v_mfma_f32_16x16x32_bf16 v[36:39], v[188:191], a[244:247], v[36:39]\n"
    "ds_read_b64_tr_b16 v[158:159], v236 offset:14336\n"
    "v_fmac_f32 v211, v99, v220\n"
    "v_mfma_f32_16x16x32_bf16 v[36:39], v[192:195], a[68:71], v[36:39]\n"
    "buffer_store_dwordx4 v[116:119], v240, s[76:79], s97 offen offset:0 sc1\n"
    "v_mfma_f32_16x16x32_bf16 v[36:39], v[196:199], a[84:87], v[36:39]\n"
    "buffer_load_dwordx4 v255, s[72:75], s62 offen offset:3072 lds\n"
    "v_mfma_f32_16x16x32_bf16 v[36:39], v[200:203], a[100:103], v[36:39]\n"
    "v_fmac_f32 v216, v100, v220\n"
    "s_waitcnt vmcnt(18)\n"
    "v_mfma_f32_16x16x32_bf16 v[36:39], v[204:207], a[116:119], v[36:39]\n"
    "s_waitcnt lgkmcnt(0)\n"
    "s_barrier\n"
    "v_mfma_f32_16x16x32_bf16 v[40:43], v[176:179], a[200:203], v[40:43]\n"
    "s_add_u32 m0, s59, 131072\n"
    "v_fmac_f32 v217, v101, v220\n"
    "v_fmac_f32 v218, v102, v220\n"
    "s_add_u32 s62, s84, 229888\n"
    "v_mfma_f32_16x16x32_bf16 v[40:43], v[180:183], a[216:219], v[40:43]\n"
    "buffer_load_dwordx4 v252, s[72:75], s62 offen offset:0 lds\n"
    "v_mfma_f32_16x16x32_bf16 v[40:43], v[184:187], a[232:235], v[40:43]\n"
    "ds_read_b64_tr_b16 v[160:161], v236 offset:16384\n"
    "v_fmac_f32 v219, v103, v220\n"
    "v_mfma_f32_16x16x32_bf16 v[40:43], v[188:191], a[248:251], v[40:43]\n"
    "ds_read_b64_tr_b16 v[162:163], v236 offset:18432\n"
    "v_lshlrev_b32 v96, 16, v68\n"
    "v_mfma_f32_16x16x32_bf16 v[40:43], v[192:195], a[72:75], v[40:43]\n"
    "ds_read_b64_tr_b16 v[164:165], v236 offset:20480\n"
    "v_and_b32 v97, 0xffff0000, v68\n"
    "v_mfma_f32_16x16x32_bf16 v[40:43], v[196:199], a[88:91], v[40:43]\n"
    "buffer_load_dwordx4 v253, s[72:75], s62 offen offset:1024 lds\n"
    "v_mfma_f32_16x16x32_bf16 v[40:43], v[200:203], a[104:107], v[40:43]\n"
    "ds_read_b64_tr_b16 v[166:167], v236 offset:22528\n"
    "v_lshlrev_b32 v98, 16, v69\n"
    "v_mfma_f32_16x16x32_bf16 v[40:43], v[204:207], a[120:123], v[40:43]\n"
    "ds_read_b64_tr_b16 v[168:169], v236 offset:24576\n"
    "v_and_b32 v99, 0xffff0000, v69\n"
    "v_lshlrev_b32 v100, 16, v70\n"
    "v_and_b32 v101, 0xffff0000, v70\n"
    "v_lshlrev_b32 v102, 16, v71\n"
    "v_mfma_f32_16x16x32_bf16 v[44:47], v[176:179], a[204:207], v[44:47]\n"
    "ds_read_b64_tr_b16 v[170:171], v236 offset:26624\n"
    "v_and_b32 v103, 0xffff0000, v71\n"
    "v_mfma_f32_16x16x32_bf16 v[44:47], v[180:183], a[220:223], v[44:47]\n"
    "buffer_load_dwordx4 v254, s[72:75], s62 offen offset:2048 lds\n"
    "v_mfma_f32_16x16x32_bf16 v[44:47], v[184:187], a[236:239], v[44:47]\n"
    "ds_read_b64_tr_b16 v[172:173], v236 offset:28672\n"
    "v_fmac_f32 v208, v96, v221\n"
    "v_mfma_f32_16x16x32_bf16 v[44:47], v[188:191], a[252:255], v[44:47]\n"
    "ds_read_b64_tr_b16 v[174:175], v236 offset:30720\n"
    "v_fmac_f32 v209, v97, v221\n"
    "v_mfma_f32_16x16x32_bf16 v[44:47], v[192:195], a[76:79], v[44:47]\n"
    "buffer_store_dwordx4 v[120:123], v240, s[76:79], s98 offen offset:0 sc1\n"
    "v_mfma_f32_16x16x32_bf16 v[44:47], v[196:199], a[92:95], v[44:47]\n"
    "buffer_load_dwordx4 v255, s[72:75], s62 offen offset:3072 lds\n"
    "v_mfma_f32_16x16x32_bf16 v[44:47], v[200:203], a[108:111], v[44:47]\n"
    "v_fmac_f32 v210, v98, v221\n"
    "s_waitcnt vmcnt(19)\n"
    "v_mfma_f32_16x16x32_bf16 v[44:47], v[204:207], a[124:127], v[44:47]\n"
    "s_waitcnt lgkmcnt(0)\n"
    "s_barrier\n"
    "v_mfma_f32_16x16x32_bf16 v[0:3], v[144:147], a[128:131], 0\n"
    "s_add_u32 m0, s59, 147456\n"
    "v_fmac_f32 v211, v99, v221\n"
    "v_fmac_f32 v216, v100, v221\n"
    "v_fmac_f32 v217, v101, v221\n"
    "v_fmac_f32 v218, v102, v221\n"
    "s_add_u32 s62, s84, 344576\n"
    "v_mfma_f32_16x16x32_bf16 v[0:3], v[148:151], a[144:147], v[0:3]\n"
    "buffer_load_dwordx4 v252, s[72:75], s62 offen offset:0 lds\n"
    "v_fmac_f32 v219, v103, v221\n"
    "v_lshlrev_b32 v96, 16, v72\n"
    "v_and_b32 v97, 0xffff0000, v72\n"
    "v_mfma_f32_16x16x32_bf16 v[0:3], v[152:155], a[160:163], v[0:3]\n"
    "ds_read_b64_tr_b16 v[176:177], v234 offset:0\n"
    "v_cvt_pk_bf16_f32 v128, v32, v33\n"
    "v_mfma_f32_16x16x32_bf16 v[0:3], v[156:159], a[176:179], v[0:3]\n"
    "ds_read_b64_tr_b16 v[178:179], v234 offset:2048\n"
    "v_cvt_pk_bf16_f32 v129, v34, v35\n"
    "v_mfma_f32_16x16x32_bf16 v[0:3], v[160:163], a[0:3], v[0:3]\n"
    "ds_read_b64_tr_b16 v[180:181], v234 offset:4096\n"
    "v_cvt_pk_bf16_f32 v132, v36, v37\n"
    "v_mfma_f32_16x16x32_bf16 v[0:3], v[164:167], a[16:19], v[0:3]\n"
    "buffer_load_dwordx4 v253, s[72:75], s62 offen offset:1024 lds\n"
    "v_mfma_f32_16x16x32_bf16 v[0:3], v[168:171], a[32:35], v[0:3]\n"
    "ds_read_b64_tr_b16 v[182:183], v234 offset:6144\n"
    "v_cvt_pk_bf16_f32 v133, v38, v39\n"
    "v_mfma_f32_16x16x32_bf16 v[0:3], v[172:175], a[48:51], v[0:3]\n"
    "ds_read_b64_tr_b16 v[184:185], v234 offset:8192\n"
    "v_lshlrev_b32 v98, 16, v73\n"
    "v_and_b32 v99, 0xffff0000, v73\n"
    "v_lshlrev_b32 v100, 16, v74\n"
    "v_and_b32 v101, 0xffff0000, v74\n"
    "v_cvt_pk_bf16_f32 v136, v40, v41\n"
    "v_mfma_f32_16x16x32_bf16 v[4:7], v[144:147], a[132:135], 0\n"
    "ds_read_b64_tr_b16 v[186:187], v234 offset:10240\n"
    "v_cvt_pk_bf16_f32 v137, v42, v43\n"
    "v_mfma_f32_16x16x32_bf16 v[4:7], v[148:151], a[148:151], v[4:7]\n"
    "buffer_load_dwordx4 v254, s[72:75], s62 offen offset:2048 lds\n"
    "v_mfma_f32_16x16x32_bf16 v[4:7], v[152:155], a[164:167], v[4:7]\n"
    "ds_read_b64_tr_b16 v[188:189], v234 offset:12288\n"
    "v_cvt_pk_bf16_f32 v140, v44, v45\n"
    "v_mfma_f32_16x16x32_bf16 v[4:7], v[156:159], a[180:183], v[4:7]\n"
    "ds_read_b64_tr_b16 v[190:191], v234 offset:14336\n"
    "v_cvt_pk_bf16_f32 v141, v46, v47\n"
    "v_mfma_f32_16x16x32_bf16 v[4:7], v[160:163], a[4:7], v[4:7]\n"
    "v_lshlrev_b32 v102, 16, v75\n"
    "v_and_b32 v103, 0xffff0000, v75\n"
    "v_mfma_f32_16x16x32_bf16 v[4:7], v[164:167], a[20:23], v[4:7]\n"
    "buffer_load_dwordx4 v255, s[72:75], s62 offen offset:3072 lds\n"
    "v_mfma_f32_16x16x32_bf16 v[4:7], v[168:171], a[36:39], v[4:7]\n"
    "v_fmac_f32 v208, v96, v222\n"
    "s_waitcnt vmcnt(19)\n"
    "v_mfma_f32_16x16x32_bf16 v[4:7], v[172:175], a[52:55], v[4:7]\n"
    "s_waitcnt lgkmcnt(0)\n"
    "s_barrier\n"
    "v_mfma_f32_16x16x32_bf16 v[8:11], v[144:147], a[136:139], 0\n"
    "s_add_u32 m0, s59, 0\n"
    "v_fmac_f32 v209, v97, v222\n"
    "v_fmac_f32 v210, v98, v222\n"
    "s_add_u32 s62, s84, 640\n"
    "v_mfma_f32_16x16x32_bf16 v[8:11], v[148:151], a[152:155], v[8:11]\n"
    "buffer_load_dwordx4 v252, s[72:75], s62 offen offset:0 lds\n"
    "v_mfma_f32_16x16x32_bf16 v[8:11], v[152:155], a[168:171], v[8:11]\n"
    "ds_read_b64_tr_b16 v[192:193], v234 offset:16384\n"
    "v_fmac_f32 v211, v99, v222\n"
    "v_mfma_f32_16x16x32_bf16 v[8:11], v[156:159], a[184:187], v[8:11]\n"
    "ds_read_b64_tr_b16 v[194:195], v234 offset:18432\n"
    "v_fmac_f32 v216, v100, v222\n"
    "v_mfma_f32_16x16x32_bf16 v[8:11], v[160:163], a[8:11], v[8:11]\n"
    "ds_read_b64_tr_b16 v[196:197], v234 offset:20480\n"
    "v_fmac_f32 v217, v101, v222\n"
    "v_mfma_f32_16x16x32_bf16 v[8:11], v[164:167], a[24:27], v[8:11]\n"
    "buffer_load_dwordx4 v253, s[72:75], s62 offen offset:1024 lds\n"
    "v_mfma_f32_16x16x32_bf16 v[8:11], v[168:171], a[40:43], v[8:11]\n"
    "ds_read_b64_tr_b16 v[198:199], v234 offset:22528\n"
    "v_fmac_f32 v218, v102, v222\n"
    "v_mfma_f32_16x16x32_bf16 v[8:11], v[172:175], a[56:59], v[8:11]\n"
    "ds_read_b64_tr_b16 v[200:201], v234 offset:24576\n"
    "v_fmac_f32 v219, v103, v222\n"
    "v_and_b32 v224, 15, v250\n"
    "v_lshlrev_b32 v224, 24, v224\n"
    "v_lshrrev_b32 v225, 1, v250\n"
    "v_mfma_f32_16x16x32_bf16 v[12:15], v[144:147], a[140:143], 0\n"
    "ds_read_b64_tr_b16 v[202:203], v234 offset:26624\n"
    "v_or_b32 v224, v224, v225\n"
    "v_mfma_f32_16x16x32_bf16 v[12:15], v[148:151], a[156:159], v[12:15]\n"
    "buffer_load_dwordx4 v254, s[72:75], s62 offen offset:2048 lds\n"
    "v_mfma_f32_16x16x32_bf16 v[12:15], v[152:155], a[172:175], v[12:15]\n"
    "ds_read_b64_tr_b16 v[204:205], v234 offset:28672\n"
    "v_permlane32_swap_b32 v208, v216\n"
    "v_mfma_f32_16x16x32_bf16 v[12:15], v[156:159], a[188:191], v[12:15]\n"
    "ds_read_b64_tr_b16 v[206:207], v234 offset:30720\n"
    "v_permlane32_swap_b32 v209, v217\n"
    "v_mfma_f32_16x16x32_bf16 v[12:15], v[160:163], a[12:15], v[12:15]\n"
    "buffer_store_dwordx4 v[124:127], v240, s[76:79], s99 offen offset:0 sc1\n"
    "v_mfma_f32_16x16x32_bf16 v[12:15], v[164:167], a[28:31], v[12:15]\n"
    "buffer_load_dwordx4 v255, s[72:75], s62 offen offset:3072 lds\n"
    "v_mfma_f32_16x16x32_bf16 v[12:15], v[168:171], a[44:47], v[12:15]\n"
    "v_permlane32_swap_b32 v210, v218\n"
    "s_waitcnt vmcnt(19)\n"
    "v_mfma_f32_16x16x32_bf16 v[12:15], v[172:175], a[60:63], v[12:15]\n"
    "s_waitcnt lgkmcnt(0)\n"
    "s_barrier\n"
    "v_mfma_f32_16x16x32_bf16 v[0:3], v[176:179], a[192:195], v[0:3]\n"
    "s_add_u32 m0, s59, 16384\n"
    "v_permlane32_swap_b32 v211, v219\n"
    "v_add_f32 v208, v208, v216\n"
    "v_add_f32 v209, v209, v217\n"
    "s_add_u32 s62, s84, 115328\n"
    "v_mfma_f32_16x16x32_bf16 v[0:3], v[180:183], a[208:211], v[0:3]\n"
    "buffer_load_dwordx4 v252, s[72:75], s62 offen offset:0 lds\n"
    "v_mfma_f32_16x16x32_bf16 v[0:3], v[184:187], a[224:227], v[0:3]\n"
    "ds_read_b64_tr_b16 v[144:145], v234 offset:32768\n"
    "v_and_b32 v76, 31, v250\n"
    "v_mfma_f32_16x16x32_bf16 v[0:3], v[188:191], a[240:243], v[0:3]\n"
    "ds_read_b64_tr_b16 v[146:147], v234 offset:34816\n"
    "v_lshlrev_b32 v76, 2, v76\n"
    "v_mfma_f32_16x16x32_bf16 v[0:3], v[192:195], a[64:67], v[0:3]\n"
    "s_lshl_b32 s68, s67, 7\n"
    "ds_read_b64_tr_b16 v[148:149], v234 offset:36864\n"
    "v_add_f32 v210, v210, v218\n"
    "v_mfma_f32_16x16x32_bf16 v[0:3], v[196:199], a[80:83], v[0:3]\n"
    "buffer_load_dwordx4 v253, s[72:75], s62 offen offset:1024 lds\n"
    "v_mfma_f32_16x16x32_bf16 v[0:3], v[200:203], a[96:99], v[0:3]\n"
    "s_add_u32 s68, s68, 16384\n"
    "ds_read_b64_tr_b16 v[150:151], v234 offset:38912\n"
    "v_add_f32 v211, v211, v219\n"
    "v_mfma_f32_16x16x32_bf16 v[0:3], v[204:207], a[112:115], v[0:3]\n"
    "s_nop 0\n"
    "ds_read_b64_tr_b16 v[152:153], v234 offset:40960\n"
    "v_permlane16_swap_b32 v208, v210\n"
    "v_permlane16_swap_b32 v209, v211\n"
    "v_bfe_u32 v242, v250, 1, 3\n"
    "v_mfma_f32_16x16x32_bf16 v[4:7], v[176:179], a[196:199], v[4:7]\n"
    "ds_read_b64_tr_b16 v[154:155], v234 offset:43008\n"
    "v_mul_u32_u24 v242, 128, v242\n"
    "v_mfma_f32_16x16x32_bf16 v[4:7], v[180:183], a[212:215], v[4:7]\n"
    "buffer_load_dwordx4 v254, s[72:75], s62 offen offset:2048 lds\n"
    "v_mfma_f32_16x16x32_bf16 v[4:7], v[184:187], a[228:231], v[4:7]\n"
    "ds_read_b64_tr_b16 v[156:157], v234 offset:45056\n"
    "v_sub_u32 v238, v240, v242\n"
    "v_mfma_f32_16x16x32_bf16 v[4:7], v[188:191], a[244:247], v[4:7]\n"
    "ds_read_b64_tr_b16 v[158:159], v234 offset:47104\n"
    "v_add_u32 v238, 65536, v238\n"
    "v_mfma_f32_16x16x32_bf16 v[4:7], v[192:195], a[68:71], v[4:7]\n"
    "v_add_f32 v208, v208, v210\n"
    "v_add_f32 v209, v209, v211\n"
    "v_mfma_f32_16x16x32_bf16 v[4:7], v[196:199], a[84:87], v[4:7]\n"
    "buffer_load_dwordx4 v255, s[72:75], s62 offen offset:3072 lds\n"
    "v_mfma_f32_16x16x32_bf16 v[4:7], v[200:203], a[100:103], v[4:7]\n"
    "s_nop 0\n"
    "v_add_f32_dpp v208, v208, v208 row_ror:8 row_mask:0xf bank_mask:0xf\n"
    "v_add_f32_dpp v209, v209, v209 row_ror:8 row_mask:0xf bank_mask:0xf\n"
    "s_nop 0\n"
    "s_waitcnt vmcnt(18)\n"
    "v_mfma_f32_16x16x32_bf16 v[4:7], v[204:207], a[116:119], v[4:7]\n"
    "s_waitcnt lgkmcnt(0)\n"
    "s_barrier\n"
    "v_mfma_f32_16x16x32_bf16 v[8:11], v[176:179], a[200:203], v[8:11]\n"
    "s_add_u32 m0, s59, 32768\n"
    "v_add_f32_dpp v208, v208, v208 row_ror:4 row_mask:0xf bank_mask:0xf\n"
    "s_add_u32 s62, s84, 230016\n"
    "v_mfma_f32_16x16x32_bf16 v[8:11], v[180:183], a[216:219], v[8:11]\n"
    "buffer_load_dwordx4 v252, s[72:75], s62 offen offset:0 lds\n"
    "v_mfma_f32_16x16x32_bf16 v[8:11], v[184:187], a[232:235], v[8:11]\n"
    "ds_read_b64_tr_b16 v[160:161], v234 offset:49152\n"
    "v_add_f32_dpp v209, v209, v209 row_ror:4 row_mask:0xf bank_mask:0xf\n"
    "v_mfma_f32_16x16x32_bf16 v[8:11], v[188:191], a[248:251], v[8:11]\n"
    "s_nop 0\n"
    "ds_read_b64_tr_b16 v[162:163], v234 offset:51200\n"
    "v_mfma_f32_16x16x32_bf16 v[8:11], v[192:195], a[72:75], v[8:11]\n"
    "ds_read_b64_tr_b16 v[164:165], v234 offset:53248\n"
    "v_add_f32_dpp v208, v208, v208 row_ror:2 row_mask:0xf bank_mask:0xf\n"
    "v_mfma_f32_16x16x32_bf16 v[8:11], v[196:199], a[88:91], v[8:11]\n"
    "buffer_load_dwordx4 v253, s[72:75], s62 offen offset:1024 lds\n"
    "v_mfma_f32_16x16x32_bf16 v[8:11], v[200:203], a[104:107], v[8:11]\n"
    "ds_read_b64_tr_b16 v[166:167], v234 offset:55296\n"
    "v_add_f32_dpp v209, v209, v209 row_ror:2 row_mask:0xf bank_mask:0xf\n"
    "v_mfma_f32_16x16x32_bf16 v[8:11], v[204:207], a[120:123], v[8:11]\n"
    "s_nop 0\n"
    "ds_read_b64_tr_b16 v[168:169], v234 offset:57344\n"
    "v_add_f32_dpp v208, v208, v208 row_ror:1 row_mask:0xf bank_mask:0xf\n"
    "v_add_f32_dpp v209, v209, v209 row_ror:1 row_mask:0xf bank_mask:0xf\n"
    "v_mfma_f32_16x16x32_bf16 v[12:15], v[176:179], a[204:207], v[12:15]\n"
    "ds_read_b64_tr_b16 v[170:171], v234 offset:59392\n"
    "v_cvt_pk_bf16_f32 v208, 0, v208\n"
    "v_mfma_f32_16x16x32_bf16 v[12:15], v[180:183], a[220:223], v[12:15]\n"
    "buffer_load_dwordx4 v254, s[72:75], s62 offen offset:2048 lds\n"
    "v_mfma_f32_16x16x32_bf16 v[12:15], v[184:187], a[236:239], v[12:15]\n"
    "ds_read_b64_tr_b16 v[172:173], v234 offset:61440\n"
    "v_cvt_pk_bf16_f32 v209, 0, v209\n"
    "v_mfma_f32_16x16x32_bf16 v[12:15], v[188:191], a[252:255], v[12:15]\n"
    "ds_read_b64_tr_b16 v[174:175], v234 offset:63488\n"
    "v_add_f32 v208, s82, v208\n"
    "v_mfma_f32_16x16x32_bf16 v[12:15], v[192:195], a[76:79], v[12:15]\n"
    "v_add_f32 v209, s82, v209\n"
    "v_cvt_pk_bf16_f32 v208, v208, v209\n"
    "v_mfma_f32_16x16x32_bf16 v[12:15], v[196:199], a[92:95], v[12:15]\n"
    "buffer_load_dwordx4 v255, s[72:75], s62 offen offset:3072 lds\n"
    "v_mfma_f32_16x16x32_bf16 v[12:15], v[200:203], a[108:111], v[12:15]\n"
    "v_mov_b32 v209, v229\n"
    "s_waitcnt vmcnt(17)\n"
    "v_mfma_f32_16x16x32_bf16 v[12:15], v[204:207], a[124:127], v[12:15]\n"
    "s_waitcnt lgkmcnt(0)\n"
    "s_barrier\n"
    "v_mfma_f32_16x16x32_bf16 v[16:19], v[144:147], a[128:131], 0\n"
    "s_add_u32 m0, s59, 49152\n"
    "s_add_u32 s62, s84, 344704\n"
    "v_mfma_f32_16x16x32_bf16 v[16:19], v[148:151], a[144:147], v[16:19]\n"
    "buffer_load_dwordx4 v252, s[72:75], s62 offen offset:0 lds\n"
    "v_mfma_f32_16x16x32_bf16 v[16:19], v[152:155], a[160:163], v[16:19]\n"
    "ds_read_b64_tr_b16 v[176:177], v236 offset:0\n"
    "v_cvt_pk_bf16_f32 v130, v0, v1\n"
    "v_mfma_f32_16x16x32_bf16 v[16:19], v[156:159], a[176:179], v[16:19]\n"
    "ds_read_b64_tr_b16 v[178:179], v236 offset:2048\n"
    "v_cvt_pk_bf16_f32 v131, v2, v3\n"
    "v_mfma_f32_16x16x32_bf16 v[16:19], v[160:163], a[0:3], v[16:19]\n"
    "ds_read_b64_tr_b16 v[180:181], v236 offset:4096\n"
    "v_cvt_pk_bf16_f32 v134, v4, v5\n"
    "v_mfma_f32_16x16x32_bf16 v[16:19], v[164:167], a[16:19], v[16:19]\n"
    "buffer_load_dwordx4 v253, s[72:75], s62 offen offset:1024 lds\n"
    "v_mfma_f32_16x16x32_bf16 v[16:19], v[168:171], a[32:35], v[16:19]\n"
    "ds_read_b64_tr_b16 v[182:183], v236 offset:6144\n"
    "v_cvt_pk_bf16_f32 v135, v6, v7\n"
    "v_mfma_f32_16x16x32_bf16 v[16:19], v[172:175], a[48:51], v[16:19]\n"
    "ds_read_b64_tr_b16 v[184:185], v236 offset:8192\n"
    "v_cvt_pk_bf16_f32 v138, v8, v9\n"
    "v_mfma_f32_16x16x32_bf16 v[20:23], v[144:147], a[132:135], 0\n"
    "ds_read_b64_tr_b16 v[186:187], v236 offset:10240\n"
    "v_cvt_pk_bf16_f32 v139, v10, v11\n"
    "v_mfma_f32_16x16x32_bf16 v[20:23], v[148:151], a[148:151], v[20:23]\n"
    "buffer_load_dwordx4 v254, s[72:75], s62 offen offset:2048 lds\n"
    "v_mfma_f32_16x16x32_bf16 v[20:23], v[152:155], a[164:167], v[20:23]\n"
    "ds_read_b64_tr_b16 v[188:189], v236 offset:12288\n"
    "v_cvt_pk_bf16_f32 v142, v12, v13\n"
    "v_mfma_f32_16x16x32_bf16 v[20:23], v[156:159], a[180:183], v[20:23]\n"
    "ds_read_b64_tr_b16 v[190:191], v236 offset:14336\n"
    "v_cvt_pk_bf16_f32 v143, v14, v15\n"
    "v_mfma_f32_16x16x32_bf16 v[20:23], v[160:163], a[4:7], v[20:23]\n"
    "buffer_store_dwordx2 v[208:209], v224, s[52:55], s86 offen sc1\n"
    "v_mfma_f32_16x16x32_bf16 v[20:23], v[164:167], a[20:23], v[20:23]\n"
    "buffer_load_dwordx4 v255, s[72:75], s62 offen offset:3072 lds\n"
    "v_mfma_f32_16x16x32_bf16 v[20:23], v[168:171], a[36:39], v[20:23]\n"
    "s_waitcnt vmcnt(18)\n"
    "v_mfma_f32_16x16x32_bf16 v[20:23], v[172:175], a[52:55], v[20:23]\n"
    "s_waitcnt lgkmcnt(0)\n"
    "s_barrier\n"
    "v_mfma_f32_16x16x32_bf16 v[24:27], v[144:147], a[136:139], 0\n"
    "s_add_u32 m0, s59, 131072\n"
    "s_add_u32 s62, s84, 768\n"
    "v_mfma_f32_16x16x32_bf16 v[24:27], v[148:151], a[152:155], v[24:27]\n"
    "buffer_load_dwordx4 v252, s[72:75], s62 offen offset:0 lds\n"
    "v_mfma_f32_16x16x32_bf16 v[24:27], v[152:155], a[168:171], v[24:27]\n"
    "ds_read_b64_tr_b16 v[192:193], v236 offset:16384\n"
    "v_mfma_f32_16x16x32_bf16 v[24:27], v[156:159], a[184:187], v[24:27]\n"
    "ds_read_b64_tr_b16 v[194:195], v236 offset:18432\n"
    "v_mfma_f32_16x16x32_bf16 v[24:27], v[160:163], a[8:11], v[24:27]\n"
    "ds_read_b64_tr_b16 v[196:197], v236 offset:20480\n"
    "v_mfma_f32_16x16x32_bf16 v[24:27], v[164:167], a[24:27], v[24:27]\n"
    "buffer_load_dwordx4 v253, s[72:75], s62 offen offset:1024 lds\n"
    "v_mfma_f32_16x16x32_bf16 v[24:27], v[168:171], a[40:43], v[24:27]\n"
    "ds_read_b64_tr_b16 v[198:199], v236 offset:22528\n"
    "v_mfma_f32_16x16x32_bf16 v[24:27], v[172:175], a[56:59], v[24:27]\n"
    "ds_read_b64_tr_b16 v[200:201], v236 offset:24576\n"
    "v_mfma_f32_16x16x32_bf16 v[28:31], v[144:147], a[140:143], 0\n"
    "ds_read_b64_tr_b16 v[202:203], v236 offset:26624\n"
    "v_mfma_f32_16x16x32_bf16 v[28:31], v[148:151], a[156:159], v[28:31]\n"
    "buffer_load_dwordx4 v254, s[72:75], s62 offen offset:2048 lds\n"
    "v_mfma_f32_16x16x32_bf16 v[28:31], v[152:155], a[172:175], v[28:31]\n"
    "ds_read_b64_tr_b16 v[204:205], v236 offset:28672\n"
    "v_mfma_f32_16x16x32_bf16 v[28:31], v[156:159], a[188:191], v[28:31]\n"
    "ds_read_b64_tr_b16 v[206:207], v236 offset:30720\n"
    "v_mfma_f32_16x16x32_bf16 v[28:31], v[160:163], a[12:15], v[28:31]\n"
    "buffer_store_dwordx4 v[128:131], v240, s[76:79], s96 offen offset:512 sc1\n"
    "v_mfma_f32_16x16x32_bf16 v[28:31], v[164:167], a[28:31], v[28:31]\n"
    "buffer_load_dwordx4 v255, s[72:75], s62 offen offset:3072 lds\n"
    "v_mfma_f32_16x16x32_bf16 v[28:31], v[168:171], a[44:47], v[28:31]\n"
    "s_waitcnt vmcnt(18)\n"
    "v_mfma_f32_16x16x32_bf16 v[28:31], v[172:175], a[60:63], v[28:31]\n"
    "s_waitcnt lgkmcnt(0)\n"
    "s_barrier\n"
    "v_mfma_f32_16x16x32_bf16 v[16:19], v[176:179], a[192:195], v[16:19]\n"
    "s_add_u32 m0, s59, 147456\n"
    "s_add_u32 s62, s84, 115456\n"
    "v_mfma_f32_16x16x32_bf16 v[16:19], v[180:183], a[208:211], v[16:19]\n"
    "buffer_load_dwordx4 v252, s[72:75], s62 offen offset:0 lds\n"
    "v_mfma_f32_16x16x32_bf16 v[16:19], v[184:187], a[224:227], v[16:19]\n"
    "ds_read_b64_tr_b16 v[144:145], v234 offset:0\n"
    "v_mfma_f32_16x16x32_bf16 v[16:19], v[188:191], a[240:243], v[16:19]\n"
    "ds_read_b64_tr_b16 v[146:147], v234 offset:2048\n"
    "v_mfma_f32_16x16x32_bf16 v[16:19], v[192:195], a[64:67], v[16:19]\n"
    "ds_read_b64_tr_b16 v[148:149], v234 offset:4096\n"
    "v_mfma_f32_16x16x32_bf16 v[16:19], v[196:199], a[80:83], v[16:19]\n"
    "buffer_load_dwordx4 v253, s[72:75], s62 offen offset:1024 lds\n"
    "v_mfma_f32_16x16x32_bf16 v[16:19], v[200:203], a[96:99], v[16:19]\n"
    "ds_read_b64_tr_b16 v[150:151], v234 offset:6144\n"
    "v_mfma_f32_16x16x32_bf16 v[16:19], v[204:207], a[112:115], v[16:19]\n"
    "ds_read_b64_tr_b16 v[152:153], v234 offset:8192\n"
    "v_mfma_f32_16x16x32_bf16 v[20:23], v[176:179], a[196:199], v[20:23]\n"
    "ds_read_b64_tr_b16 v[154:155], v234 offset:10240\n"
    "v_mfma_f32_16x16x32_bf16 v[20:23], v[180:183], a[212:215], v[20:23]\n"
    "buffer_load_dwordx4 v254, s[72:75], s62 offen offset:2048 lds\n"
    "v_mfma_f32_16x16x32_bf16 v[20:23], v[184:187], a[228:231], v[20:23]\n"
    "ds_read_b64_tr_b16 v[156:157], v234 offset:12288\n"
    "v_mfma_f32_16x16x32_bf16 v[20:23], v[188:191], a[244:247], v[20:23]\n"
    "ds_read_b64_tr_b16 v[158:159], v234 offset:14336\n"
    "v_mfma_f32_16x16x32_bf16 v[20:23], v[192:195], a[68:71], v[20:23]\n"
    "buffer_store_dwordx4 v[132:135], v240, s[76:79], s97 offen offset:512 sc1\n"
    "v_mfma_f32_16x16x32_bf16 v[20:23], v[196:199], a[84:87], v[20:23]\n"
    "buffer_load_dwordx4 v255, s[72:75], s62 offen offset:3072 lds\n"
    "v_mfma_f32_16x16x32_bf16 v[20:23], v[200:203], a[100:103], v[20:23]\n"
    "s_waitcnt vmcnt(19)\n"
    "v_mfma_f32_16x16x32_bf16 v[20:23], v[204:207], a[116:119], v[20:23]\n"
    "s_waitcnt lgkmcnt(0)\n"
    "s_barrier\n"
    "v_mfma_f32_16x16x32_bf16 v[24:27], v[176:179], a[200:203], v[24:27]\n"
    "s_add_u32 m0, s59, 0\n"
    "s_add_u32 s62, s84, 230144\n"
    "v_mfma_f32_16x16x32_bf16 v[24:27], v[180:183], a[216:219], v[24:27]\n"
    "buffer_load_dwordx4 v252, s[72:75], s62 offen offset:0 lds\n"
    "v_mfma_f32_16x16x32_bf16 v[24:27], v[184:187], a[232:235], v[24:27]\n"
    "ds_read_b64_tr_b16 v[160:161], v234 offset:16384\n"
    "v_mfma_f32_16x16x32_bf16 v[24:27], v[188:191], a[248:251], v[24:27]\n"
    "ds_read_b64_tr_b16 v[162:163], v234 offset:18432\n"
    "v_mfma_f32_16x16x32_bf16 v[24:27], v[192:195], a[72:75], v[24:27]\n"
    "ds_read_b64_tr_b16 v[164:165], v234 offset:20480\n"
    "v_mfma_f32_16x16x32_bf16 v[24:27], v[196:199], a[88:91], v[24:27]\n"
    "buffer_load_dwordx4 v253, s[72:75], s62 offen offset:1024 lds\n"
    "v_mfma_f32_16x16x32_bf16 v[24:27], v[200:203], a[104:107], v[24:27]\n"
    "ds_read_b64_tr_b16 v[166:167], v234 offset:22528\n"
    "v_mfma_f32_16x16x32_bf16 v[24:27], v[204:207], a[120:123], v[24:27]\n"
    "ds_read_b64_tr_b16 v[168:169], v234 offset:24576\n"
    "v_mfma_f32_16x16x32_bf16 v[28:31], v[176:179], a[204:207], v[28:31]\n"
    "ds_read_b64_tr_b16 v[170:171], v234 offset:26624\n"
    "v_mfma_f32_16x16x32_bf16 v[28:31], v[180:183], a[220:223], v[28:31]\n"
    "buffer_load_dwordx4 v254, s[72:75], s62 offen offset:2048 lds\n"
    "v_mfma_f32_16x16x32_bf16 v[28:31], v[184:187], a[236:239], v[28:31]\n"
    "ds_read_b64_tr_b16 v[172:173], v234 offset:28672\n"
    "v_mfma_f32_16x16x32_bf16 v[28:31], v[188:191], a[252:255], v[28:31]\n"
    "ds_read_b64_tr_b16 v[174:175], v234 offset:30720\n"
    "v_mfma_f32_16x16x32_bf16 v[28:31], v[192:195], a[76:79], v[28:31]\n"
    "buffer_store_dwordx4 v[136:139], v240, s[76:79], s98 offen offset:512 sc1\n"
    "v_mfma_f32_16x16x32_bf16 v[28:31], v[196:199], a[92:95], v[28:31]\n"
    "buffer_load_dwordx4 v255, s[72:75], s62 offen offset:3072 lds\n"
    "v_mfma_f32_16x16x32_bf16 v[28:31], v[200:203], a[108:111], v[28:31]\n"
    "s_waitcnt vmcnt(20)\n"
    "v_mfma_f32_16x16x32_bf16 v[28:31], v[204:207], a[124:127], v[28:31]\n"
    "s_waitcnt lgkmcnt(0)\n"
    "s_barrier\n"
    "v_mfma_f32_16x16x32_bf16 v[32:35], v[144:147], a[128:131], 0\n"
    "s_add_u32 m0, s59, 16384\n"
    "s_add_u32 s62, s84, 344832\n"
    "v_mfma_f32_16x16x32_bf16 v[32:35], v[148:151], a[144:147], v[32:35]\n"
    "buffer_load_dwordx4 v252, s[72:75], s62 offen offset:0 lds\n"
    "v_mfma_f32_16x16x32_bf16 v[32:35], v[152:155], a[160:163], v[32:35]\n"
    "ds_read_b64_tr_b16 v[176:177], v234 offset:32768\n"
    "v_cvt_pk_bf16_f32 v80, v16, v17\n"
    "v_mfma_f32_16x16x32_bf16 v[32:35], v[156:159], a[176:179], v[32:35]\n"
    "ds_read_b64_tr_b16 v[178:179], v234 offset:34816\n"
    "v_cvt_pk_bf16_f32 v81, v18, v19\n"
    "v_mfma_f32_16x16x32_bf16 v[32:35], v[160:163], a[0:3], v[32:35]\n"
    "ds_read_b64_tr_b16 v[180:181], v234 offset:36864\n"
    "v_cvt_pk_bf16_f32 v84, v20, v21\n"
    "v_mfma_f32_16x16x32_bf16 v[32:35], v[164:167], a[16:19], v[32:35]\n"
    "buffer_load_dwordx4 v253, s[72:75], s62 offen offset:1024 lds\n"
    "v_mfma_f32_16x16x32_bf16 v[32:35], v[168:171], a[32:35], v[32:35]\n"
    "ds_read_b64_tr_b16 v[182:183], v234 offset:38912\n"
    "v_cvt_pk_bf16_f32 v85, v22, v23\n"
    "v_mfma_f32_16x16x32_bf16 v[32:35], v[172:175], a[48:51], v[32:35]\n"
    "ds_read_b64_tr_b16 v[184:185], v234 offset:40960\n"
    "v_cvt_pk_bf16_f32 v88, v24, v25\n"
    "v_mfma_f32_16x16x32_bf16 v[36:39], v[144:147], a[132:135], 0\n"
    "ds_read_b64_tr_b16 v[186:187], v234 offset:43008\n"
    "v_cvt_pk_bf16_f32 v89, v26, v27\n"
    "v_mfma_f32_16x16x32_bf16 v[36:39], v[148:151], a[148:151], v[36:39]\n"
    "buffer_load_dwordx4 v254, s[72:75], s62 offen offset:2048 lds\n"
    "v_mfma_f32_16x16x32_bf16 v[36:39], v[152:155], a[164:167], v[36:39]\n"
    "ds_read_b64_tr_b16 v[188:189], v234 offset:45056\n"
    "v_cvt_pk_bf16_f32 v92, v28, v29\n"
    "v_mfma_f32_16x16x32_bf16 v[36:39], v[156:159], a[180:183], v[36:39]\n"
    "ds_read_b64_tr_b16 v[190:191], v234 offset:47104\n"
    "v_cvt_pk_bf16_f32 v93, v30, v31\n"
    "v_mfma_f32_16x16x32_bf16 v[36:39], v[160:163], a[4:7], v[36:39]\n"
    "v_mfma_f32_16x16x32_bf16 v[36:39], v[164:167], a[20:23], v[36:39]\n"
    "buffer_load_dwordx4 v255, s[72:75], s62 offen offset:3072 lds\n"
    "v_mfma_f32_16x16x32_bf16 v[36:39], v[168:171], a[36:39], v[36:39]\n"
    "s_waitcnt vmcnt(19)\n"
    "v_mfma_f32_16x16x32_bf16 v[36:39], v[172:175], a[52:55], v[36:39]\n"
    "s_waitcnt lgkmcnt(0)\n"
    "s_barrier\n"
    "v_mfma_f32_16x16x32_bf16 v[40:43], v[144:147], a[136:139], 0\n"
    "ds_read_b64_tr_b16 v[192:193], v234 offset:49152\n"
    "v_mfma_f32_16x16x32_bf16 v[40:43], v[148:151], a[152:155], v[40:43]\n"
    "ds_read_b64_tr_b16 v[194:195], v234 offset:51200\n"
    "v_mfma_f32_16x16x32_bf16 v[40:43], v[152:155], a[168:171], v[40:43]\n"
    "ds_read_b64_tr_b16 v[196:197], v234 offset:53248\n"
    "v_mfma_f32_16x16x32_bf16 v[40:43], v[156:159], a[184:187], v[40:43]\n"
    "ds_read_b64_tr_b16 v[198:199], v234 offset:55296\n"
    "v_mfma_f32_16x16x32_bf16 v[40:43], v[160:163], a[8:11], v[40:43]\n"
    "ds_read_b64_tr_b16 v[200:201], v234 offset:57344\n"
    "v_mfma_f32_16x16x32_bf16 v[40:43], v[164:167], a[24:27], v[40:43]\n"
    "ds_read_b64_tr_b16 v[202:203], v234 offset:59392\n"
    "v_mfma_f32_16x16x32_bf16 v[40:43], v[168:171], a[40:43], v[40:43]\n"
    "ds_read_b64_tr_b16 v[204:205], v234 offset:61440\n"
    "v_mfma_f32_16x16x32_bf16 v[40:43], v[172:175], a[56:59], v[40:43]\n"
    "ds_read_b64_tr_b16 v[206:207], v234 offset:63488\n"
    "v_mfma_f32_16x16x32_bf16 v[44:47], v[144:147], a[140:143], 0\n"
    "buffer_store_dwordx4 v[140:143], v240, s[76:79], s99 offen offset:512 sc1\n"
    "v_mfma_f32_16x16x32_bf16 v[44:47], v[148:151], a[156:159], v[44:47]\n"
    "v_mfma_f32_16x16x32_bf16 v[44:47], v[152:155], a[172:175], v[44:47]\n"
    "v_mfma_f32_16x16x32_bf16 v[44:47], v[156:159], a[188:191], v[44:47]\n"
    "v_mfma_f32_16x16x32_bf16 v[44:47], v[160:163], a[12:15], v[44:47]\n"
    "v_mfma_f32_16x16x32_bf16 v[44:47], v[164:167], a[28:31], v[44:47]\n"
    "v_mfma_f32_16x16x32_bf16 v[44:47], v[168:171], a[44:47], v[44:47]\n"
    "s_waitcnt vmcnt(15)\n"
    "v_mfma_f32_16x16x32_bf16 v[44:47], v[172:175], a[60:63], v[44:47]\n"
    "s_waitcnt lgkmcnt(0)\n"
    "s_barrier\n"
    "v_mfma_f32_16x16x32_bf16 v[32:35], v[176:179], a[192:195], v[32:35]\n"
    "ds_read_b64_tr_b16 v[144:145], v236 offset:0\n"
    "v_mfma_f32_16x16x32_bf16 v[32:35], v[180:183], a[208:211], v[32:35]\n"
    "ds_read_b64_tr_b16 v[146:147], v236 offset:2048\n"
    "v_mfma_f32_16x16x32_bf16 v[32:35], v[184:187], a[224:227], v[32:35]\n"
    "ds_read_b64_tr_b16 v[148:149], v236 offset:4096\n"
    "v_mov_b32 v97, v229\n"
    "v_mfma_f32_16x16x32_bf16 v[32:35], v[188:191], a[240:243], v[32:35]\n"
    "ds_read_b64_tr_b16 v[150:151], v236 offset:6144\n"
    "v_mov_b32 v99, v229\n"
    "v_mfma_f32_16x16x32_bf16 v[32:35], v[192:195], a[64:67], v[32:35]\n"
    "ds_read_b64_tr_b16 v[152:153], v236 offset:8192\n"
    "v_mov_b32 v101, v229\n"
    "v_mfma_f32_16x16x32_bf16 v[32:35], v[196:199], a[80:83], v[32:35]\n"
    "ds_read_b64_tr_b16 v[154:155], v236 offset:10240\n"
    "v_mov_b32 v103, v229\n"
    "v_mfma_f32_16x16x32_bf16 v[32:35], v[200:203], a[96:99], v[32:35]\n"
    "ds_read_b64_tr_b16 v[156:157], v236 offset:12288\n"
    "v_mov_b32 v105, v229\n"
    "v_mfma_f32_16x16x32_bf16 v[32:35], v[204:207], a[112:115], v[32:35]\n"
    "ds_read_b64_tr_b16 v[158:159], v236 offset:14336\n"
    "v_mov_b32 v107, v229\n"
    "v_mfma_f32_16x16x32_bf16 v[36:39], v[176:179], a[196:199], v[36:39]\n"
    "v_mov_b32 v109, v229\n"
    "v_mov_b32 v111, v229\n"
    "v_mfma_f32_16x16x32_bf16 v[36:39], v[180:183], a[212:215], v[36:39]\n"
    "v_mfma_f32_16x16x32_bf16 v[36:39], v[184:187], a[228:231], v[36:39]\n"
    "v_mfma_f32_16x16x32_bf16 v[36:39], v[188:191], a[244:247], v[36:39]\n"
    "v_mfma_f32_16x16x32_bf16 v[36:39], v[192:195], a[68:71], v[36:39]\n"
    "v_mfma_f32_16x16x32_bf16 v[36:39], v[196:199], a[84:87], v[36:39]\n"
    "v_mfma_f32_16x16x32_bf16 v[36:39], v[200:203], a[100:103], v[36:39]\n"
    "s_waitcnt vmcnt(10)\n"
    "v_mfma_f32_16x16x32_bf16 v[36:39], v[204:207], a[116:119], v[36:39]\n"
    "s_waitcnt lgkmcnt(0)\n"
    "s_barrier\n"
    "v_mfma_f32_16x16x32_bf16 v[40:43], v[176:179], a[200:203], v[40:43]\n"
    "ds_read_b64_tr_b16 v[160:161], v236 offset:16384\n"
    "v_mfma_f32_16x16x32_bf16 v[40:43], v[180:183], a[216:219], v[40:43]\n"
    "ds_read_b64_tr_b16 v[162:163], v236 offset:18432\n"
    "v_mfma_f32_16x16x32_bf16 v[40:43], v[184:187], a[232:235], v[40:43]\n"
    "ds_read_b64_tr_b16 v[164:165], v236 offset:20480\n"
    "v_mfma_f32_16x16x32_bf16 v[40:43], v[188:191], a[248:251], v[40:43]\n"
    "ds_read_b64_tr_b16 v[166:167], v236 offset:22528\n"
    "v_mfma_f32_16x16x32_bf16 v[40:43], v[192:195], a[72:75], v[40:43]\n"
    "ds_read_b64_tr_b16 v[168:169], v236 offset:24576\n"
    "v_mfma_f32_16x16x32_bf16 v[40:43], v[196:199], a[88:91], v[40:43]\n"
    "ds_read_b64_tr_b16 v[170:171], v236 offset:26624\n"
    "v_mfma_f32_16x16x32_bf16 v[40:43], v[200:203], a[104:107], v[40:43]\n"
    "ds_read_b64_tr_b16 v[172:173], v236 offset:28672\n"
    "v_mfma_f32_16x16x32_bf16 v[40:43], v[204:207], a[120:123], v[40:43]\n"
    "ds_read_b64_tr_b16 v[174:175], v236 offset:30720\n"
    "v_mfma_f32_16x16x32_bf16 v[44:47], v[176:179], a[204:207], v[44:47]\n"
    "v_mfma_f32_16x16x32_bf16 v[44:47], v[180:183], a[220:223], v[44:47]\n"
    "v_mfma_f32_16x16x32_bf16 v[44:47], v[184:187], a[236:239], v[44:47]\n"
    "v_mfma_f32_16x16x32_bf16 v[44:47], v[188:191], a[252:255], v[44:47]\n"
    "v_mfma_f32_16x16x32_bf16 v[44:47], v[192:195], a[76:79], v[44:47]\n"
    "v_mfma_f32_16x16x32_bf16 v[44:47], v[196:199], a[92:95], v[44:47]\n"
    "v_mfma_f32_16x16x32_bf16 v[44:47], v[200:203], a[108:111], v[44:47]\n"
    "s_waitcnt vmcnt(5)\n"
    "v_mfma_f32_16x16x32_bf16 v[44:47], v[204:207], a[124:127], v[44:47]\n"
    "s_waitcnt lgkmcnt(0)\n"
    "s_barrier\n"
    "v_mfma_f32_16x16x32_bf16 v[0:3], v[144:147], a[128:131], 0\n"
    "ds_read_b64_tr_b16 v[176:177], v234 offset:0\n"
    "v_mfma_f32_16x16x32_bf16 v[0:3], v[148:151], a[144:147], v[0:3]\n"
    "ds_read_b64_tr_b16 v[178:179], v234 offset:2048\n"
    "v_mfma_f32_16x16x32_bf16 v[0:3], v[152:155], a[160:163], v[0:3]\n"
    "ds_read_b64_tr_b16 v[180:181], v234 offset:4096\n"
    "v_cvt_pk_bf16_f32 v82, v32, v33\n"
    "v_mfma_f32_16x16x32_bf16 v[0:3], v[156:159], a[176:179], v[0:3]\n"
    "ds_read_b64_tr_b16 v[182:183], v234 offset:6144\n"
    "v_cvt_pk_bf16_f32 v83, v34, v35\n"
    "v_mfma_f32_16x16x32_bf16 v[0:3], v[160:163], a[0:3], v[0:3]\n"
    "ds_read_b64_tr_b16 v[184:185], v234 offset:8192\n"
    "v_cvt_pk_bf16_f32 v86, v36, v37\n"
    "v_mfma_f32_16x16x32_bf16 v[0:3], v[164:167], a[16:19], v[0:3]\n"
    "ds_read_b64_tr_b16 v[186:187], v234 offset:10240\n"
    "v_cvt_pk_bf16_f32 v87, v38, v39\n"
    "v_mfma_f32_16x16x32_bf16 v[0:3], v[168:171], a[32:35], v[0:3]\n"
    "ds_read_b64_tr_b16 v[188:189], v234 offset:12288\n"
    "v_cvt_pk_bf16_f32 v90, v40, v41\n"
    "v_mfma_f32_16x16x32_bf16 v[0:3], v[172:175], a[48:51], v[0:3]\n"
    "ds_read_b64_tr_b16 v[190:191], v234 offset:14336\n"
    "v_cvt_pk_bf16_f32 v91, v42, v43\n"
    "v_mfma_f32_16x16x32_bf16 v[4:7], v[144:147], a[132:135], 0\n"
    "v_cvt_pk_bf16_f32 v94, v44, v45\n"
    "v_cvt_pk_bf16_f32 v95, v46, v47\n"
    "v_mfma_f32_16x16x32_bf16 v[4:7], v[148:151], a[148:151], v[4:7]\n"
    "buffer_store_dwordx4 v[80:83], v240, s[76:79], s96 offen offset:1024 sc1\n"
    "v_mfma_f32_16x16x32_bf16 v[4:7], v[152:155], a[164:167], v[4:7]\n"
    "buffer_store_dwordx4 v[84:87], v240, s[76:79], s97 offen offset:1024 sc1\n"
    "v_mfma_f32_16x16x32_bf16 v[4:7], v[156:159], a[180:183], v[4:7]\n"
    "buffer_store_dwordx4 v[88:91], v240, s[76:79], s98 offen offset:1024 sc1\n"
    "v_mfma_f32_16x16x32_bf16 v[4:7], v[160:163], a[4:7], v[4:7]\n"
    "buffer_store_dwordx4 v[92:95], v240, s[76:79], s99 offen offset:1024 sc1\n"
    "v_mfma_f32_16x16x32_bf16 v[4:7], v[164:167], a[20:23], v[4:7]\n"
    "v_mfma_f32_16x16x32_bf16 v[4:7], v[168:171], a[36:39], v[4:7]\n"
    "s_waitcnt vmcnt(5)\n"
    "v_mfma_f32_16x16x32_bf16 v[4:7], v[172:175], a[52:55], v[4:7]\n"
    "s_waitcnt lgkmcnt(0)\n"
    "s_barrier\n"
    "v_mfma_f32_16x16x32_bf16 v[8:11], v[144:147], a[136:139], 0\n"
    "ds_read_b64_tr_b16 v[192:193], v234 offset:16384\n"
    "v_mfma_f32_16x16x32_bf16 v[8:11], v[148:151], a[152:155], v[8:11]\n"
    "ds_read_b64_tr_b16 v[194:195], v234 offset:18432\n"
    "v_mfma_f32_16x16x32_bf16 v[8:11], v[152:155], a[168:171], v[8:11]\n"
    "ds_read_b64_tr_b16 v[196:197], v234 offset:20480\n"
    "v_mfma_f32_16x16x32_bf16 v[8:11], v[156:159], a[184:187], v[8:11]\n"
    "ds_read_b64_tr_b16 v[198:199], v234 offset:22528\n"
    "v_mfma_f32_16x16x32_bf16 v[8:11], v[160:163], a[8:11], v[8:11]\n"
    "ds_read_b64_tr_b16 v[200:201], v234 offset:24576\n"
    "v_mfma_f32_16x16x32_bf16 v[8:11], v[164:167], a[24:27], v[8:11]\n"
    "ds_read_b64_tr_b16 v[202:203], v234 offset:26624\n"
    "v_mfma_f32_16x16x32_bf16 v[8:11], v[168:171], a[40:43], v[8:11]\n"
    "ds_read_b64_tr_b16 v[204:205], v234 offset:28672\n"
    "v_mfma_f32_16x16x32_bf16 v[8:11], v[172:175], a[56:59], v[8:11]\n"
    "ds_read_b64_tr_b16 v[206:207], v234 offset:30720\n"
    "v_mfma_f32_16x16x32_bf16 v[12:15], v[144:147], a[140:143], 0\n"
    "buffer_load_dword v76, v76, s[52:55], s68 offen sc1\n"
    "v_mfma_f32_16x16x32_bf16 v[12:15], v[148:151], a[156:159], v[12:15]\n"
    "v_mfma_f32_16x16x32_bf16 v[12:15], v[152:155], a[172:175], v[12:15]\n"
    "v_mfma_f32_16x16x32_bf16 v[12:15], v[156:159], a[188:191], v[12:15]\n"
    "v_mfma_f32_16x16x32_bf16 v[12:15], v[160:163], a[12:15], v[12:15]\n"
    "v_mfma_f32_16x16x32_bf16 v[12:15], v[164:167], a[28:31], v[12:15]\n"
    "v_mfma_f32_16x16x32_bf16 v[12:15], v[168:171], a[44:47], v[12:15]\n"
    "v_mfma_f32_16x16x32_bf16 v[12:15], v[172:175], a[60:63], v[12:15]\n"
    "s_waitcnt lgkmcnt(0)\n"
    "v_mfma_f32_16x16x32_bf16 v[0:3], v[176:179], a[192:195], v[0:3]\n"
    "v_mfma_f32_16x16x32_bf16 v[0:3], v[180:183], a[208:211], v[0:3]\n"
    "v_mfma_f32_16x16x32_bf16 v[0:3], v[184:187], a[224:227], v[0:3]\n"
    "s_lshl_b32 s61, s67, 3\n"
    "s_add_u32 s61, s61, s66\n"
    "v_mfma_f32_16x16x32_bf16 v[0:3], v[188:191], a[240:243], v[0:3]\n"
    "s_lshl_b32 s61, s61, 2\n"
    "s_add_u32 s61, s61, %[wv]\n"
    "v_mfma_f32_16x16x32_bf16 v[0:3], v[192:195], a[64:67], v[0:3]\n"
    "s_lshl_b32 s61, s61, 2\n"
    "s_add_u32 s61, s61, 16384\n"
    "v_mfma_f32_16x16x32_bf16 v[0:3], v[196:199], a[80:83], v[0:3]\n"
    "v_mfma_f32_16x16x32_bf16 v[0:3], v[200:203], a[96:99], v[0:3]\n"
    "v_mfma_f32_16x16x32_bf16 v[0:3], v[204:207], a[112:115], v[0:3]\n"
    "v_mfma_f32_16x16x32_bf16 v[4:7], v[176:179], a[196:199], v[4:7]\n"
    "v_mfma_f32_16x16x32_bf16 v[4:7], v[180:183], a[212:215], v[4:7]\n"
    "v_mfma_f32_16x16x32_bf16 v[4:7], v[184:187], a[228:231], v[4:7]\n"
    "v_mfma_f32_16x16x32_bf16 v[4:7], v[188:191], a[244:247], v[4:7]\n"
    "v_mfma_f32_16x16x32_bf16 v[4:7], v[192:195], a[68:71], v[4:7]\n"
    "v_mfma_f32_16x16x32_bf16 v[4:7], v[196:199], a[84:87], v[4:7]\n"
    "v_mfma_f32_16x16x32_bf16 v[4:7], v[200:203], a[100:103], v[4:7]\n"
    "v_mfma_f32_16x16x32_bf16 v[4:7], v[204:207], a[116:119], v[4:7]\n"
    "v_mfma_f32_16x16x32_bf16 v[8:11], v[176:179], a[200:203], v[8:11]\n"
    "v_mfma_f32_16x16x32_bf16 v[8:11], v[180:183], a[216:219], v[8:11]\n"
    "v_mfma_f32_16x16x32_bf16 v[8:11], v[184:187], a[232:235], v[8:11]\n"
    "v_cvt_pk_bf16_f32 v96, v0, v1\n"
    "v_cvt_pk_bf16_f32 v98, v2, v3\n"
    "v_mfma_f32_16x16x32_bf16 v[8:11], v[188:191], a[248:251], v[8:11]\n"
    "v_cvt_pk_bf16_f32 v100, v4, v5\n"
    "v_cvt_pk_bf16_f32 v102, v6, v7\n"
    "v_mfma_f32_16x16x32_bf16 v[8:11], v[192:195], a[72:75], v[8:11]\n"
    "buffer_store_dwordx4 v[96:99], v240, s[76:79], s96 offen offset:1536 sc1\n"
    "v_mfma_f32_16x16x32_bf16 v[8:11], v[196:199], a[88:91], v[8:11]\n"
    "buffer_store_dwordx4 v[100:103], v240, s[76:79], s97 offen offset:1536 sc1\n"
    "v_mfma_f32_16x16x32_bf16 v[8:11], v[200:203], a[104:107], v[8:11]\n"
    "v_mfma_f32_16x16x32_bf16 v[8:11], v[204:207], a[120:123], v[8:11]\n"
    "v_mfma_f32_16x16x32_bf16 v[12:15], v[176:179], a[204:207], v[12:15]\n"
    "v_mfma_f32_16x16x32_bf16 v[12:15], v[180:183], a[220:223], v[12:15]\n"
    "v_mfma_f32_16x16x32_bf16 v[12:15], v[184:187], a[236:239], v[12:15]\n"
    "v_mfma_f32_16x16x32_bf16 v[12:15], v[188:191], a[252:255], v[12:15]\n"
    "v_cvt_pk_bf16_f32 v104, v8, v9\n"
    "v_cvt_pk_bf16_f32 v106, v10, v11\n"
    "v_mfma_f32_16x16x32_bf16 v[12:15], v[192:195], a[76:79], v[12:15]\n"
    "buffer_store_dwordx4 v[104:107], v240, s[76:79], s98 offen offset:1536 sc1\n"
    "v_mfma_f32_16x16x32_bf16 v[12:15], v[196:199], a[92:95], v[12:15]\n"
    "v_mfma_f32_16x16x32_bf16 v[12:15], v[200:203], a[108:111], v[12:15]\n"
    "v_mfma_f32_16x16x32_bf16 v[12:15], v[204:207], a[124:127], v[12:15]\n"
    "s_nop 7\n"
    "v_cvt_pk_bf16_f32 v108, v12, v13\n"
    "v_cvt_pk_bf16_f32 v110, v14, v15\n"
    "buffer_store_dwordx4 v[108:111], v240, s[76:79], s99 offen offset:1536 sc1\n"
    "s_lshr_b32 s68, s66, 1\n"
    "s_cmp_lg_u32 s68, 0\n"
    "s_cbranch_scc1 fb_Lvarown1\n"
    "ds_write_b128 v238, v[112:115] offset:0\n"
    "ds_write_b128 v238, v[128:131] offset:512\n"
    "ds_write_b128 v238, v[80:83] offset:1024\n"
    "ds_write_b128 v238, v[96:99] offset:1536\n"
    "s_branch fb_Lvarownend\n"
    "fb_Lvarown1:\n"
    "s_cmp_lg_u32 s68, 1\n"
    "s_cbranch_scc1 fb_Lvarown2\n"
    "ds_write_b128 v238, v[116:119] offset:0\n"
    "ds_write_b128 v238, v[132:135] offset:512\n"
    "ds_write_b128 v238, v[84:87] offset:1024\n"
    "ds_write_b128 v238, v[100:103] offset:1536\n"
    "s_branch fb_Lvarownend\n"
    "fb_Lvarown2:\n"
    "s_cmp_lg_u32 s68, 2\n"
    "s_cbranch_scc1 fb_Lvarown3\n"
    "ds_write_b128 v238, v[120:123] offset:0\n"
    "ds_write_b128 v238, v[136:139] offset:512\n"
    "ds_write_b128 v238, v[88:91] offset:1024\n"
    "ds_write_b128 v238, v[104:107] offset:1536\n"
    "s_branch fb_Lvarownend\n"
    "fb_Lvarown3:\n"
    "ds_write_b128 v238, v[124:127] offset:0\n"
    "ds_write_b128 v238, v[140:143] offset:512\n"
    "ds_write_b128 v238, v[92:95] offset:1024\n"
    "ds_write_b128 v238, v[108:111] offset:1536\n"
    "fb_Lvarownend:\n"
    "s_mov_b64 exec, 1\n"
    "s_waitcnt vmcnt(5)\n"
    "buffer_store_dword v229, off, s[52:55], s61 sc1\n"
    "s_mov_b64 exec, -1\n"
    "s_waitcnt vmcnt(5) lgkmcnt(0)\n"
    "s_barrier\n"
    "s_branch fb_Lend\n"
    "fb_Lslow4:\n"
    "s_call_b64 s[80:81], fb_Lfix1\n"
    "v_cvt_pk_bf16_f32 v60, v232, v233\n"
    "v_cvt_pk_bf16_f32 v61, v234, v235\n"
    "v_cvt_pk_bf16_f32 v76, v236, v237\n"
    "v_cvt_pk_bf16_f32 v77, v238, v239\n"
    "v_cvt_pk_bf16_f32 v62, v240, v241\n"
    "v_cvt_pk_bf16_f32 v63, v242, v243\n"
    "v_cvt_pk_bf16_f32 v78, v244, v245\n"
    "v_cvt_pk_bf16_f32 v79, v246, v247\n"
    "s_nop 1\n"
    "s_branch fb_Lback4\n"
    "fb_Lslow5:\n"
    "s_call_b64 s[80:81], fb_Lfix0\n"
    "v_cvt_pk_bf16_f32 v80, v232, v233\n"
    "v_cvt_pk_bf16_f32 v81, v234, v235\n"
    "v_cvt_pk_bf16_f32 v96, v236, v237\n"
    "v_cvt_pk_bf16_f32 v97, v238, v239\n"
    "v_cvt_pk_bf16_f32 v82, v240, v241\n"
    "v_cvt_pk_bf16_f32 v83, v242, v243\n"
    "v_cvt_pk_bf16_f32 v98, v244, v245\n"
    "v_cvt_pk_bf16_f32 v99, v246, v247\n"
    "s_nop 1\n"
    "s_branch fb_Lback5\n"
    "fb_Lslow6:\n"
    "s_call_b64 s[80:81], fb_Lfix1\n"
    "v_cvt_pk_bf16_f32 v84, v232, v233\n"
    "v_cvt_pk_bf16_f32 v85, v234, v235\n"
    "v_cvt_pk_bf16_f32 v100, v236, v237\n"
    "v_cvt_pk_bf16_f32 v101, v238, v239\n"
    "v_cvt_pk_bf16_f32 v86, v240, v241\n"
    "v_cvt_pk_bf16_f32 v87, v242, v243\n"
    "v_cvt_pk_bf16_f32 v102, v244, v245\n"
    "v_cvt_pk_bf16_f32 v103, v246, v247\n"
    "s_nop 1\n"
    "s_branch fb_Lback6\n"
    "fb_Lslow7:\n"
    "s_call_b64 s[80:81], fb_Lfix0\n"
    "v_cvt_pk_bf16_f32 v88, v232, v233\n"
    "v_cvt_pk_bf16_f32 v89, v234, v235\n"
    "v_cvt_pk_bf16_f32 v104, v236, v237\n"
    "v_cvt_pk_bf16_f32 v105, v238, v239\n"
    "v_cvt_pk_bf16_f32 v90, v240, v241\n"
    "v_cvt_pk_bf16_f32 v91, v242, v243\n"
    "v_cvt_pk_bf16_f32 v106, v244, v245\n"
    "v_cvt_pk_bf16_f32 v107, v246, v247\n"
    "s_nop 1\n"
    "s_branch fb_Lback7\n"
    "fb_Lslow8:\n"
    "s_call_b64 s[80:81], fb_Lfix1\n"
    "v_cvt_pk_bf16_f32 v92, v232, v233\n"
    "v_cvt_pk_bf16_f32 v93, v234, v235\n"
    "v_cvt_pk_bf16_f32 v108, v236, v237\n"
    "v_cvt_pk_bf16_f32 v109, v238, v239\n"
    "v_cvt_pk_bf16_f32 v94, v240, v241\n"
    "v_cvt_pk_bf16_f32 v95, v242, v243\n"
    "v_cvt_pk_bf16_f32 v110, v244, v245\n"
    "v_cvt_pk_bf16_f32 v111, v246, v247\n"
    "s_nop 1\n"
    "s_branch fb_Lback8\n"
    "fb_Lslow9:\n"
    "s_call_b64 s[80:81], fb_Lfix0\n"
    "v_cvt_pk_bf16_f32 v48, v232, v233\n"
    "v_cvt_pk_bf16_f32 v49, v234, v235\n"
    "v_cvt_pk_bf16_f32 v64, v236, v237\n"
    "v_cvt_pk_bf16_f32 v65, v238, v239\n"
    "v_cvt_pk_bf16_f32 v50, v240, v241\n"
    "v_cvt_pk_bf16_f32 v51, v242, v243\n"
    "v_cvt_pk_bf16_f32 v66, v244, v245\n"
    "v_cvt_pk_bf16_f32 v67, v246, v247\n"
    "s_nop 1\n"
    "s_branch fb_Lback9\n"
    "fb_Lslow10:\n"
    "s_call_b64 s[80:81], fb_Lfix1\n"
    "v_cvt_pk_bf16_f32 v52, v232, v233\n"
    "v_cvt_pk_bf16_f32 v53, v234, v235\n"
    "v_cvt_pk_bf16_f32 v68, v236, v237\n"
    "v_cvt_pk_bf16_f32 v69, v238, v239\n"
    "v_cvt_pk_bf16_f32 v54, v240, v241\n"
    "v_cvt_pk_bf16_f32 v55, v242, v243\n"
    "v_cvt_pk_bf16_f32 v70, v244, v245\n"
    "v_cvt_pk_bf16_f32 v71, v246, v247\n"
    "s_nop 1\n"
    "s_branch fb_Lback10\n"
    "fb_Lslow11:\n"
    "s_call_b64 s[80:81], fb_Lfix0\n"
    "v_cvt_pk_bf16_f32 v56, v232, v233\n"
    "v_cvt_pk_bf16_f32 v57, v234, v235\n"
    "v_cvt_pk_bf16_f32 v72, v236, v237\n"
    "v_cvt_pk_bf16_f32 v73, v238, v239\n"
    "v_cvt_pk_bf16_f32 v58, v240, v241\n"
    "v_cvt_pk_bf16_f32 v59, v242, v243\n"
    "v_cvt_pk_bf16_f32 v74, v244, v245\n"
    "v_cvt_pk_bf16_f32 v75, v246, v247\n"
    "s_nop 1\n"
    "s_branch fb_Lback11\n"
    "fb_Lslow60:\n"
    "s_call_b64 s[80:81], fb_Lfix1\n"
    "v_cvt_pk_bf16_f32 v60, v232, v233\n"
    "v_cvt_pk_bf16_f32 v61, v234, v235\n"
    "v_cvt_pk_bf16_f32 v76, v236, v237\n"
    "v_cvt_pk_bf16_f32 v77, v238, v239\n"
    "v_cvt_pk_bf16_f32 v62, v240, v241\n"
    "v_cvt_pk_bf16_f32 v63, v242, v243\n"
    "v_cvt_pk_bf16_f32 v78, v244, v245\n"
    "v_cvt_pk_bf16_f32 v79, v246, v247\n"
    "s_nop 1\n"
    "s_branch fb_Lback60\n"
    "fb_Lslow61:\n"
    "s_call_b64 s[80:81], fb_Lfix0\n"
    "v_cvt_pk_bf16_f32 v80, v232, v233\n"
    "v_cvt_pk_bf16_f32 v81, v234, v235\n"
    "v_cvt_pk_bf16_f32 v96, v236, v237\n"
    "v_cvt_pk_bf16_f32 v97, v238, v239\n"
    "v_cvt_pk_bf16_f32 v82, v240, v241\n"
    "v_cvt_pk_bf16_f32 v83, v242, v243\n"
    "v_cvt_pk_bf16_f32 v98, v244, v245\n"
    "v_cvt_pk_bf16_f32 v99, v246, v247\n"
    "s_nop 1\n"
    "s_branch fb_Lback61\n"
    "fb_Lslow62:\n"
    "s_call_b64 s[80:81], fb_Lfix1\n"
    "v_cvt_pk_bf16_f32 v84, v232, v233\n"
    "v_cvt_pk_bf16_f32 v85, v234, v235\n"
    "v_cvt_pk_bf16_f32 v100, v236, v237\n"
    "v_cvt_pk_bf16_f32 v101, v238, v239\n"
    "v_cvt_pk_bf16_f32 v86, v240, v241\n"
    "v_cvt_pk_bf16_f32 v87, v242, v243\n"
    "v_cvt_pk_bf16_f32 v102, v244, v245\n"
    "v_cvt_pk_bf16_f32 v103, v246, v247\n"
    "s_nop 1\n"
    "s_branch fb_Lback62\n"
    "fb_Lslow63:\n"
    "s_call_b64 s[80:81], fb_Lfix0\n"
    "v_cvt_pk_bf16_f32 v88, v232, v233\n"
    "v_cvt_pk_bf16_f32 v89, v234, v235\n"
    "v_cvt_pk_bf16_f32 v104, v236, v237\n"
    "v_cvt_pk_bf16_f32 v105, v238, v239\n"
    "v_cvt_pk_bf16_f32 v90, v240, v241\n"
    "v_cvt_pk_bf16_f32 v91, v242, v243\n"
    "v_cvt_pk_bf16_f32 v106, v244, v245\n"
    "v_cvt_pk_bf16_f32 v107, v246, v247\n"
    "s_nop 1\n"
    "s_branch fb_Lback63\n"
    "fb_Lslow64:\n"
    "s_call_b64 s[80:81], fb_Lfix1\n"
    "v_cvt_pk_bf16_f32 v92, v232, v233\n"
    "v_cvt_pk_bf16_f32 v93, v234, v235\n"
    "v_cvt_pk_bf16_f32 v108, v236, v237\n"
    "v_cvt_pk_bf16_f32 v109, v238, v239\n"
    "v_cvt_pk_bf16_f32 v94, v240, v241\n"
    "v_cvt_pk_bf16_f32 v95, v242, v243\n"
    "v_cvt_pk_bf16_f32 v110, v244, v245\n"
    "v_cvt_pk_bf16_f32 v111, v246, v247\n"
    "s_nop 1\n"
    "s_branch fb_Lback64\n"
    "fb_Lfix0:\n"
    "s_nop 15\n"
    "v_max3_f32 v230, v0, v1, v2\n"
    "v_max3_f32 v42, v3, v8, v9\n"
    "v_max3_f32 v231, v4, v5, v6\n"
    "v_max3_f32 v43, v7, v12, v13\n"
    "v_max3_f32 v230, v230, v10, v11\n"
    "v_max3_f32 v231, v231, v14, v15\n"
    "v_max_f32 v230, v230, v42\n"
    "v_max_f32 v231, v231, v43\n"
    "v_mov_b32 v42, v230\n"
    "v_mov_b32 v43, v231\n"
    "s_nop 0\n"
    "v_permlane32_swap_b32 v230, v42\n"
    "v_permlane32_swap_b32 v231, v43\n"
    "v_max_f32 v230, v230, v42\n"
    "v_max_f32 v231, v231, v43\n"
    "v_mov_b32 v42, v230\n"
    "v_mov_b32 v43, v231\n"
    "s_nop 0\n"
    "v_permlane16_swap_b32 v230, v42\n"
    "v_permlane16_swap_b32 v231, v43\n"
    "v_max_f32 v230, v230, v42\n"
    "v_max_f32 v231, v231, v43\n"
    "v_add_f32 v230, 0x42c80000, v230\n"
    "v_ceil_f32 v230, v230\n"
    "v_max_f32 v230, 0, v230\n"
    "v_sub_f32 v40, 0, v230\n"
    "v_exp_f32 v40, v40\n"
    "v_sub_f32 v32, v32, v230\n"
    "v_sub_f32 v33, v33, v230\n"
    "v_sub_f32 v34, v34, v230\n"
    "v_sub_f32 v35, v35, v230\n"
    "v_add_f32 v231, 0x42c80000, v231\n"
    "v_ceil_f32 v231, v231\n"
    "v_max_f32 v231, 0, v231\n"
    "v_sub_f32 v41, 0, v231\n"
    "v_exp_f32 v41, v41\n"
    "v_sub_f32 v36, v36, v231\n"
    "v_sub_f32 v37, v37, v231\n"
    "v_sub_f32 v38, v38, v231\n"
    "v_sub_f32 v39, v39, v231\n"
    "v_sub_f32 v232, v0, v230\n"
    "v_sub_f32 v233, v1, v230\n"
    "v_sub_f32 v234, v2, v230\n"
    "v_sub_f32 v235, v3, v230\n"
    "v_sub_f32 v236, v4, v231\n"
    "v_sub_f32 v237, v5, v231\n"
    "v_sub_f32 v238, v6, v231\n"
    "v_sub_f32 v239, v7, v231\n"
    "v_sub_f32 v240, v8, v230\n"
    "v_sub_f32 v241, v9, v230\n"
    "v_sub_f32 v242, v10, v230\n"
    "v_sub_f32 v243, v11, v230\n"
    "v_sub_f32 v244, v12, v231\n"
    "v_sub_f32 v245, v13, v231\n"
    "v_sub_f32 v246, v14, v231\n"
    "v_sub_f32 v247, v15, v231\n"
    "v_exp_f32 v232, v232\n"
    "v_exp_f32 v233, v233\n"
    "v_exp_f32 v234, v234\n"
    "v_exp_f32 v235, v235\n"
    "v_exp_f32 v236, v236\n"
    "v_exp_f32 v237, v237\n"
    "v_exp_f32 v238, v238\n"
    "v_exp_f32 v239, v239\n"
    "v_exp_f32 v240, v240\n"
    "v_exp_f32 v241, v241\n"
    "v_exp_f32 v242, v242\n"
    "v_exp_f32 v243, v243\n"
    "v_exp_f32 v244, v244\n"
    "v_exp_f32 v245, v245\n"
    "v_exp_f32 v246, v246\n"
    "v_exp_f32 v247, v247\n"
    "v_sub_f32 v16, v16, v230\n"
    "v_sub_f32 v17, v17, v230\n"
    "v_sub_f32 v18, v18, v230\n"
    "v_sub_f32 v19, v19, v230\n"
    "s_branch fb_Lscale\n"
    "fb_Lfix1:\n"
    "s_nop 15\n"
    "v_max3_f32 v230, v16, v17, v18\n"
    "v_max3_f32 v42, v19, v24, v25\n"
    "v_max3_f32 v231, v20, v21, v22\n"
    "v_max3_f32 v43, v23, v28, v29\n"
    "v_max3_f32 v230, v230, v26, v27\n"
    "v_max3_f32 v231, v231, v30, v31\n"
    "v_max_f32 v230, v230, v42\n"
    "v_max_f32 v231, v231, v43\n"
    "v_mov_b32 v42, v230\n"
    "v_mov_b32 v43, v231\n"
    "s_nop 0\n"
    "v_permlane32_swap_b32 v230, v42\n"
    "v_permlane32_swap_b32 v231, v43\n"
    "v_max_f32 v230, v230, v42\n"
    "v_max_f32 v231, v231, v43\n"
    "v_mov_b32 v42, v230\n"
    "v_mov_b32 v43, v231\n"
    "s_nop 0\n"
    "v_permlane16_swap_b32 v230, v42\n"
    "v_permlane16_swap_b32 v231, v43\n"
    "v_max_f32 v230, v230, v42\n"
    "v_max_f32 v231, v231, v43\n"
    "v_add_f32 v230, 0x42c80000, v230\n"
    "v_ceil_f32 v230, v230\n"
    "v_max_f32 v230, 0, v230\n"
    "v_sub_f32 v40, 0, v230\n"
    "v_exp_f32 v40, v40\n"
    "v_sub_f32 v32, v32, v230\n"
    "v_sub_f32 v33, v33, v230\n"
    "v_sub_f32 v34, v34, v230\n"
    "v_sub_f32 v35, v35, v230\n"
    "v_add_f32 v231, 0x42c80000, v231\n"
    "v_ceil_f32 v231, v231\n"
    "v_max_f32 v231, 0, v231\n"
    "v_sub_f32 v41, 0, v231\n"
    "v_exp_f32 v41, v41\n"
    "v_sub_f32 v36, v36, v231\n"
    "v_sub_f32 v37, v37, v231\n"
    "v_sub_f32 v38, v38, v231\n"
    "v_sub_f32 v39, v39, v231\n"
    "v_sub_f32 v232, v16, v230\n"
    "v_sub_f32 v233, v17, v230\n"
    "v_sub_f32 v234, v18, v230\n"
    "v_sub_f32 v235, v19, v230\n"
    "v_sub_f32 v236, v20, v231\n"
    "v_sub_f32 v237, v21, v231\n"
    "v_sub_f32 v238, v22, v231\n"
    "v_sub_f32 v239, v23, v231\n"
    "v_sub_f32 v240, v24, v230\n"
    "v_sub_f32 v241, v25, v230\n"
    "v_sub_f32 v242, v26, v230\n"
    "v_sub_f32 v243, v27, v230\n"
    "v_sub_f32 v244, v28, v231\n"
    "v_sub_f32 v245, v29, v231\n"
    "v_sub_f32 v246, v30, v231\n"
    "v_sub_f32 v247, v31, v231\n"
    "v_exp_f32 v232, v232\n"
    "v_exp_f32 v233, v233\n"
    "v_exp_f32 v234, v234\n"
    "v_exp_f32 v235, v235\n"
    "v_exp_f32 v236, v236\n"
    "v_exp_f32 v237, v237\n"
    "v_exp_f32 v238, v238\n"
    "v_exp_f32 v239, v239\n"
    "v_exp_f32 v240, v240\n"
    "v_exp_f32 v241, v241\n"
    "v_exp_f32 v242, v242\n"
    "v_exp_f32 v243, v243\n"
    "v_exp_f32 v244, v244\n"
    "v_exp_f32 v245, v245\n"
    "v_exp_f32 v246, v246\n"
    "v_exp_f32 v247, v247\n"
    "v_sub_f32 v0, v0, v230\n"
    "v_sub_f32 v1, v1, v230\n"
    "v_sub_f32 v2, v2, v230\n"
    "v_sub_f32 v3, v3, v230\n"
    "fb_Lscale:\n"
    "s_mov_b32 s69, 0xffff0000\n"
    "v_accvgpr_read_b32 v42, a0\n"
    "v_mul_f32 v42, v42, v40\n"
    "v_accvgpr_write_b32 a0, v42\n"
    "v_accvgpr_read_b32 v43, a1\n"
    "v_mul_f32 v43, v43, v40\n"
    "v_accvgpr_write_b32 a1, v43\n"
    "v_accvgpr_read_b32 v44, a2\n"
    "v_mul_f32 v44, v44, v40\n"
    "v_accvgpr_write_b32 a2, v44\n"
    "v_accvgpr_read_b32 v45, a3\n"
    "v_mul_f32 v45, v45, v40\n"
    "v_accvgpr_write_b32 a3, v45\n"
    "v_accvgpr_read_b32 v42, a4\n"
    "v_mul_f32 v42, v42, v41\n"
    "v_accvgpr_write_b32 a4, v42\n"
    "v_accvgpr_read_b32 v43, a5\n"
    "v_mul_f32 v43, v43, v41\n"
    "v_accvgpr_write_b32 a5, v43\n"
    "v_accvgpr_read_b32 v44, a6\n"
    "v_mul_f32 v44, v44, v41\n"
    "v_accvgpr_write_b32 a6, v44\n"
    "v_accvgpr_read_b32 v45, a7\n"
    "v_mul_f32 v45, v45, v41\n"
    "v_accvgpr_write_b32 a7, v45\n"
    "v_accvgpr_read_b32 v42, a8\n"
    "v_mul_f32 v42, v42, v40\n"
    "v_accvgpr_write_b32 a8, v42\n"
    "v_accvgpr_read_b32 v43, a9\n"
    "v_mul_f32 v43, v43, v40\n"
    "v_accvgpr_write_b32 a9, v43\n"
    "v_accvgpr_read_b32 v44, a10\n"
    "v_mul_f32 v44, v44, v40\n"
    "v_accvgpr_write_b32 a10, v44\n"
    "v_accvgpr_read_b32 v45, a11\n"
    "v_mul_f32 v45, v45, v40\n"
    "v_accvgpr_write_b32 a11, v45\n"
    "v_accvgpr_read_b32 v42, a12\n"
    "v_mul_f32 v42, v42, v41\n"
    "v_accvgpr_write_b32 a12, v42\n"
    "v_accvgpr_read_b32 v43, a13\n"
    "v_mul_f32 v43, v43, v41\n"
    "v_accvgpr_write_b32 a13, v43\n"
    "v_accvgpr_read_b32 v44, a14\n"
    "v_mul_f32 v44, v44, v41\n"
    "v_accvgpr_write_b32 a14, v44\n"
    "v_accvgpr_read_b32 v45, a15\n"
    "v_mul_f32 v45, v45, v41\n"
    "v_accvgpr_write_b32 a15, v45\n"
    "v_accvgpr_read_b32 v42, a16\n"
    "v_mul_f32 v42, v42, v40\n"
    "v_accvgpr_write_b32 a16, v42\n"
    "v_accvgpr_read_b32 v43, a17\n"
    "v_mul_f32 v43, v43, v40\n"
    "v_accvgpr_write_b32 a17, v43\n"
    "v_accvgpr_read_b32 v44, a18\n"
    "v_mul_f32 v44, v44, v40\n"
    "v_accvgpr_write_b32 a18, v44\n"
    "v_accvgpr_read_b32 v45, a19\n"
    "v_mul_f32 v45, v45, v40\n"
    "v_accvgpr_write_b32 a19, v45\n"
    "v_accvgpr_read_b32 v42, a20\n"
    "v_mul_f32 v42, v42, v41\n"
    "v_accvgpr_write_b32 a20, v42\n"
    "v_accvgpr_read_b32 v43, a21\n"
    "v_mul_f32 v43, v43, v41\n"
    "v_accvgpr_write_b32 a21, v43\n"
    "v_accvgpr_read_b32 v44, a22\n"
    "v_mul_f32 v44, v44, v41\n"
    "v_accvgpr_write_b32 a22, v44\n"
    "v_accvgpr_read_b32 v45, a23\n"
    "v_mul_f32 v45, v45, v41\n"
    "v_accvgpr_write_b32 a23, v45\n"
    "v_accvgpr_read_b32 v42, a24\n"
    "v_mul_f32 v42, v42, v40\n"
    "v_accvgpr_write_b32 a24, v42\n"
    "v_accvgpr_read_b32 v43, a25\n"
    "v_mul_f32 v43, v43, v40\n"
    "v_accvgpr_write_b32 a25, v43\n"
    "v_accvgpr_read_b32 v44, a26\n"
    "v_mul_f32 v44, v44, v40\n"
    "v_accvgpr_write_b32 a26, v44\n"
    "v_accvgpr_read_b32 v45, a27\n"
    "v_mul_f32 v45, v45, v40\n"
    "v_accvgpr_write_b32 a27, v45\n"
    "v_accvgpr_read_b32 v42, a28\n"
    "v_mul_f32 v42, v42, v41\n"
    "v_accvgpr_write_b32 a28, v42\n"
    "v_accvgpr_read_b32 v43, a29\n"
    "v_mul_f32 v43, v43, v41\n"
    "v_accvgpr_write_b32 a29, v43\n"
    "v_accvgpr_read_b32 v44, a30\n"
    "v_mul_f32 v44, v44, v41\n"
    "v_accvgpr_write_b32 a30, v44\n"
    "v_accvgpr_read_b32 v45, a31\n"
    "v_mul_f32 v45, v45, v41\n"
    "v_accvgpr_write_b32 a31, v45\n"
    "v_accvgpr_read_b32 v42, a32\n"
    "v_mul_f32 v42, v42, v40\n"
    "v_accvgpr_write_b32 a32, v42\n"
    "v_accvgpr_read_b32 v43, a33\n"
    "v_mul_f32 v43, v43, v40\n"
    "v_accvgpr_write_b32 a33, v43\n"
    "v_accvgpr_read_b32 v44, a34\n"
    "v_mul_f32 v44, v44, v40\n"
    "v_accvgpr_write_b32 a34, v44\n"
    "v_accvgpr_read_b32 v45, a35\n"
    "v_mul_f32 v45, v45, v40\n"
    "v_accvgpr_write_b32 a35, v45\n"
    "v_accvgpr_read_b32 v42, a36\n"
    "v_mul_f32 v42, v42, v41\n"
    "v_accvgpr_write_b32 a36, v42\n"
    "v_accvgpr_read_b32 v43, a37\n"
    "v_mul_f32 v43, v43, v41\n"
    "v_accvgpr_write_b32 a37, v43\n"
    "v_accvgpr_read_b32 v44, a38\n"
    "v_mul_f32 v44, v44, v41\n"
    "v_accvgpr_write_b32 a38, v44\n"
    "v_accvgpr_read_b32 v45, a39\n"
    "v_mul_f32 v45, v45, v41\n"
    "v_accvgpr_write_b32 a39, v45\n"
    "v_accvgpr_read_b32 v42, a40\n"
    "v_mul_f32 v42, v42, v40\n"
    "v_accvgpr_write_b32 a40, v42\n"
    "v_accvgpr_read_b32 v43, a41\n"
    "v_mul_f32 v43, v43, v40\n"
    "v_accvgpr_write_b32 a41, v43\n"
    "v_accvgpr_read_b32 v44, a42\n"
    "v_mul_f32 v44, v44, v40\n"
    "v_accvgpr_write_b32 a42, v44\n"
    "v_accvgpr_read_b32 v45, a43\n"
    "v_mul_f32 v45, v45, v40\n"
    "v_accvgpr_write_b32 a43, v45\n"
    "v_accvgpr_read_b32 v42, a44\n"
    "v_mul_f32 v42, v42, v41\n"
    "v_accvgpr_write_b32 a44, v42\n"
    "v_accvgpr_read_b32 v43, a45\n"
    "v_mul_f32 v43, v43, v41\n"
    "v_accvgpr_write_b32 a45, v43\n"
    "v_accvgpr_read_b32 v44, a46\n"
    "v_mul_f32 v44, v44, v41\n"
    "v_accvgpr_write_b32 a46, v44\n"
    "v_accvgpr_read_b32 v45, a47\n"
    "v_mul_f32 v45, v45, v41\n"
    "v_accvgpr_write_b32 a47, v45\n"
    "v_accvgpr_read_b32 v42, a48\n"
    "v_mul_f32 v42, v42, v40\n"
    "v_accvgpr_write_b32 a48, v42\n"
    "v_accvgpr_read_b32 v43, a49\n"
    "v_mul_f32 v43, v43, v40\n"
    "v_accvgpr_write_b32 a49, v43\n"
    "v_accvgpr_read_b32 v44, a50\n"
    "v_mul_f32 v44, v44, v40\n"
    "v_accvgpr_write_b32 a50, v44\n"
    "v_accvgpr_read_b32 v45, a51\n"
    "v_mul_f32 v45, v45, v40\n"
    "v_accvgpr_write_b32 a51, v45\n"
    "v_accvgpr_read_b32 v42, a52\n"
    "v_mul_f32 v42, v42, v41\n"
    "v_accvgpr_write_b32 a52, v42\n"
    "v_accvgpr_read_b32 v43, a53\n"
    "v_mul_f32 v43, v43, v41\n"
    "v_accvgpr_write_b32 a53, v43\n"
    "v_accvgpr_read_b32 v44, a54\n"
    "v_mul_f32 v44, v44, v41\n"
    "v_accvgpr_write_b32 a54, v44\n"
    "v_accvgpr_read_b32 v45, a55\n"
    "v_mul_f32 v45, v45, v41\n"
    "v_accvgpr_write_b32 a55, v45\n"
    "v_accvgpr_read_b32 v42, a56\n"
    "v_mul_f32 v42, v42, v40\n"
    "v_accvgpr_write_b32 a56, v42\n"
    "v_accvgpr_read_b32 v43, a57\n"
    "v_mul_f32 v43, v43, v40\n"
    "v_accvgpr_write_b32 a57, v43\n"
    "v_accvgpr_read_b32 v44, a58\n"
    "v_mul_f32 v44, v44, v40\n"
    "v_accvgpr_write_b32 a58, v44\n"
    "v_accvgpr_read_b32 v45, a59\n"
    "v_mul_f32 v45, v45, v40\n"
    "v_accvgpr_write_b32 a59, v45\n"
    "v_accvgpr_read_b32 v42, a60\n"
    "v_mul_f32 v42, v42, v41\n"
    "v_accvgpr_write_b32 a60, v42\n"
    "v_accvgpr_read_b32 v43, a61\n"
    "v_mul_f32 v43, v43, v41\n"
    "v_accvgpr_write_b32 a61, v43\n"
    "v_accvgpr_read_b32 v44, a62\n"
    "v_mul_f32 v44, v44, v41\n"
    "v_accvgpr_write_b32 a62, v44\n"
    "v_accvgpr_read_b32 v45, a63\n"
    "v_mul_f32 v45, v45, v41\n"
    "v_accvgpr_write_b32 a63, v45\n"
    "v_accvgpr_read_b32 v42, a64\n"
    "v_mul_f32 v42, v42, v40\n"
    "v_accvgpr_write_b32 a64, v42\n"
    "v_accvgpr_read_b32 v43, a65\n"
    "v_mul_f32 v43, v43, v40\n"
    "v_accvgpr_write_b32 a65, v43\n"
    "v_accvgpr_read_b32 v44, a66\n"
    "v_mul_f32 v44, v44, v40\n"
    "v_accvgpr_write_b32 a66, v44\n"
    "v_accvgpr_read_b32 v45, a67\n"
    "v_mul_f32 v45, v45, v40\n"
    "v_accvgpr_write_b32 a67, v45\n"
    "v_accvgpr_read_b32 v42, a68\n"
    "v_mul_f32 v42, v42, v41\n"
    "v_accvgpr_write_b32 a68, v42\n"
    "v_accvgpr_read_b32 v43, a69\n"
    "v_mul_f32 v43, v43, v41\n"
    "v_accvgpr_write_b32 a69, v43\n"
    "v_accvgpr_read_b32 v44, a70\n"
    "v_mul_f32 v44, v44, v41\n"
    "v_accvgpr_write_b32 a70, v44\n"
    "v_accvgpr_read_b32 v45, a71\n"
    "v_mul_f32 v45, v45, v41\n"
    "v_accvgpr_write_b32 a71, v45\n"
    "v_accvgpr_read_b32 v42, a72\n"
    "v_mul_f32 v42, v42, v40\n"
    "v_accvgpr_write_b32 a72, v42\n"
    "v_accvgpr_read_b32 v43, a73\n"
    "v_mul_f32 v43, v43, v40\n"
    "v_accvgpr_write_b32 a73, v43\n"
    "v_accvgpr_read_b32 v44, a74\n"
    "v_mul_f32 v44, v44, v40\n"
    "v_accvgpr_write_b32 a74, v44\n"
    "v_accvgpr_read_b32 v45, a75\n"
    "v_mul_f32 v45, v45, v40\n"
    "v_accvgpr_write_b32 a75, v45\n"
    "v_accvgpr_read_b32 v42, a76\n"
    "v_mul_f32 v42, v42, v41\n"
    "v_accvgpr_write_b32 a76, v42\n"
    "v_accvgpr_read_b32 v43, a77\n"
    "v_mul_f32 v43, v43, v41\n"
    "v_accvgpr_write_b32 a77, v43\n"
    "v_accvgpr_read_b32 v44, a78\n"
    "v_mul_f32 v44, v44, v41\n"
    "v_accvgpr_write_b32 a78, v44\n"
    "v_accvgpr_read_b32 v45, a79\n"
    "v_mul_f32 v45, v45, v41\n"
    "v_accvgpr_write_b32 a79, v45\n"
    "v_accvgpr_read_b32 v42, a80\n"
    "v_mul_f32 v42, v42, v40\n"
    "v_accvgpr_write_b32 a80, v42\n"
    "v_accvgpr_read_b32 v43, a81\n"
    "v_mul_f32 v43, v43, v40\n"
    "v_accvgpr_write_b32 a81, v43\n"
    "v_accvgpr_read_b32 v44, a82\n"
    "v_mul_f32 v44, v44, v40\n"
    "v_accvgpr_write_b32 a82, v44\n"
    "v_accvgpr_read_b32 v45, a83\n"
    "v_mul_f32 v45, v45, v40\n"
    "v_accvgpr_write_b32 a83, v45\n"
    "v_accvgpr_read_b32 v42, a84\n"
    "v_mul_f32 v42, v42, v41\n"
    "v_accvgpr_write_b32 a84, v42\n"
    "v_accvgpr_read_b32 v43, a85\n"
    "v_mul_f32 v43, v43, v41\n"
    "v_accvgpr_write_b32 a85, v43\n"
    "v_accvgpr_read_b32 v44, a86\n"
    "v_mul_f32 v44, v44, v41\n"
    "v_accvgpr_write_b32 a86, v44\n"
    "v_accvgpr_read_b32 v45, a87\n"
    "v_mul_f32 v45, v45, v41\n"
    "v_accvgpr_write_b32 a87, v45\n"
    "v_accvgpr_read_b32 v42, a88\n"
    "v_mul_f32 v42, v42, v40\n"
    "v_accvgpr_write_b32 a88, v42\n"
    "v_accvgpr_read_b32 v43, a89\n"
    "v_mul_f32 v43, v43, v40\n"
    "v_accvgpr_write_b32 a89, v43\n"
    "v_accvgpr_read_b32 v44, a90\n"
    "v_mul_f32 v44, v44, v40\n"
    "v_accvgpr_write_b32 a90, v44\n"
    "v_accvgpr_read_b32 v45, a91\n"
    "v_mul_f32 v45, v45, v40\n"
    "v_accvgpr_write_b32 a91, v45\n"
    "v_accvgpr_read_b32 v42, a92\n"
    "v_mul_f32 v42, v42, v41\n"
    "v_accvgpr_write_b32 a92, v42\n"
    "v_accvgpr_read_b32 v43, a93\n"
    "v_mul_f32 v43, v43, v41\n"
    "v_accvgpr_write_b32 a93, v43\n"
    "v_accvgpr_read_b32 v44, a94\n"
    "v_mul_f32 v44, v44, v41\n"
    "v_accvgpr_write_b32 a94, v44\n"
    "v_accvgpr_read_b32 v45, a95\n"
    "v_mul_f32 v45, v45, v41\n"
    "v_accvgpr_write_b32 a95, v45\n"
    "v_accvgpr_read_b32 v42, a96\n"
    "v_mul_f32 v42, v42, v40\n"
    "v_accvgpr_write_b32 a96, v42\n"
    "v_accvgpr_read_b32 v43, a97\n"
    "v_mul_f32 v43, v43, v40\n"
    "v_accvgpr_write_b32 a97, v43\n"
    "v_accvgpr_read_b32 v44, a98\n"
    "v_mul_f32 v44, v44, v40\n"
    "v_accvgpr_write_b32 a98, v44\n"
    "v_accvgpr_read_b32 v45, a99\n"
    "v_mul_f32 v45, v45, v40\n"
    "v_accvgpr_write_b32 a99, v45\n"
    "v_accvgpr_read_b32 v42, a100\n"
    "v_mul_f32 v42, v42, v41\n"
    "v_accvgpr_write_b32 a100, v42\n"
    "v_accvgpr_read_b32 v43, a101\n"
    "v_mul_f32 v43, v43, v41\n"
    "v_accvgpr_write_b32 a101, v43\n"
    "v_accvgpr_read_b32 v44, a102\n"
    "v_mul_f32 v44, v44, v41\n"
    "v_accvgpr_write_b32 a102, v44\n"
    "v_accvgpr_read_b32 v45, a103\n"
    "v_mul_f32 v45, v45, v41\n"
    "v_accvgpr_write_b32 a103, v45\n"
    "v_accvgpr_read_b32 v42, a104\n"
    "v_mul_f32 v42, v42, v40\n"
    "v_accvgpr_write_b32 a104, v42\n"
    "v_accvgpr_read_b32 v43, a105\n"
    "v_mul_f32 v43, v43, v40\n"
    "v_accvgpr_write_b32 a105, v43\n"
    "v_accvgpr_read_b32 v44, a106\n"
    "v_mul_f32 v44, v44, v40\n"
    "v_accvgpr_write_b32 a106, v44\n"
    "v_accvgpr_read_b32 v45, a107\n"
    "v_mul_f32 v45, v45, v40\n"
    "v_accvgpr_write_b32 a107, v45\n"
    "v_accvgpr_read_b32 v42, a108\n"
    "v_mul_f32 v42, v42, v41\n"
    "v_accvgpr_write_b32 a108, v42\n"
    "v_accvgpr_read_b32 v43, a109\n"
    "v_mul_f32 v43, v43, v41\n"
    "v_accvgpr_write_b32 a109, v43\n"
    "v_accvgpr_read_b32 v44, a110\n"
    "v_mul_f32 v44, v44, v41\n"
    "v_accvgpr_write_b32 a110, v44\n"
    "v_accvgpr_read_b32 v45, a111\n"
    "v_mul_f32 v45, v45, v41\n"
    "v_accvgpr_write_b32 a111, v45\n"
    "v_accvgpr_read_b32 v42, a112\n"
    "v_mul_f32 v42, v42, v40\n"
    "v_accvgpr_write_b32 a112, v42\n"
    "v_accvgpr_read_b32 v43, a113\n"
    "v_mul_f32 v43, v43, v40\n"
    "v_accvgpr_write_b32 a113, v43\n"
    "v_accvgpr_read_b32 v44, a114\n"
    "v_mul_f32 v44, v44, v40\n"
    "v_accvgpr_write_b32 a114, v44\n"
    "v_accvgpr_read_b32 v45, a115\n"
    "v_mul_f32 v45, v45, v40\n"
    "v_accvgpr_write_b32 a115, v45\n"
    "v_accvgpr_read_b32 v42, a116\n"
    "v_mul_f32 v42, v42, v41\n"
    "v_accvgpr_write_b32 a116, v42\n"
    "v_accvgpr_read_b32 v43, a117\n"
    "v_mul_f32 v43, v43, v41\n"
    "v_accvgpr_write_b32 a117, v43\n"
    "v_accvgpr_read_b32 v44, a118\n"
    "v_mul_f32 v44, v44, v41\n"
    "v_accvgpr_write_b32 a118, v44\n"
    "v_accvgpr_read_b32 v45, a119\n"
    "v_mul_f32 v45, v45, v41\n"
    "v_accvgpr_write_b32 a119, v45\n"
    "v_accvgpr_read_b32 v42, a120\n"
    "v_mul_f32 v42, v42, v40\n"
    "v_accvgpr_write_b32 a120, v42\n"
    "v_accvgpr_read_b32 v43, a121\n"
    "v_mul_f32 v43, v43, v40\n"
    "v_accvgpr_write_b32 a121, v43\n"
    "v_accvgpr_read_b32 v44, a122\n"
    "v_mul_f32 v44, v44, v40\n"
    "v_accvgpr_write_b32 a122, v44\n"
    "v_accvgpr_read_b32 v45, a123\n"
    "v_mul_f32 v45, v45, v40\n"
    "v_accvgpr_write_b32 a123, v45\n"
    "v_accvgpr_read_b32 v42, a124\n"
    "v_mul_f32 v42, v42, v41\n"
    "v_accvgpr_write_b32 a124, v42\n"
    "v_accvgpr_read_b32 v43, a125\n"
    "v_mul_f32 v43, v43, v41\n"
    "v_accvgpr_write_b32 a125, v43\n"
    "v_accvgpr_read_b32 v44, a126\n"
    "v_mul_f32 v44, v44, v41\n"
    "v_accvgpr_write_b32 a126, v44\n"
    "v_accvgpr_read_b32 v45, a127\n"
    "v_mul_f32 v45, v45, v41\n"
    "v_accvgpr_write_b32 a127, v45\n"
    "v_accvgpr_read_b32 v42, a192\n"
    "v_mul_f32 v42, v42, v40\n"
    "v_accvgpr_write_b32 a192, v42\n"
    "v_accvgpr_read_b32 v43, a193\n"
    "v_mul_f32 v43, v43, v40\n"
    "v_accvgpr_write_b32 a193, v43\n"
    "v_accvgpr_read_b32 v44, a194\n"
    "v_mul_f32 v44, v44, v40\n"
    "v_accvgpr_write_b32 a194, v44\n"
    "v_accvgpr_read_b32 v45, a195\n"
    "v_mul_f32 v45, v45, v40\n"
    "v_accvgpr_write_b32 a195, v45\n"
    "v_accvgpr_read_b32 v42, a196\n"
    "v_mul_f32 v42, v42, v41\n"
    "v_accvgpr_write_b32 a196, v42\n"
    "v_accvgpr_read_b32 v43, a197\n"
    "v_mul_f32 v43, v43, v41\n"
    "v_accvgpr_write_b32 a197, v43\n"
    "v_accvgpr_read_b32 v44, a198\n"
    "v_mul_f32 v44, v44, v41\n"
    "v_accvgpr_write_b32 a198, v44\n"
    "v_accvgpr_read_b32 v45, a199\n"
    "v_mul_f32 v45, v45, v41\n"
    "v_accvgpr_write_b32 a199, v45\n"
    "v_lshlrev_b32 v42, 16, v48\n"
    "v_and_b32 v43, s69, v48\n"
    "v_mul_f32 v42, v42, v40\n"
    "v_mul_f32 v43, v43, v40\n"
    "v_cvt_pk_bf16_f32 v48, v42, v43\n"
    "v_lshlrev_b32 v44, 16, v49\n"
    "v_and_b32 v45, s69, v49\n"
    "v_mul_f32 v44, v44, v40\n"
    "v_mul_f32 v45, v45, v40\n"
    "v_cvt_pk_bf16_f32 v49, v44, v45\n"
    "v_lshlrev_b32 v42, 16, v50\n"
    "v_and_b32 v43, s69, v50\n"
    "v_mul_f32 v42, v42, v40\n"
    "v_mul_f32 v43, v43, v40\n"
    "v_cvt_pk_bf16_f32 v50, v42, v43\n"
    "v_lshlrev_b32 v44, 16, v51\n"
    "v_and_b32 v45, s69, v51\n"
    "v_mul_f32 v44, v44, v40\n"
    "v_mul_f32 v45, v45, v40\n"
    "v_cvt_pk_bf16_f32 v51, v44, v45\n"
    "v_lshlrev_b32 v42, 16, v52\n"
    "v_and_b32 v43, s69, v52\n"
    "v_mul_f32 v42, v42, v40\n"
    "v_mul_f32 v43, v43, v40\n"
    "v_cvt_pk_bf16_f32 v52, v42, v43\n"
    "v_lshlrev_b32 v44, 16, v53\n"
    "v_and_b32 v45, s69, v53\n"
    "v_mul_f32 v44, v44, v40\n"
    "v_mul_f32 v45, v45, v40\n"
    "v_cvt_pk_bf16_f32 v53, v44, v45\n"
    "v_lshlrev_b32 v42, 16, v54\n"
    "v_and_b32 v43, s69, v54\n"
    "v_mul_f32 v42, v42, v40\n"
    "v_mul_f32 v43, v43, v40\n"
    "v_cvt_pk_bf16_f32 v54, v42, v43\n"
    "v_lshlrev_b32 v44, 16, v55\n"
    "v_and_b32 v45, s69, v55\n"
    "v_mul_f32 v44, v44, v40\n"
    "v_mul_f32 v45, v45, v40\n"
    "v_cvt_pk_bf16_f32 v55, v44, v45\n"
    "v_lshlrev_b32 v42, 16, v56\n"
    "v_and_b32 v43, s69, v56\n"
    "v_mul_f32 v42, v42, v40\n"
    "v_mul_f32 v43, v43, v40\n"
    "v_cvt_pk_bf16_f32 v56, v42, v43\n"
    "v_lshlrev_b32 v44, 16, v57\n"
    "v_and_b32 v45, s69, v57\n"
    "v_mul_f32 v44, v44, v40\n"
    "v_mul_f32 v45, v45, v40\n"
    "v_cvt_pk_bf16_f32 v57, v44, v45\n"
    "v_lshlrev_b32 v42, 16, v58\n"
    "v_and_b32 v43, s69, v58\n"
    "v_mul_f32 v42, v42, v40\n"
    "v_mul_f32 v43, v43, v40\n"
    "v_cvt_pk_bf16_f32 v58, v42, v43\n"
    "v_lshlrev_b32 v44, 16, v59\n"
    "v_and_b32 v45, s69, v59\n"
    "v_mul_f32 v44, v44, v40\n"
    "v_mul_f32 v45, v45, v40\n"
    "v_cvt_pk_bf16_f32 v59, v44, v45\n"
    "v_lshlrev_b32 v42, 16, v60\n"
    "v_and_b32 v43, s69, v60\n"
    "v_mul_f32 v42, v42, v40\n"
    "v_mul_f32 v43, v43, v40\n"
    "v_cvt_pk_bf16_f32 v60, v42, v43\n"
    "v_lshlrev_b32 v44, 16, v61\n"
    "v_and_b32 v45, s69, v61\n"
    "v_mul_f32 v44, v44, v40\n"
    "v_mul_f32 v45, v45, v40\n"
    "v_cvt_pk_bf16_f32 v61, v44, v45\n"
    "v_lshlrev_b32 v42, 16, v62\n"
    "v_and_b32 v43, s69, v62\n"
    "v_mul_f32 v42, v42, v40\n"
    "v_mul_f32 v43, v43, v40\n"
    "v_cvt_pk_bf16_f32 v62, v42, v43\n"
    "v_lshlrev_b32 v44, 16, v63\n"
    "v_and_b32 v45, s69, v63\n"
    "v_mul_f32 v44, v44, v40\n"
    "v_mul_f32 v45, v45, v40\n"
    "v_cvt_pk_bf16_f32 v63, v44, v45\n"
    "v_lshlrev_b32 v42, 16, v64\n"
    "v_and_b32 v43, s69, v64\n"
    "v_mul_f32 v42, v42, v41\n"
    "v_mul_f32 v43, v43, v41\n"
    "v_cvt_pk_bf16_f32 v64, v42, v43\n"
    "v_lshlrev_b32 v44, 16, v65\n"
    "v_and_b32 v45, s69, v65\n"
    "v_mul_f32 v44, v44, v41\n"
    "v_mul_f32 v45, v45, v41\n"
    "v_cvt_pk_bf16_f32 v65, v44, v45\n"
    "v_lshlrev_b32 v42, 16, v66\n"
    "v_and_b32 v43, s69, v66\n"
    "v_mul_f32 v42, v42, v41\n"
    "v_mul_f32 v43, v43, v41\n"
    "v_cvt_pk_bf16_f32 v66, v42, v43\n"
    "v_lshlrev_b32 v44, 16, v67\n"
    "v_and_b32 v45, s69, v67\n"
    "v_mul_f32 v44, v44, v41\n"
    "v_mul_f32 v45, v45, v41\n"
    "v_cvt_pk_bf16_f32 v67, v44, v45\n"
    "v_lshlrev_b32 v42, 16, v68\n"
    "v_and_b32 v43, s69, v68\n"
    "v_mul_f32 v42, v42, v41\n"
    "v_mul_f32 v43, v43, v41\n"
    "v_cvt_pk_bf16_f32 v68, v42, v43\n"
    "v_lshlrev_b32 v44, 16, v69\n"
    "v_and_b32 v45, s69, v69\n"
    "v_mul_f32 v44, v44, v41\n"
    "v_mul_f32 v45, v45, v41\n"
    "v_cvt_pk_bf16_f32 v69, v44, v45\n"
    "v_lshlrev_b32 v42, 16, v70\n"
    "v_and_b32 v43, s69, v70\n"
    "v_mul_f32 v42, v42, v41\n"
    "v_mul_f32 v43, v43, v41\n"
    "v_cvt_pk_bf16_f32 v70, v42, v43\n"
    "v_lshlrev_b32 v44, 16, v71\n"
    "v_and_b32 v45, s69, v71\n"
    "v_mul_f32 v44, v44, v41\n"
    "v_mul_f32 v45, v45, v41\n"
    "v_cvt_pk_bf16_f32 v71, v44, v45\n"
    "v_lshlrev_b32 v42, 16, v72\n"
    "v_and_b32 v43, s69, v72\n"
    "v_mul_f32 v42, v42, v41\n"
    "v_mul_f32 v43, v43, v41\n"
    "v_cvt_pk_bf16_f32 v72, v42, v43\n"
    "v_lshlrev_b32 v44, 16, v73\n"
    "v_and_b32 v45, s69, v73\n"
    "v_mul_f32 v44, v44, v41\n"
    "v_mul_f32 v45, v45, v41\n"
    "v_cvt_pk_bf16_f32 v73, v44, v45\n"
    "v_lshlrev_b32 v42, 16, v74\n"
    "v_and_b32 v43, s69, v74\n"
    "v_mul_f32 v42, v42, v41\n"
    "v_mul_f32 v43, v43, v41\n"
    "v_cvt_pk_bf16_f32 v74, v42, v43\n"
    "v_lshlrev_b32 v44, 16, v75\n"
    "v_and_b32 v45, s69, v75\n"
    "v_mul_f32 v44, v44, v41\n"
    "v_mul_f32 v45, v45, v41\n"
    "v_cvt_pk_bf16_f32 v75, v44, v45\n"
    "v_lshlrev_b32 v42, 16, v76\n"
    "v_and_b32 v43, s69, v76\n"
    "v_mul_f32 v42, v42, v41\n"
    "v_mul_f32 v43, v43, v41\n"
    "v_cvt_pk_bf16_f32 v76, v42, v43\n"
    "v_lshlrev_b32 v44, 16, v77\n"
    "v_and_b32 v45, s69, v77\n"
    "v_mul_f32 v44, v44, v41\n"
    "v_mul_f32 v45, v45, v41\n"
    "v_cvt_pk_bf16_f32 v77, v44, v45\n"
    "v_lshlrev_b32 v42, 16, v78\n"
    "v_and_b32 v43, s69, v78\n"
    "v_mul_f32 v42, v42, v41\n"
    "v_mul_f32 v43, v43, v41\n"
    "v_cvt_pk_bf16_f32 v78, v42, v43\n"
    "v_lshlrev_b32 v44, 16, v79\n"
    "v_and_b32 v45, s69, v79\n"
    "v_mul_f32 v44, v44, v41\n"
    "v_mul_f32 v45, v45, v41\n"
    "v_cvt_pk_bf16_f32 v79, v44, v45\n"
    "v_lshlrev_b32 v42, 16, v80\n"
    "v_and_b32 v43, s69, v80\n"
    "v_mul_f32 v42, v42, v40\n"
    "v_mul_f32 v43, v43, v40\n"
    "v_cvt_pk_bf16_f32 v80, v42, v43\n"
    "v_lshlrev_b32 v44, 16, v81\n"
    "v_and_b32 v45, s69, v81\n"
    "v_mul_f32 v44, v44, v40\n"
    "v_mul_f32 v45, v45, v40\n"
    "v_cvt_pk_bf16_f32 v81, v44, v45\n"
    "v_lshlrev_b32 v42, 16, v82\n"
    "v_and_b32 v43, s69, v82\n"
    "v_mul_f32 v42, v42, v40\n"
    "v_mul_f32 v43, v43, v40\n"
    "v_cvt_pk_bf16_f32 v82, v42, v43\n"
    "v_lshlrev_b32 v44, 16, v83\n"
    "v_and_b32 v45, s69, v83\n"
    "v_mul_f32 v44, v44, v40\n"
    "v_mul_f32 v45, v45, v40\n"
    "v_cvt_pk_bf16_f32 v83, v44, v45\n"
    "v_lshlrev_b32 v42, 16, v84\n"
    "v_and_b32 v43, s69, v84\n"
    "v_mul_f32 v42, v42, v40\n"
    "v_mul_f32 v43, v43, v40\n"
    "v_cvt_pk_bf16_f32 v84, v42, v43\n"
    "v_lshlrev_b32 v44, 16, v85\n"
    "v_and_b32 v45, s69, v85\n"
    "v_mul_f32 v44, v44, v40\n"
    "v_mul_f32 v45, v45, v40\n"
    "v_cvt_pk_bf16_f32 v85, v44, v45\n"
    "v_lshlrev_b32 v42, 16, v86\n"
    "v_and_b32 v43, s69, v86\n"
    "v_mul_f32 v42, v42, v40\n"
    "v_mul_f32 v43, v43, v40\n"
    "v_cvt_pk_bf16_f32 v86, v42, v43\n"
    "v_lshlrev_b32 v44, 16, v87\n"
    "v_and_b32 v45, s69, v87\n"
    "v_mul_f32 v44, v44, v40\n"
    "v_mul_f32 v45, v45, v40\n"
    "v_cvt_pk_bf16_f32 v87, v44, v45\n"
    "v_lshlrev_b32 v42, 16, v88\n"
    "v_and_b32 v43, s69, v88\n"
    "v_mul_f32 v42, v42, v40\n"
    "v_mul_f32 v43, v43, v40\n"
    "v_cvt_pk_bf16_f32 v88, v42, v43\n"
    "v_lshlrev_b32 v44, 16, v89\n"
    "v_and_b32 v45, s69, v89\n"
    "v_mul_f32 v44, v44, v40\n"
    "v_mul_f32 v45, v45, v40\n"
    "v_cvt_pk_bf16_f32 v89, v44, v45\n"
    "v_lshlrev_b32 v42, 16, v90\n"
    "v_and_b32 v43, s69, v90\n"
    "v_mul_f32 v42, v42, v40\n"
    "v_mul_f32 v43, v43, v40\n"
    "v_cvt_pk_bf16_f32 v90, v42, v43\n"
    "v_lshlrev_b32 v44, 16, v91\n"
    "v_and_b32 v45, s69, v91\n"
    "v_mul_f32 v44, v44, v40\n"
    "v_mul_f32 v45, v45, v40\n"
    "v_cvt_pk_bf16_f32 v91, v44, v45\n"
    "v_lshlrev_b32 v42, 16, v92\n"
    "v_and_b32 v43, s69, v92\n"
    "v_mul_f32 v42, v42, v40\n"
    "v_mul_f32 v43, v43, v40\n"
    "v_cvt_pk_bf16_f32 v92, v42, v43\n"
    "v_lshlrev_b32 v44, 16, v93\n"
    "v_and_b32 v45, s69, v93\n"
    "v_mul_f32 v44, v44, v40\n"
    "v_mul_f32 v45, v45, v40\n"
    "v_cvt_pk_bf16_f32 v93, v44, v45\n"
    "v_lshlrev_b32 v42, 16, v94\n"
    "v_and_b32 v43, s69, v94\n"
    "v_mul_f32 v42, v42, v40\n"
    "v_mul_f32 v43, v43, v40\n"
    "v_cvt_pk_bf16_f32 v94, v42, v43\n"
    "v_lshlrev_b32 v44, 16, v95\n"
    "v_and_b32 v45, s69, v95\n"
    "v_mul_f32 v44, v44, v40\n"
    "v_mul_f32 v45, v45, v40\n"
    "v_cvt_pk_bf16_f32 v95, v44, v45\n"
    "v_lshlrev_b32 v42, 16, v96\n"
    "v_and_b32 v43, s69, v96\n"
    "v_mul_f32 v42, v42, v41\n"
    "v_mul_f32 v43, v43, v41\n"
    "v_cvt_pk_bf16_f32 v96, v42, v43\n"
    "v_lshlrev_b32 v44, 16, v97\n"
    "v_and_b32 v45, s69, v97\n"
    "v_mul_f32 v44, v44, v41\n"
    "v_mul_f32 v45, v45, v41\n"
    "v_cvt_pk_bf16_f32 v97, v44, v45\n"
    "v_lshlrev_b32 v42, 16, v98\n"
    "v_and_b32 v43, s69, v98\n"
    "v_mul_f32 v42, v42, v41\n"
    "v_mul_f32 v43, v43, v41\n"
    "v_cvt_pk_bf16_f32 v98, v42, v43\n"
    "v_lshlrev_b32 v44, 16, v99\n"
    "v_and_b32 v45, s69, v99\n"
    "v_mul_f32 v44, v44, v41\n"
    "v_mul_f32 v45, v45, v41\n"
    "v_cvt_pk_bf16_f32 v99, v44, v45\n"
    "v_lshlrev_b32 v42, 16, v100\n"
    "v_and_b32 v43, s69, v100\n"
    "v_mul_f32 v42, v42, v41\n"
    "v_mul_f32 v43, v43, v41\n"
    "v_cvt_pk_bf16_f32 v100, v42, v43\n"
    "v_lshlrev_b32 v44, 16, v101\n"
    "v_and_b32 v45, s69, v101\n"
    "v_mul_f32 v44, v44, v41\n"
    "v_mul_f32 v45, v45, v41\n"
    "v_cvt_pk_bf16_f32 v101, v44, v45\n"
    "v_lshlrev_b32 v42, 16, v102\n"
    "v_and_b32 v43, s69, v102\n"
    "v_mul_f32 v42, v42, v41\n"
    "v_mul_f32 v43, v43, v41\n"
    "v_cvt_pk_bf16_f32 v102, v42, v43\n"
    "v_lshlrev_b32 v44, 16, v103\n"
    "v_and_b32 v45, s69, v103\n"
    "v_mul_f32 v44, v44, v41\n"
    "v_mul_f32 v45, v45, v41\n"
    "v_cvt_pk_bf16_f32 v103, v44, v45\n"
    "v_lshlrev_b32 v42, 16, v104\n"
    "v_and_b32 v43, s69, v104\n"
    "v_mul_f32 v42, v42, v41\n"
    "v_mul_f32 v43, v43, v41\n"
    "v_cvt_pk_bf16_f32 v104, v42, v43\n"
    "v_lshlrev_b32 v44, 16, v105\n"
    "v_and_b32 v45, s69, v105\n"
    "v_mul_f32 v44, v44, v41\n"
    "v_mul_f32 v45, v45, v41\n"
    "v_cvt_pk_bf16_f32 v105, v44, v45\n"
    "v_lshlrev_b32 v42, 16, v106\n"
    "v_and_b32 v43, s69, v106\n"
    "v_mul_f32 v42, v42, v41\n"
    "v_mul_f32 v43, v43, v41\n"
    "v_cvt_pk_bf16_f32 v106, v42, v43\n"
    "v_lshlrev_b32 v44, 16, v107\n"
    "v_and_b32 v45, s69, v107\n"
    "v_mul_f32 v44, v44, v41\n"
    "v_mul_f32 v45, v45, v41\n"
    "v_cvt_pk_bf16_f32 v107, v44, v45\n"
    "v_lshlrev_b32 v42, 16, v108\n"
    "v_and_b32 v43, s69, v108\n"
    "v_mul_f32 v42, v42, v41\n"
    "v_mul_f32 v43, v43, v41\n"
    "v_cvt_pk_bf16_f32 v108, v42, v43\n"
    "v_lshlrev_b32 v44, 16, v109\n"
    "v_and_b32 v45, s69, v109\n"
    "v_mul_f32 v44, v44, v41\n"
    "v_mul_f32 v45, v45, v41\n"
    "v_cvt_pk_bf16_f32 v109, v44, v45\n"
    "v_lshlrev_b32 v42, 16, v110\n"
    "v_and_b32 v43, s69, v110\n"
    "v_mul_f32 v42, v42, v41\n"
    "v_mul_f32 v43, v43, v41\n"
    "v_cvt_pk_bf16_f32 v110, v42, v43\n"
    "v_lshlrev_b32 v44, 16, v111\n"
    "v_and_b32 v45, s69, v111\n"
    "v_mul_f32 v44, v44, v41\n"
    "v_mul_f32 v45, v45, v41\n"
    "v_cvt_pk_bf16_f32 v111, v44, v45\n"
    "s_setpc_b64 s[80:81]\n"
    "fb_Lend:\n"
    : [tag] "=&s"(tag), "=&{v76}"(f), "=&{v[48:51]}"(wv[0]), "=&{v[52:55]}"(wv[1]), "=&{v[56:59]}"(wv[2]), "=&{v[60:63]}"(wv[3]), "=&{v[64:67]}"(wv[4]), "=&{v[68:71]}"(wv[5]), "=&{v[72:75]}"(wv[6])
    : [x] "s"(x), [k] "s"(kc), [v] "s"(vc), [wq] "s"(wq), [wo] "s"(wo), [ws] "s"(ws), [c] "s"(c), [wmod] "s"(wmod), [wg] "s"(wg), [wu] "s"(wu), [wd] "s"(wd),
      [pf] "s"(pf), [blk] "s"(blockIdx.x), [wv] "s"(w), "s"((unsigned)(size_t)lds)
    : "v0", "v1", "v2", "v3", "v4", "v5", "v6", "v7", "v8", "v9", "v10", "v11", "v12", "v13", "v14", "v15", "v16", "v17", "v18", "v19", "v20", "v21", "v22", "v23", "v24", "v25", "v26", "v27", "v28", "v29", "v30", "v31", "v32", "v33", "v34", "v35", "v36", "v37", "v38", "v39", "v40", "v41", "v42", "v43", "v44", "v45", "v46", "v47", "v77", "v78", "v79", "v80", "v81", "v82", "v83", "v84", "v85", "v86", "v87", "v88", "v89", "v90", "v91", "v92", "v93", "v94", "v95", "v96", "v97", "v98", "v99", "v100", "v101", "v102", "v103", "v104", "v105", "v106", "v107", "v108", "v109", "v110", "v111", "v112", "v113", "v114", "v115", "v116", "v117", "v118", "v119", "v120", "v121", "v122", "v123", "v124", "v125", "v126", "v127", "v128", "v129", "v130", "v131", "v132", "v133", "v134", "v135", "v136", "v137", "v138", "v139", "v140", "v141", "v142", "v143", "v144", "v145", "v146", "v147", "v148", "v149", "v150", "v151", "v152", "v153", "v154", "v155", "v156", "v157", "v158", "v159", "v160", "v161", "v162", "v163", "v164", "v165", "v166", "v167", "v168", "v169", "v170", "v171", "v172", "v173", "v174", "v175", "v176", "v177", "v178", "v179", "v180", "v181", "v182", "v183", "v184", "v185", "v186", "v187", "v188", "v189", "v190", "v191", "v192", "v193", "v194", "v195", "v196", "v197", "v198", "v199", "v200", "v201", "v202", "v203", "v204", "v205", "v206", "v207", "v208", "v209", "v210", "v211", "v212", "v213", "v214", "v215", "v216", "v217", "v218", "v219", "v220", "v221", "v222", "v223", "v224", "v225", "v226", "v227", "v228", "v229", "v230", "v231", "v232", "v233", "v234", "v235", "v236", "v237", "v238", "v239", "v240", "v241", "v242", "v243", "v244", "v245", "v246", "v247", "v248", "v249", "v250", "v251", "v252", "v253", "v254", "v255", "a0", "a1", "a2", "a3", "a4", "a5", "a6", "a7", "a8", "a9", "a10", "a11", "a12", "a13", "a14", "a15", "a16", "a17", "a18", "a19", "a20", "a21", "a22", "a23", "a24", "a25", "a26", "a27", "a28", "a29", "a30", "a31", "a32", "a33", "a34", "a35", "a36", "a37", "a38", "a39", "a40", "a41", "a42", "a43", "a44", "a45", "a46", "a47", "a48", "a49", "a50", "a51", "a52", "a53", "a54", "a55", "a56", "a57", "a58", "a59", "a60", "a61", "a62", "a63", "a64", "a65", "a66", "a67", "a68", "a69", "a70", "a71", "a72", "a73", "a74", "a75", "a76", "a77", "a78", "a79", "a80", "a81", "a82", "a83", "a84", "a85", "a86", "a87", "a88", "a89", "a90", "a91", "a92", "a93", "a94", "a95", "a96", "a97", "a98", "a99", "a100", "a101", "a102", "a103", "a104", "a105", "a106", "a107", "a108", "a109", "a110", "a111", "a112", "a113", "a114", "a115", "a116", "a117", "a118", "a119", "a120", "a121", "a122", "a123", "a124", "a125", "a126", "a127", "a128", "a129", "a130", "a131", "a132", "a133", "a134", "a135", "a136", "a137", "a138", "a139", "a140", "a141", "a142", "a143", "a144", "a145", "a146", "a147", "a148", "a149", "a150", "a151", "a152", "a153", "a154", "a155", "a156", "a157", "a158", "a159", "a160", "a161", "a162", "a163", "a164", "a165", "a166", "a167", "a168", "a169", "a170", "a171", "a172", "a173", "a174", "a175", "a176", "a177", "a178", "a179", "a180", "a181", "a182", "a183", "a184", "a185", "a186", "a187", "a188", "a189", "a190", "a191", "a192", "a193", "a194", "a195", "a196", "a197", "a198", "a199", "a200", "a201", "a202", "a203", "a204", "a205", "a206", "a207", "a208", "a209", "a210", "a211", "a212", "a213", "a214", "a215", "a216", "a217", "a218", "a219", "a220", "a221", "a222", "a223", "a224", "a225", "a226", "a227", "a228", "a229", "a230", "a231", "a232", "a233", "a234", "a235", "a236", "a237", "a238", "a239", "a240", "a241", "a242", "a243", "a244", "a245", "a246", "a247", "a248", "a249", "a250", "a251", "a252", "a253", "a254", "a255", "s44", "s45", "s46", "s47", "s48", "s49", "s50", "s51", "s52", "s53", "s54", "s55", "s56", "s57", "s58", "s59", "s60", "s61", "s62", "s63", "s64", "s65", "s66", "s67", "s68", "s69", "s70", "s71", "s72", "s73", "s74", "s75", "s76", "s77", "s78", "s79", "s80", "s81", "s82", "s83", "s84", "s85", "s86", "s87", "s88", "s89", "s90", "s91", "s92", "s93", "s94", "s95", "s96", "s97", "s98", "s99", "vcc", "scc", "memory");

  const u32 lane = __builtin_amdgcn_mbcnt_hi(~0u, __builtin_amdgcn_mbcnt_lo(~0u, 0u));
  u32 b = blockIdx.x;
  asm volatile("" : "+s"(b), "+s"(w), "+s"(x), "+s"(wr), "+s"(wd), "+s"(out), "+s"(ws));
  const u32 g = b & 7, rb = b >> 3, t = w * 64 + lane;
  const u32 ln = (lane >> 3) + ((lane & 4) << 1);
  const bool act = ln < 12 || !(lane & 1);
  const u32 l8 = (ln >> 2) * 128 + (ln < 12 ? (lane & 1) * 64 : 0) + (ln & 3) * 16 + (lane & 2) * 4;
  const u32 row0 = rb * 64 + g * 8 + w * 2;
  auto rs = RSRC(ws, WS_SMALL);
  auto rp = RSRC(ws + WS_PART, 8 * PGS);
  float* lf = (float*)lds; u32* lu = (u32*)lds;
  u32x4 xv[2], wrv, mg[8];
  auto rx = RSRC(x, 2048 * 896);
  auto ro = RSRC(out, 2048 * 896);
  const u32 fo = WS_FLAG + (rb * 32 + (lane & 31)) * 4;
#pragma unroll
  for (int i = 0; i < 8; ++i) mg[i] = LD16(rs, WS_MODG + (2 + (i >> 1)) * 1792 + l8 * 4 + (i & 1) * 16);
#pragma unroll
  for (int k = 0; k < 2; ++k) xv[k] = LDP(rx, (row0 + k) * 896 + l8 * 2);
  wrv = LDP(RSRC(wr, 896), l8 * 2);
  MEMBAR();
  const u32 gl = g + 8 * (rb >> 2);
  const bool isG = gl < 28, isD = b >= 128 && b < 184 && w == 0;
  const u32 pb = (row0 >> 1) * PBS + lane * 16;
  u32x4 p[2][8];
  const u32 others = 0x11111111u & ~(1u << (4 * g));
  u32 pend = others, fly;
#define READY(m) do { m = (u32)__ballot(f == tag); m &= m >> 2; m &= m >> 1; m &= others; } while (0)
#define ISSUE() _Pragma("unroll") for (int q = 0; q < 8; ++q) if (fly & (1u << (4 * q))) { p[0][q] = LD16(rp, q * PGS + pb); p[1][q] = LD16(rp, q * PGS + pb + 1024); }
  READY(fly);
  f = LD4(rs, fo);
#pragma unroll
  for (int q = 0; q < 8; ++q) { const u32 so = (fly & (1u << (4 * q))) ? q * PGS : 0x7f000000u;
    p[0][q] = __builtin_amdgcn_raw_buffer_load_b128(rp, pb, so, 16); p[1][q] = __builtin_amdgcn_raw_buffer_load_b128(rp, pb + 1024, so, 16); }
  u32 wdv[16];
  {
    auto rd = RSRC(wd, isG ? 1792 * 896 : 0);
    const u32 o = (gl * 64 + (rb & 3) * 16) * 896 + (t < 224 ? t : 0) * 4;
#pragma unroll
    for (int i = 0; i < 16; ++i) wdv[i] = __builtin_amdgcn_raw_buffer_load_b32(rd, o, i * 896, 0);
  }
  MEMBAR();
  f32x2 ga[4], sb[4], cb[4], gb[4], uu[4], xf[2][4], wf[4];
  float su = 0.f, c0 = 0.f;
  bool prep = false;
#pragma unroll
  for (int j = 0; j < 4; ++j) { wf[j] = up2(act ? wrv[j] : 0u); xf[0][j] = up2(xv[0][j]); xf[1][j] = up2(xv[1][j]); ga[j] = sb[j] = cb[j] = gb[j] = uu[j] = (f32x2){0.f, 0.f}; }
#define PREP() if (!prep) { u32 bad = 0; \
    _Pragma("unroll") for (int i = 0; i < 8; ++i) bad |= (mg[i][1] ^ tag) | (mg[i][3] ^ tag); \
    if (__all(bad == 0)) { prep = true; f32x2 s1 = {0.f, 0.f}, s2 = {0.f, 0.f}; \
      _Pragma("unroll") for (int j = 0; j < 4; ++j) { \
        ga[j] = up2(mg[j >> 1][(j & 1) * 2]); sb[j] = up2(mg[2 + (j >> 1)][(j & 1) * 2]); \
        cb[j] = up2(mg[4 + (j >> 1)][(j & 1) * 2]); gb[j] = up2(mg[6 + (j >> 1)][(j & 1) * 2]); \
        uu[j] = cb[j] * wf[j]; s1 += uu[j]; s2 += sb[j] * wf[j]; } \
      su = wsum(s1[0] + s1[1]); c0 = wsum(s2[0] + s2[1]); } }
  PREP();
  f32x2 ac[2][4];
#pragma unroll
  for (int k = 0; k < 2; ++k) {
    const u32x4 o = *(const u32x4*)(lds + 65536 + ((g & 1) * 4 + w) * 2048 + k * 1024 + lane * 16);
#pragma unroll
    for (int j = 0; j < 4; ++j) ac[k][j] = up2(o[j]);
  }
#define ACCUM() _Pragma("unroll") for (int q = 0; q < 8; ++q) if (fly & (1u << (4 * q))) { \
        const u32 bad = (p[1][q][1] ^ tag) | (p[1][q][3] ^ tag); \
        if ((__ballot(bad != 0) >> 32) == 0) { pend &= ~(1u << (4 * q)); \
          _Pragma("unroll") for (int k = 0; k < 2; ++k) _Pragma("unroll") for (int j = 0; j < 4; ++j) ac[k][j] += up2(p[k][q][j]); } }
  ACCUM();
  for (u32 sp = 0; (pend || !prep) && sp < SPIN; ++sp) {
    u32 m; READY(m);
    fly = pend & m;
    if (m != others) f = LD4(rs, fo);
    const bool rl = !prep && ((sp & 3) == 3 || !pend);
    if (rl) {
#pragma unroll
      for (int i = 0; i < 8; ++i) mg[i] = LD16(rs, WS_MODG + (2 + (i >> 1)) * 1792 + l8 * 4 + (i & 1) * 16);
    }
    ISSUE();
    MEMBAR();
    if (rl) { PREP(); }
    ACCUM();
  }
  u32x4 x1p[2], hp[2]; u32 key[2];
  f32x2 x1[2][4];
  float st[2][3];
  u32 a2[2][4];
  {
    const bool hi = lane & 4;
#pragma unroll
    for (int j = 0; j < 4; ++j) {
      const u32 a = pk2(ac[0][j][0], ac[0][j][1]), c2 = pk2(ac[1][j][0], ac[1][j][1]);
      const u32 give = hi ? a : c2;
      u32 got = (u32)__builtin_amdgcn_update_dpp(0, (int)give, 0x104, 0xf, 0x5, false);
      got = (u32)__builtin_amdgcn_update_dpp((int)got, (int)give, 0x114, 0xf, 0xa, false);
      a2[0][j] = hi ? got : a; a2[1][j] = hi ? c2 : got;
    }
  }
#pragma unroll
  for (int k = 0; k < 2; ++k) {
    u32 at[4] = {a2[k][0], a2[k][1], a2[k][2], a2[k][3]};
    {
      const bool od = lane & 1;
      if (ln >= 12) at[1] = at[2];
#pragma unroll
      for (int j = 0; j < 2; ++j) {
        const u32 give = od ? at[j] : at[2 + j];
        const u32 got = (u32)__builtin_amdgcn_update_dpp(0, (int)give, 0xb1, 0xf, 0xf, false);
        if (od) at[j] = got; else at[2 + j] = got;
      }
    }
    f32x2 s0 = {0.f, 0.f}, s1 = {0.f, 0.f}, s2 = {0.f, 0.f};
#pragma unroll
    for (int j = 0; j < 4; ++j) {
      f32x2 a = up2(at[j]) * ga[j];
      a = up2(pk2(a[0], a[1])) + xf[k][j];
      x1p[k][j] = act ? pk2(a[0], a[1]) : 0u;
      x1[k][j] = up2(x1p[k][j]);
      s0 += x1[k][j]; s1 += x1[k][j] * x1[k][j]; s2 += x1[k][j] * uu[j];
    }
    st[k][0] = s0[0] + s0[1]; st[k][1] = s1[0] + s1[1]; st[k][2] = s2[0] + s2[1];
  }
  float mrow[2], rden[2];
  {
    float s[3];
#pragma unroll
    for (int i = 0; i < 3; ++i) {
      auto a = __builtin_amdgcn_permlane32_swap(__float_as_uint(st[0][i]), __float_as_uint(st[1][i]), false, false);
      float v = row_sum(__uint_as_float(a[0]) + __uint_as_float(a[1]));
      auto e = __builtin_amdgcn_permlane16_swap(__float_as_uint(v), __float_as_uint(v), false, false);
      s[i] = __uint_as_float(e[0]) + __uint_as_float(e[1]);
    }
    const float mean = s[0] * (1.f / D_);
    const float var = rbf(fmaxf(s[1] * (1.f / D_) - mean * mean, 0.f));
    const float m = rbf(mean), rd = __builtin_amdgcn_rcpf(rbf(__builtin_amdgcn_sqrtf(rbf(var + 1e-5f))));
    const float r = (s[2] - m * su) * rd + c0;
    u32 u = __float_as_uint(r);
    u = (u & 0x80000000u) ? ~u : (u | 0x80000000u);
    if (r != r) u = 0;
    u &= 0xfffff800u;
#pragma unroll
    for (int k = 0; k < 2; ++k) {
      key[k] = (u32)__builtin_amdgcn_readlane((int)u, 32 * k) | (2047u - (row0 + k));
      mrow[k] = __int_as_float(__builtin_amdgcn_readlane(__float_as_int(m), 32 * k));
      rden[k] = __int_as_float(__builtin_amdgcn_readlane(__float_as_int(rd), 32 * k));
    }
  }
  const u32 kw = key[0] > key[1] ? key[0] : key[1];
  if (lane == 0) lu[w] = kw;
  LDSBAR();
  u32x4 kk = *(const u32x4*)lu;
  const u32 kb = __builtin_amdgcn_readfirstlane(max(max(kk[0], kk[1]), max(kk[2], kk[3])));
  const bool own = (kw == kb);
  const bool k1 = key[1] == kb;
  if (own && lane == 0) { u32x2 kg = {kb, tag}; ST8(kg, rs, WS_KEY + b * 8); }
  MEMBAR();
#pragma unroll
  for (int k = 0; k < 2; ++k)
#pragma unroll
    for (int j = 0; j < 4; ++j) {
      f32x2 a = x1[k][j] - mrow[k];
      a = up2(pk2(a[0], a[1])) * rden[k];
      a = up2(pk2(a[0], a[1])) * cb[j];
      a = up2(pk2(a[0], a[1])) + sb[j];
      hp[k][j] = pk2(a[0], a[1]);
    }
  if (own && act) {
#pragma unroll
    for (int j = 0; j < 4; ++j) {
      u32x4 v = {k1 ? hp[1][j] : hp[0][j], tag, k1 ? x1p[1][j] : x1p[0][j], tag};
      ST16(v, rs, WS_ROWG + b * 3584 + j * 896 + l8 * 2);
    }
  }
  u32x4 og[2];
#pragma unroll
  for (int k = 0; k < 2; ++k)
#pragma unroll
    for (int j = 0; j < 4; ++j) { f32x2 a = up2(hp[k][j]) * gb[j]; a = up2(pk2(a[0], a[1])) + x1[k][j]; og[k][j] = pk2(a[0], a[1]); }
  if (!(own || isG || isD)) {
    if (act) {
#pragma unroll
      for (int k = 0; k < 2; ++k) STP(og[k], ro, (row0 + k) * 896 + l8 * 2);
    }
    return;
  }
  f32x2 wf2[7][4], wd2[16];
  if (isG) {
#pragma unroll
    for (int i = 0; i < 7; ++i)
#pragma unroll
      for (int j = 0; j < 4; ++j) { wf2[i][j] = up2(wv[i][j]); asm volatile("" : "+v"(wf2[i][j])); }
#pragma unroll
    for (int i = 0; i < 16; ++i) { wd2[i] = up2(wdv[i]); asm volatile("" : "+v"(wd2[i])); }
  }
  u32 win = 0;
  if (!isG || w == 0) {
    const bool lazy = !isG;
    for (u32 sp = 0; sp < SPIN; ++sp) {
      if (lazy) __builtin_amdgcn_s_sleep(20);
      MEMBAR();
      u32x4 k0 = LD16(rs, WS_KEY + lane * 16), k2 = LD16(rs, WS_KEY + lane * 16 + 1024);
      u32 bad = (k0[1] ^ tag) | (k0[3] ^ tag) | (k2[1] ^ tag) | (k2[3] ^ tag);
      win = max(max(k0[0], k0[2]), max(k2[0], k2[2]));
      if (__all(bad == 0)) break;
    }
    win = wmax(win);
    if (isG && lane == 0) lu[8] = win;
  }
  if (isG) { LDSBAR(); win = lu[8]; }
  win = __builtin_amdgcn_readfirstlane(win);
  const u32 wrow = 2047u - (win & 2047u);
  const u32 wb = (wrow >> 6) * 8 + ((wrow >> 3) & 7);
  if (isG) {
    u32x2 hv[7];
    const u32 rg = (w & 1) * 32 + (lane >> 1);
    for (u32 sp = 0; sp < SPIN; ++sp) {
      MEMBAR();
      u32 bad = 0;
#pragma unroll
      for (int i = 0; i < 7; ++i) hv[i] = LD8(rs, WS_ROWG + wb * 3584 + ((rg >> 1) & 3) * 896 + (rg >> 3) * 16 + i * 128);
#pragma unroll
      for (int i = 0; i < 7; ++i) bad |= hv[i][1] ^ tag;
      if (__all(bad == 0)) break;
    }
    f32x2 acc[4] = {{0.f, 0.f}, {0.f, 0.f}, {0.f, 0.f}, {0.f, 0.f}};
#pragma unroll
    for (int i = 0; i < 7; ++i) { const float xs = (lane & 2) ? bfhi(hv[i][0]) : bflo(hv[i][0]);
#pragma unroll
      for (int j = 0; j < 4; ++j) acc[j] += wf2[i][j] * xs; }
    float a8[8];
#pragma unroll
    for (int j = 0; j < 8; ++j) { float a = acc[j >> 1][j & 1]; a = dpp_add<0x128>(a); a = dpp_add<0x124>(a); a8[j] = dpp_add<0x122>(a); }
    if ((lane & 15) < 2) {
      float* d = lf + 64 + (t >> 4) * 16 + (lane & 1) * 8;
      *(float4*)d = make_float4(a8[0], a8[1], a8[2], a8[3]); *(float4*)(d + 4) = make_float4(a8[4], a8[5], a8[6], a8[7]);
    }
    LDSBAR();
    float s = 0;
#pragma unroll
    for (int i = 0; i < 4; ++i) s += lf[64 + ((lane >> 4) * 4 + i) * 16 + (lane & 15)];
    auto e = __builtin_amdgcn_permlane16_swap(__float_as_uint(s), __float_as_uint(s), false, false);
    s = rbf(__uint_as_float(e[0]) + __uint_as_float(e[1]));
    auto a = __builtin_amdgcn_permlane32_swap(__float_as_uint(s), __float_as_uint(s), false, false);
    const float gg = __uint_as_float(a[0]), uu = __uint_as_float(a[1]);
    const float hid = rbf(rbf(gg * __builtin_amdgcn_rcpf(1.f + __expf(-gg))) * uu);
    f32x2 y = {0.f, 0.f};
#pragma unroll
    for (int i = 0; i < 16; ++i) y += wd2[i] * __int_as_float(__builtin_amdgcn_readlane(__float_as_int(hid), i));
    if (t < 224) { u32x4 v = {__float_as_uint(y[0]), tag, __float_as_uint(y[1]), tag}; ST16(v, rs, WS_YP + (gl * 4 + (rb & 3)) * 3584 + t * 16); }
  }
  if (act && !isD) {
#pragma unroll
    for (int k = 0; k < 2; ++k) if (key[k] != win) STP(og[k], ro, (row0 + k) * 896 + l8 * 2);
  }
  if (isD) {
    const u32 col0 = (b - 128) * 8;
    u32x4 rw, yv[7]; u32x2 gv;
    for (u32 sp = 0; sp < SPIN; ++sp) {
      MEMBAR();
      rw = LD16(rs, WS_ROWG + wb * 3584 + (lane & 3) * 896 + col0 * 2);
      gv = LD8(rs, WS_MODG + 5 * 1792 + (col0 / 2 + (lane & 3)) * 8);
#pragma unroll
      for (int i = 0; i < 7; ++i) yv[i] = LD16(rs, WS_YP + ((lane >> 2) + 16 * i) * 3584 + col0 * 8 + (lane & 3) * 16);
      u32 bad = (rw[1] ^ tag) | (rw[3] ^ tag) | (gv[1] ^ tag);
#pragma unroll
      for (int i = 0; i < 7; ++i) bad |= (yv[i][1] ^ tag) | (yv[i][3] ^ tag);
      if (__all(bad == 0)) break;
    }
    float y2[2] = {0.f, 0.f};
#pragma unroll
    for (int i = 0; i < 7; ++i) { y2[0] += __uint_as_float(yv[i][0]); y2[1] += __uint_as_float(yv[i][2]); }
#pragma unroll
    for (int j = 0; j < 2; ++j) {
      float s = dpp_add<0x128>(y2[j]); s = dpp_add<0x124>(s);
      auto a = __builtin_amdgcn_permlane16_swap(__float_as_uint(s), __float_as_uint(s), false, false);
      s = __uint_as_float(a[0]) + __uint_as_float(a[1]);
      auto e = __builtin_amdgcn_permlane32_swap(__float_as_uint(s), __float_as_uint(s), false, false);
      y2[j] = __uint_as_float(e[0]) + __uint_as_float(e[1]);
    }
    u32 u = (win & 0x80000000u) ? (win & 0x7fffffffu) : ~win;
    const float r = rbf(__uint_as_float((u & 0xfffff800u) | 0x400u));
    const float sg = rbf(__builtin_amdgcn_rcpf(1.f + __expf(-r)));
    f32x2 a = up2(pk2(y2[0], y2[1])) * sg;
    a = up2(pk2(a[0], a[1])) + up2(rw[0]);
    a = up2(pk2(a[0], a[1])) * up2(gv[0]);
    a = up2(pk2(a[0], a[1])) + up2(rw[2]);
    if (lane < 4) __builtin_amdgcn_raw_buffer_store_b32(pk2(a[0], a[1]), ro, wrow * 896 + col0 * 2 + lane * 4, 0, 16);
    if (b == 128 && lane == 0) ST4(tag, rs, 0);
    if (act) {
#pragma unroll
      for (int k = 0; k < 2; ++k) if (key[k] != win) STP(og[k], ro, (row0 + k) * 896 + l8 * 2);
    }
  }
}

extern "C" void kernel_launch(void* const* d_in, const int* in_sizes, int n_in, void* d_out, int out_size, void* d_ws, size_t ws_size, hipStream_t stream) {
  fused_block<<<256, 256, 0, stream>>>((const u16*)d_in[0], (const u16*)d_in[1], (const u16*)d_in[2], (const u16*)d_in[3], (const u16*)d_in[4],
                                       (const u16*)d_in[6], (const u16*)d_in[7], (const u16*)d_in[8], (const u16*)d_in[9],
                                       (const u16*)d_in[10], (const u16*)d_in[11], (u16*)d_out, (char*)d_ws);
}
```
